```python
import torch

def _softmax(x, axis=-1):
    return torch.softmax(x, dim=axis)
def _gelu(x):
    return torch.nn.functional.gelu(x, approximate="tanh")
def _silu(x):
    return torch.nn.functional.silu(x)
def _sigmoid(x):
    return torch.sigmoid(x)
def _layernorm(x, axis=-1, eps=1e-5):
    m = x.mean(dim=axis, keepdim=True)
    v = x.var(dim=axis, keepdim=True, unbiased=False)
    return (x - m) / torch.sqrt(v + eps)
def _scatter_add(tgt, idx, src, axis=0):
    return tgt.clone().index_add_(axis, idx, src)
def _segment_sum(src, seg, n):
    out = torch.zeros((n,) + src.shape[1:], dtype=src.dtype)
    return out.index_add_(0, seg, src)
def _pad(x, pw):
    flat = tuple(v for pair in reversed(pw) for v in pair)
    return torch.nn.functional.pad(x, flat)

kernel_name = 'gated_mix__compressed_kv_cla__linear_attn_block__prompt'

def setup_inputs():

    _w = (lambda a: (lambda t: t.to(torch.bfloat16) if t.is_floating_point() else t)(torch.as_tensor(a)))
    def _A_setup():
        B, H, Sq, Sk, D, M = 1, 16, 2048, 2048, 256, 448
        Dc = 4 * D
        return {
            "x": _w((lambda *s: torch.randn(*s, dtype=torch.bfloat16) * 0.05)(B, Sq, M)),
            "c_kv": _w((lambda *s: torch.randn(*s, dtype=torch.bfloat16) * 1.6)(B, Sk, Dc)),
            "w_q": _w((lambda *s: torch.randn(*s, dtype=torch.bfloat16) * 3.2)(M, H, D) * (M ** -0.5)),
            "w_uk": _w((lambda *s: torch.randn(*s, dtype=torch.bfloat16) * 3.2)(Dc, H, D) * (Dc ** -0.5)),
            "w_uv": _w((lambda *s: torch.randn(*s, dtype=torch.bfloat16) * 1.6)(Dc, H, D) * (Dc ** -0.5)),
            "w_o": _w((lambda *s: torch.randn(*s, dtype=torch.bfloat16) * 1.6)(H, D, M) * ((H * D) ** -0.5)),
        }
    _A_in = _A_setup()

    def _B_setup():
        B, S, D = 1, 2048, 448
        return {
            "x": _w((lambda *s: torch.randn(*s, dtype=torch.bfloat16) * 0.05)(B, S, D)),
            "wqkv": _w((lambda *s: torch.randn(*s, dtype=torch.bfloat16) * 1.6)(D, 3 * D)),
            "wo": _w((lambda *s: torch.randn(*s, dtype=torch.bfloat16) * 1.6)(D, D)),
        }
    _B_in = _B_setup()

    return {
        "A_x": _A_in["x"],
        "A_c_kv": _A_in["c_kv"],
        "A_w_q": _A_in["w_q"],
        "A_w_uk": _A_in["w_uk"],
        "A_w_uv": _A_in["w_uv"],
        "A_w_o": _A_in["w_o"],
        "B_x": _B_in["x"],
        "B_wqkv": _B_in["wqkv"],
        "B_wo": _B_in["wo"],
        "gate_w": _w((lambda *s: torch.randn(*s, dtype=torch.bfloat16) * 0.05)(448) * 16.0),
    }

def reference(A_x, A_c_kv, A_w_q, A_w_uk, A_w_uv, A_w_o, B_x, B_wqkv, B_wo, gate_w):
    def _A_ref(x, c_kv, w_q, w_uk, w_uv, w_o):
        B, Sq, M = x.shape[0], x.shape[1], x.shape[2]
        Sk = c_kv.shape[1]
        H, D = w_q.shape[1], w_q.shape[2]
        q = torch.einsum("bqm,mhd->bhqd", x, w_q)
        k = torch.einsum("bkc,chd->bhkd", c_kv, w_uk)
        v = torch.einsum("bkc,chd->bhkd", c_kv, w_uv)
        scores = torch.einsum("bhqd,bhkd->bhqk", q, k) * (D ** -0.5)
        off = Sk - Sq
        qi = torch.arange(Sq)[:, None]
        kj = torch.arange(Sk)[None, :]
        cmask = kj <= (qi + off)
        scores = torch.where(cmask[None, None], scores, -1e9)
        p = _softmax(scores, axis=-1)
        ctx = torch.einsum("bhqk,bhkd->bhqd", p, v)
        return torch.einsum("bhqd,hdm->bqm", ctx, w_o)

    def _B_ref(x, wqkv, wo):
        B, S, D = x.shape
        H = 16; Dh = D // H
        qkv = (lambda x, *d: x.permute(*d))((x @ wqkv).reshape(B, S, 3, H, Dh), 2, 0, 3, 1, 4)
        q, k, v = qkv[0], qkv[1], qkv[2]
        phi = lambda t: torch.where(t > 0, t + 1, torch.exp(t)) + 1e-6
        pq, pk = phi(q), phi(k)
        kv = torch.einsum("bhsd,bhse->bhde", pk, v)
        z = torch.einsum("bhsd->bhd", pk)
        num = torch.einsum("bhqd,bhde->bhqe", pq, kv)
        den = torch.einsum("bhqd,bhd->bhq", pq, z)[..., None] + 1e-6
        o = num / den
        return (lambda x, *d: x.permute(*d))(o, 0, 2, 1, 3).reshape(B, S, D) @ wo

    x = A_x
    g = _sigmoid((x * gate_w).sum(-1, keepdims=True))
    a = _A_ref(x, A_c_kv, A_w_q, A_w_uk, A_w_uv, A_w_o)
    b = _B_ref(x, B_wqkv, B_wo)
    return g * a + (1 - g) * b
```

```cpp
#include <hip/hip_runtime.h>
#include <hip/hip_bf16.h>
typedef unsigned short u16;
__device__ inline float bf2f(u16 b){ return __uint_as_float(((unsigned)b)<<16); }
__device__ inline u16 f2bf(float f){ unsigned u=__float_as_uint(f); u += 0x7fffu + ((u>>16)&1u); return (u16)(u>>16); }
__device__ inline float rbf(float f){ return bf2f(f2bf(f)); }

__global__ void gemm_naive(const u16* A, const u16* B, u16* C, int M, int N, int K){
  __shared__ float As[16][17], Bs[16][17];
  int tx=threadIdx.x, ty=threadIdx.y;
  int n=blockIdx.x*16+tx, m=blockIdx.y*16+ty;
  float acc=0;
  for(int k0=0;k0<K;k0+=16){
    As[ty][tx]=bf2f(A[(size_t)m*K+k0+tx]);
    Bs[ty][tx]=bf2f(B[(size_t)(k0+ty)*N+n]);
    __syncthreads();
    for(int k=0;k<16;k++) acc+=As[ty][k]*Bs[k][tx];
    __syncthreads();
  }
  C[(size_t)m*N+n]=f2bf(acc);
}
__global__ void attn_naive(const u16* Q, const u16* K, const u16* V, u16* ctx){
  __shared__ float q[256]; __shared__ float p[2048]; __shared__ float red[256];
  int row=blockIdx.x, h=blockIdx.y, t=threadIdx.x;
  q[t]=bf2f(Q[(size_t)row*4096+h*256+t]);
  __syncthreads();
  float mx=-1e30f;
  for(int j=t;j<=row;j+=256){
    float s=0; const u16* kr=K+(size_t)j*4096+h*256;
    for(int d=0;d<256;d++) s+=q[d]*bf2f(kr[d]);
    s*=0.0625f; p[j]=s; mx=fmaxf(mx,s);
  }
  red[t]=mx; __syncthreads();
  for(int o=128;o>0;o>>=1){ if(t<o) red[t]=fmaxf(red[t],red[t+o]); __syncthreads(); }
  mx=red[0]; __syncthreads();
  float sum=0;
  for(int j=t;j<=row;j+=256){ float e=__expf(p[j]-mx); p[j]=e; sum+=e; }
  red[t]=sum; __syncthreads();
  for(int o=128;o>0;o>>=1){ if(t<o) red[t]+=red[t+o]; __syncthreads(); }
  sum=red[0];
  float acc=0;
  for(int j=0;j<=row;j++) acc+=p[j]*bf2f(V[(size_t)j*4096+h*256+t]);
  ctx[(size_t)row*4096+h*256+t]=f2bf(acc/sum);
}
__device__ inline float phi(float t){ float r = t>0 ? rbf(t+1.f) : rbf(__expf(t)); return rbf(r+1e-6f); }
__global__ void kv_naive(const u16* qkv, float* kv){
  int h=blockIdx.x; int t=threadIdx.x; int d=t/32, e=t%32; if(d>=28||e>28) return;
  float acc=0;
  for(int s=0;s<2048;s++){ float pk=phi(bf2f(qkv[(size_t)s*1344+448+h*28+d])); float v = e<28? bf2f(qkv[(size_t)s*1344+896+h*28+e]) : 1.f; acc+=pk*v; }
  kv[(h*32+d)*32+e]=rbf(acc);
}
__global__ void o_naive(const u16* qkv, const float* kv, u16* o){
  int s=blockIdx.x; int t=threadIdx.x; if(t>=448) return; int h=t/28, e=t%28;
  float num=0, den=0;
  for(int d=0;d<28;d++){ float pq=phi(bf2f(qkv[(size_t)s*1344+h*28+d])); num+=pq*kv[(h*32+d)*32+e]; den+=pq*kv[(h*32+d)*32+28]; }
  num=rbf(num); den=rbf(rbf(den)+1e-6f);
  o[(size_t)s*448+t]=f2bf(num/den);
}
__global__ void fin_naive(const u16* x, const u16* gw, const u16* a, const u16* b, u16* out){
  __shared__ float red[512];
  int s=blockIdx.x, t=threadIdx.x;
  red[t] = t<448 ? rbf(bf2f(x[(size_t)s*448+t])*bf2f(gw[t])) : 0.f; __syncthreads();
  for(int o=256;o>0;o>>=1){ if(t<o) red[t]+=red[t+o]; __syncthreads(); }
  float g=rbf(red[0]); g=rbf(1.f/(1.f+__expf(-g)));
  if(t<448){ float r=rbf(g*bf2f(a[(size_t)s*448+t]))+rbf(rbf(1.f-g)*bf2f(b[(size_t)s*448+t])); out[(size_t)s*448+t]=f2bf(r); }
}
extern "C" void kernel_launch(void* const* d_in, const int* in_sizes, int n_in, void* d_out, int out_size, void* d_ws, size_t ws_size, hipStream_t stream){
  const u16* x=(const u16*)d_in[0]; const u16* ckv=(const u16*)d_in[1]; const u16* wq=(const u16*)d_in[2]; const u16* wuk=(const u16*)d_in[3];
  const u16* wuv=(const u16*)d_in[4]; const u16* wo=(const u16*)d_in[5]; const u16* wqkv=(const u16*)d_in[7]; const u16* bwo=(const u16*)d_in[8]; const u16* gw=(const u16*)d_in[9];
  char* ws=(char*)d_ws; size_t MB=1<<20;
  u16* Q=(u16*)ws; u16* K=(u16*)(ws+16*MB); u16* V=(u16*)(ws+32*MB); u16* ctx=(u16*)(ws+48*MB); u16* qkv=(u16*)(ws+64*MB); float* kv=(float*)(ws+72*MB);
  u16* o=(u16*)(ws+73*MB); u16* a=(u16*)(ws+76*MB); u16* b=(u16*)(ws+80*MB);
  dim3 bl(16,16);
  gemm_naive<<<dim3(4096/16,2048/16),bl,0,stream>>>(x,wq,Q,2048,4096,448);
  gemm_naive<<<dim3(4096/16,2048/16),bl,0,stream>>>(ckv,wuk,K,2048,4096,1024);
  gemm_naive<<<dim3(4096/16,2048/16),bl,0,stream>>>(ckv,wuv,V,2048,4096,1024);
  gemm_naive<<<dim3(1344/16,2048/16),bl,0,stream>>>(x,wqkv,qkv,2048,1344,448);
  attn_naive<<<dim3(2048,16),256,0,stream>>>(Q,K,V,ctx);
  kv_naive<<<16,1024,0,stream>>>(qkv,kv);
  o_naive<<<2048,512,0,stream>>>(qkv,kv,o);
  gemm_naive<<<dim3(448/16,2048/16),bl,0,stream>>>(ctx,wo,a,2048,448,4096);
  gemm_naive<<<dim3(448/16,2048/16),bl,0,stream>>>(o,bwo,b,2048,448,448);
  fin_naive<<<2048,512,0,stream>>>(x,gw,a,b,(u16*)d_out);
}
```

```cpp
#include <hip/hip_runtime.h>
typedef unsigned u32x2 __attribute__((ext_vector_type(2)));
typedef unsigned u32x4 __attribute__((ext_vector_type(4)));
typedef unsigned u32x8 __attribute__((ext_vector_type(8)));
typedef unsigned u32x16 __attribute__((ext_vector_type(16)));
constexpr size_t WS_Q = 0, WS_K = 16u << 20, WS_V = 32u << 20, WS_WB = 68u << 20, WS_KVP = 70u << 20, WS_FLAG = (71u << 20) + 65536u,
                 WS_PART = 72u << 20, WS_ML = 82u << 20, WS_WF = 84u << 20, WS_OP = 96u << 20;
#define DI __device__ __forceinline__
DI u32x4 mk_srd(const void* p, unsigned n) { const size_t a = (size_t)p; u32x4 r; r[0] = (unsigned)a; r[1] = (unsigned)(a >> 32) & 0xffffu; r[2] = n; r[3] = 0x20000u; return r; }
DI const char* usel(bool c, const char* a, const char* b) { const size_t m = c ? ~(size_t)0 : 0; return (const char*)(((size_t)a & m) | ((size_t)b & ~m)); }

__global__ __launch_bounds__(256, 1) void kf_main(const char* x_, const char* ckv_, const char* wq_, const char* wuk_, const char* wuv_, const char* wqkv_, const char* gw_, const char* wo_, const char* bwo_, char* ws_, char* out_) {
  __shared__ __attribute__((aligned(16))) char lds[163840];
  u32x16 ka; u32x4 kb4; u32x2 kc; const char* code;
  unsigned bid = blockIdx.x, tid = threadIdx.x;
  asm volatile("s_load_dwordx16 s[52:67], %5, 0x0\n s_load_dwordx4 s[76:79], %5, 0x40\n s_load_dwordx2 s[40:41], %5, 0x50\n s_getpc_b64 %3\n v_lshlrev_b32 v1, 6, %6\n global_load_dword a222, v1, %3"
    : "={s[52:67]}"(ka), "={s[76:79]}"(kb4), "={s[40:41]}"(kc), "=&s"(code), "+s"(bid) : "s"(__builtin_amdgcn_kernarg_segment_ptr()), "v"(tid) : "v1", "a222", "memory");
  const unsigned ldsb = (unsigned)(size_t)lds;
  const unsigned lane = tid & 63u, w = __builtin_amdgcn_readfirstlane(tid >> 6), wm = w >> 1, wn = w & 1u;
  const unsigned xcd = bid & 7u, j = (bid >> 3) & 15u, hs = bid >> 7, head = 2u * xcd + hs, rb = ((j >> 1) & 1u) + ((j >> 2) & 1u) * 4u + ((j & 1u) ? 0u : 2u), isv = j >> 3, c4 = hs * 2u + isv;
  u32x8 sM;
  sM[0] = ldsb + w * 8192u; sM[1] = ldsb + w * 8192u + 4096u; sM[2] = ldsb + 65536u + w * 8192u; sM[3] = isv;
  sM[4] = ldsb + w * 4096u; sM[5] = ldsb + 16384u + w * 4096u; sM[6] = 128u; sM[7] = w;
  const unsigned swz = ((lane & 7u) ^ (lane >> 4)) * 16u;
  const unsigned w0 = (16u * w + ((lane >> 2) & 3u)) * 8192u + (lane >> 4) * 64u + (lane & 3u) * 16u;
  u32x2 vA01, vW01;
  vA01[0] = (64u * w + (lane >> 3)) * 2048u + swz; vA01[1] = vA01[0] ^ 64u; vW01[0] = w0; vW01[1] = w0 ^ 32u;
  const unsigned oW = ((lane >> 5) * 64u + rb * 8u + ((lane >> 2) & 7u)) * 8192u + (lane & 3u) * 128u;
  const unsigned oA = ((xcd * 4u + c4) * 8u + (lane & 7u)) * 2048u;
  unsigned o0 = w == 0u ? oW : (w == 3u ? oA + (lane >> 3) * 128u : 0u);
  const unsigned flg = (unsigned)WS_FLAG + head * 4352u;
  const unsigned pi = j >> 1, role = j & 1u, slot = head * 8u + pi, qa = 15u - pi, TA = 32u - 2u * pi, n1 = __builtin_amdgcn_readfirstlane(TA > 19u + 1u ? TA - 19u : 0u), rb2 = role ? pi : qa;
  const bool two = role && n1;
  const unsigned kb = (unsigned)WS_K + head * 0x100000u, qb = (unsigned)WS_Q + head * 0x100000u, wf = (unsigned)WS_WF + head * 262144u;
  unsigned tb = lane;
#define TB(k, v) asm volatile("v_writelane_b32 %0, %1, " #k : "+v"(tb) : "s"((unsigned)(v)))
  TB(0, qb + qa * 65536u); TB(1, kb + (two ? (19u - 2u) * 32768u : 0u)); TB(2, role ? (n1 ? n1 : 2u * pi + 2u) : TA - n1); TB(3, role ? (n1 ? 2u : 1u) : (n1 ? 5u : 1u));
  TB(4, (unsigned)WS_OP + head * 1835008u + rb2 * 8u * 14336u); TB(5, role ? 0u : n1 * 32768u); TB(6, two ? kb : wf); TB(7, two ? 2u * pi + 2u : 0u);
  TB(8, head & 1u); TB(9, ldsb + w * 8192u); TB(10, w); TB(11, wf); TB(12, (unsigned)WS_PART + slot * 65536u); TB(13, (unsigned)WS_ML + slot * 4096u + w * 1024u);
  TB(14, flg + 256u + j * 8u); TB(15, flg + 256u); TB(16, flg + 512u + j * 16u); TB(17, pi * 16u + w * 4u); TB(18, (unsigned)WS_KVP + head * 65536u);
  TB(19, (unsigned)WS_KVP + head * 65536u + rb2 * 4096u + w * 1024u); TB(20, role); TB(21, flg + 1024u + pi * 128u); TB(22, bid == (0 ? 8u : 0u) ? (unsigned)WS_FLAG + 16u * 4352u : 0x40000000u); TB(23, flg + 256u + (16u + pi) * 8u);
  { const unsigned gf = (unsigned)WS_FLAG + 17u * 4352u + rb2 * 2304u, qt = rb2 * 8u + (head >> 1), pr = (head & 1u) + 2u * w;
    TB(30, gf + head * 8u); TB(31, gf); TB(32, (unsigned)WS_OP + (qt * 14u + pr) * 1024u); TB(33, (rb2 * 8u + ((head & 2u) ? 7u - (head >> 2) : (head >> 2))) * 16u * 896u + pr * 64u); TB(34, qa - pi); }
  asm volatile("" : "+{v[2:3]}"(vA01), "+{v[6:7]}"(vW01), "+v"(o0));
  asm volatile("s_waitcnt lgkmcnt(0)" : "+{s[52:67]}"(ka), "+{s[76:79]}"(kb4), "+{s[40:41]}"(kc), "+{s[68:75]}"(sM));
#define KP(a, i) ((char*)(((size_t)(a)[2 * (i) + 1] << 32) | (a)[2 * (i)]))
  const char *x = KP(ka, 0), *ckv = KP(ka, 1), *wq = KP(ka, 2), *wuk = KP(ka, 3), *wuv = KP(ka, 4), *wqkv = KP(ka, 5), *gw = KP(ka, 6), *wo = KP(ka, 7), *bwo = KP(kb4, 0);
  char *ws = KP(kb4, 1), *out = KP(kc, 0);
#undef KP
  const char* wmat = (isv ? wuv : wuk) + head * 512u;
  const char* arow = ckv + (size_t)rb * 524288u;
  u32x4 sA0 = mk_srd(arow, 0x400000u - rb * 524288u);
  u32x4 sWd = mk_srd(wmat - 4096, 0x800000u + 4096u - head * 512u);
  const char* pf0 = (w == 0u ? wmat : arow) + o0;
  u32x16 sOFF;
  asm volatile(
    "global_load_dword a222, v[10:11], off\n"
    "s_mov_b32 s88, 4096\n"
    "s_mov_b32 s89, 3328\n"
    "s_mov_b32 s90, 34816\n"
    "s_mov_b32 s91, 34048\n"
    "s_mov_b32 s80, 0\n"
    "s_mov_b32 s81, 15360\n"
    "s_mov_b32 s82, 30720\n"
    "s_mov_b32 s83, 46080\n"
    "s_add_u32 m0, s70, 0\n"
    "s_nop 0\n"
    "buffer_load_dwordx4 v6, s[44:47], s88 offen offset:0 lds\n"
    "buffer_load_dwordx4 v6, s[44:47], s89 offen offset:1024 lds\n"
    "buffer_load_dwordx4 v6, s[44:47], s90 offen offset:2048 lds\n"
    "buffer_load_dwordx4 v6, s[44:47], s91 offen offset:3072 lds\n"
    "s_mov_b32 s92, 69632\n"
    "s_mov_b32 s93, 68864\n"
    "s_mov_b32 s94, 100352\n"
    "s_mov_b32 s95, 99584\n"
    "s_mov_b32 s84, 65536\n"
    "s_mov_b32 s85, 80896\n"
    "s_mov_b32 s86, 96256\n"
    "s_mov_b32 s87, 111616\n"
    : "={s[80:95]}"(sOFF) : "{v[2:3]}"(vA01), "{v[6:7]}"(vW01), "{s[36:39]}"(sA0), "{s[44:47]}"(sWd), "{s[68:75]}"(sM), "{v[10:11]}"(pf0) : "scc", "memory", "a222", "a223");
  u32x8 aR;
  for (unsigned k = 0; k < 2u; k++) { const unsigned p = 0 && k ? 64u + (j * 4u + w) % 20u : j * 4u + w + 64u * k, tw = p / 3u, part = p - tw * 3u;
    aR[k] = p < 84u ? (tw * 16u + (lane >> 2)) * 2688u + part * 896u + (((lane & 3u) ^ (((lane >> 5) & 1u) * 2u)) * 16u) : 0x40000000u;
    aR[2 + k] = p < 84u ? (unsigned)WS_WB + head * 86016u + p * 1024u + lane * 16u : 0x40000000u; }
  aR[4] = tid == 0u ? flg + 768u + j * 16u : 0x40000000u; aR[5] = aR[4]; aR[6] = aR[4]; aR[7] = aR[4];
  u32x4 oE; unsigned o1;
  { const unsigned ph = rb & 3u, l56 = (lane < 56u ? lane : 55u) * 128u, wrow = ((ph + 4u) * 64u + (rb >> 2) * 32u + w * 16u + (lane >> 2)) * 8192u + (lane & 3u) * 128u;
    const unsigned arow8 = oA + (8u + ((lane >> 3) & 3u)) * 128u, cod = 11264u + ph * 4608u + lane * 72u, lin = bid * 14336u + l56;
    const bool w01 = w < 2u, w2 = w == 2u;
    oE[0] = w01 ? wrow : (w2 ? arow8 : bid * 7168u + l56);
    oE[1] = w01 ? wrow + 256u * 8192u : (w2 ? arow8 + 512u : lin);
    oE[2] = w01 ? wrow + 512u * 8192u : (w2 ? cod : lin + 7168u);
    oE[3] = w01 ? lin + w * 7168u : (w2 ? cod + 18432u : cod + 36864u);
    o1 = w == 1u ? oW + 128u * 8192u : (w2 ? bid * 4704u + (lane < 37u ? lane : 37u) * 124u : (w == 3u ? bid * 1568u + (lane < 12u ? lane : 12u) * 128u : 0u)); }
  const char* pE = ws + WS_FLAG + 16u * 4352u;
  const size_t wop = (size_t)(wo + (size_t)head * 256u * 896u), bop = (size_t)(bwo + (size_t)(head * 28u - (head & 1u) * 4u) * 896u);
  TB(24, wop); TB(25, wop >> 32); TB(26, bop); TB(27, bop >> 32); TB(28, (size_t)out); TB(29, (size_t)out >> 32);
#undef TB
  u32x2 aW; aW[0] = tb; aW[1] = tb;
  u32x8 aE;
  { const char* bE[4] = {usel(w < 2u, wmat, usel(w == 2u, arow, x)), usel(w < 2u, wmat, usel(w == 2u, arow, wq)), usel(w < 2u, wmat, usel(w == 2u, code, wq)), usel(w < 2u, wo, code)};
    for (int e = 0; e < 4; e++) { const size_t p = (size_t)(bE[e] + oE[e]); aE[2 * e] = (unsigned)p; aE[2 * e + 1] = (unsigned)(p >> 32); } }
  asm volatile("" : "+v"(aE));
  asm volatile(
    "s_add_u32 m0, s68, 0\n"
    "s_nop 0\n"
    "buffer_load_dwordx4 v2, s[36:39], s80 offen offset:0 lds\n"
    "buffer_load_dwordx4 v3, s[36:39], s81 offen offset:1024 lds\n"
    "buffer_load_dwordx4 v2, s[36:39], s82 offen offset:2048 lds\n"
    "buffer_load_dwordx4 v3, s[36:39], s83 offen offset:3072 lds\n"
    "s_add_u32 m0, s70, 4096\n"
    "s_nop 0\n"
    "buffer_load_dwordx4 v7, s[44:47], s92 offen offset:0 lds\n"
    "buffer_load_dwordx4 v7, s[44:47], s93 offen offset:1024 lds\n"
    "buffer_load_dwordx4 v7, s[44:47], s94 offen offset:2048 lds\n"
    "buffer_load_dwordx4 v7, s[44:47], s95 offen offset:3072 lds\n"
    "s_add_u32 m0, s69, 0\n"
    "s_nop 0\n"
    "buffer_load_dwordx4 v2, s[36:39], s84 offen offset:0 sc1 lds\n"
    "buffer_load_dwordx4 v3, s[36:39], s85 offen offset:1024 sc1 lds\n"
    "buffer_load_dwordx4 v2, s[36:39], s86 offen offset:2048 sc1 lds\n"
    "buffer_load_dwordx4 v3, s[36:39], s87 offen offset:3072 lds\n"
    :: "{s[80:95]}"(sOFF), "{v[2:3]}"(vA01), "{v[6:7]}"(vW01), "{s[36:39]}"(sA0), "{s[44:47]}"(sWd), "{s[68:75]}"(sM) : "scc", "memory");
  const unsigned r16 = lane & 15u, g = lane >> 4, q4 = (lane & 15u) >> 2, p4 = lane & 3u, f = (r16 >> 1) & 7u;
  u32x2 vRA; u32x4 vRW;
  vRA[0] = ldsb + (wm * 128u + r16) * 128u + ((g ^ f) * 16u); vRA[1] = ldsb + (wm * 128u + r16) * 128u + (((4u + g) ^ f) * 16u);
  vRW[0] = ldsb + 65536u + wn * 1024u + g * 4096u + q4 * 64u + (g & 1u) * 32u + p4 * 8u; vRW[1] = vRW[0] ^ 32u;
  vRW[2] = kb + isv * 0x1000000u + rb * 131072u + lane * 16u + wm * 65536u + wn * (isv ? 8192u : 4096u); vRW[3] = flg + 768u + (lane & 15u) * 16u;
  u32x16 aP;
  aP[0] = (lane >> 3) * 896u + swz; aP[1] = aP[0] ^ 64u;
  aP[2] = (unsigned)WS_WB + head * 86016u + w * 3072u + lane * 16u;
  for (unsigned s = 0; s < 2; s++) for (unsigned a = 0; a < 2; a++) aP[3 + 2 * s + a] = ldsb + ((4u * wm + ((2u * a) ^ (2u * wn))) * 16u + r16) * 128u + (((4u * s + g) ^ f) * 16u);
  aP[7] = ldsb + 16384u + (g >> 1) * 4096u + (g & 1u) * 512u + q4 * 64u + (g & 1u) * 32u + p4 * 8u;
  for (unsigned a = 0; a < 2; a++) { const unsigned o = lane * 16u + (4u * wm + ((2u * a) ^ (2u * wn))) * 8192u + wn * 4096u;
    aP[8 + a] = (n1 && !role ? qb + rb2 * 65536u : 0x40000000u) + o; aP[12 + a] = ldsb + 98304u + lane * 16u + (2u * wm + (a ^ wn)) * 8192u + wn * 4096u; }
  aP[10] = w0; aP[11] = ldsb + 16384u + w * 4096u + 3072u + g * 16u; aP[14] = w0; aP[15] = w0;
  u32x4 sH; sH[0] = (unsigned)(size_t)gw; sH[1] = (unsigned)((size_t)gw >> 32); sH[2] = 0x1111u << (rb & 3u); sH[3] = w;
  const u32x4 sO = mk_srd(ws, 0x8000000u);
  const unsigned bo = head * 56u - (head & 1u) * 8u;
  u32x4 sX = mk_srd(x + (size_t)rb2 * 114688u, 2048u * 896u - rb2 * 114688u), sWb = mk_srd(wqkv + bo, 448u * 2688u - bo), sWq = mk_srd(wq + head * 512u - 4096, 448u * 8192u + 4096u - head * 512u);
  const char* pf1 = usel(w == 1u, wmat, usel(w == 2u, wqkv, usel(w == 3u, bwo, gw))) + o1;
  unsigned ln16 = lane * 16u;
  asm volatile(
    "s_mov_b32 s99, s71\n"
    "s_load_dwordx2 s[16:17], s[16:17], 0x0\n"
    "v_mov_b32 v4, v2\n"
    "v_mov_b32 v5, v3\n"
    "v_mov_b32 v0, v3\n"
    "s_mov_b64 s[40:41], s[36:37]\n"
    "s_mov_b64 s[42:43], s[38:39]\n"
    "s_mov_b64 s[76:77], s[36:37]\n"
    "s_mov_b64 s[78:79], s[38:39]\n"
    "s_getpc_b64 s[34:35]\n"
    ".Lk1_pc:\n"
    "s_cmp_eq_u32 s99, 0\n"
    "s_cselect_b32 s97, .Lk1_h1kr_ret-.Lk1_pc, s97\n"
    "s_cmp_lg_u32 s99, 0\n"
    "s_cselect_b32 s97, .Lk1_h1vr_ret-.Lk1_pc, s97\n"
    "s_add_u32 s18, s34, s97\n"
    "s_addc_u32 s19, s35, 0\n"
    "s_add_u32 s34, s34, .Lk1_rp0-.Lk1_pc\n"
    "s_addc_u32 s35, s35, 0\n"
    "v_add_u32 v2, 128, v2\n"
    "v_add_u32 v3, 128, v3\n"
    "v_add_u32 v4, s74, v4\n"
    "v_add_u32 v5, s74, v5\n"
    "v_add_u32 v0, s74, v0\n"
    "v_add_u32 v6, 0x80000, v6\n"
    "v_add_u32 v7, 0x80000, v7\n"
    "global_load_dword a222, v[10:11], off\n"
    "v_mov_b32 v10, s64\n"
    "v_mov_b32 v11, s65\n"
    "s_mov_b32 s67, 0\n"
    "v_mov_b64 v[16:17], 0\n"
    "v_mov_b64 v[18:19], 0\n"
    "v_mov_b64 v[20:21], 0\n"
    "v_mov_b64 v[22:23], 0\n"
    "v_mov_b64 v[24:25], 0\n"
    "v_mov_b64 v[26:27], 0\n"
    "v_mov_b64 v[28:29], 0\n"
    "v_mov_b64 v[30:31], 0\n"
    "v_mov_b64 v[32:33], 0\n"
    "v_mov_b64 v[34:35], 0\n"
    "v_mov_b64 v[36:37], 0\n"
    "v_mov_b64 v[38:39], 0\n"
    "v_mov_b64 v[40:41], 0\n"
    "v_mov_b64 v[42:43], 0\n"
    "v_mov_b64 v[44:45], 0\n"
    "v_mov_b64 v[46:47], 0\n"
    "v_mov_b64 v[48:49], 0\n"
    "v_mov_b64 v[50:51], 0\n"
    "v_mov_b64 v[52:53], 0\n"
    "v_mov_b64 v[54:55], 0\n"
    "v_mov_b64 v[56:57], 0\n"
    "v_mov_b64 v[58:59], 0\n"
    "v_mov_b64 v[60:61], 0\n"
    "v_mov_b64 v[62:63], 0\n"
    "v_mov_b64 v[64:65], 0\n"
    "v_mov_b64 v[66:67], 0\n"
    "v_mov_b64 v[68:69], 0\n"
    "v_mov_b64 v[70:71], 0\n"
    "v_mov_b64 v[72:73], 0\n"
    "v_mov_b64 v[74:75], 0\n"
    "v_mov_b64 v[76:77], 0\n"
    "v_mov_b64 v[78:79], 0\n"
    "v_mov_b64 v[80:81], 0\n"
    "v_mov_b64 v[82:83], 0\n"
    "v_mov_b64 v[84:85], 0\n"
    "v_mov_b64 v[86:87], 0\n"
    "v_mov_b64 v[88:89], 0\n"
    "v_mov_b64 v[90:91], 0\n"
    "v_mov_b64 v[92:93], 0\n"
    "v_mov_b64 v[94:95], 0\n"
    "v_mov_b64 v[96:97], 0\n"
    "v_mov_b64 v[98:99], 0\n"
    "v_mov_b64 v[100:101], 0\n"
    "v_mov_b64 v[102:103], 0\n"
    "v_mov_b64 v[104:105], 0\n"
    "v_mov_b64 v[106:107], 0\n"
    "v_mov_b64 v[108:109], 0\n"
    "v_mov_b64 v[110:111], 0\n"
    "v_mov_b64 v[112:113], 0\n"
    "v_mov_b64 v[114:115], 0\n"
    "v_mov_b64 v[116:117], 0\n"
    "v_mov_b64 v[118:119], 0\n"
    "v_mov_b64 v[120:121], 0\n"
    "v_mov_b64 v[122:123], 0\n"
    "v_mov_b64 v[124:125], 0\n"
    "v_mov_b64 v[126:127], 0\n"
    "v_mov_b64 v[128:129], 0\n"
    "v_mov_b64 v[130:131], 0\n"
    "v_mov_b64 v[132:133], 0\n"
    "v_mov_b64 v[134:135], 0\n"
    "v_mov_b64 v[136:137], 0\n"
    "v_mov_b64 v[138:139], 0\n"
    "v_mov_b64 v[140:141], 0\n"
    "v_mov_b64 v[142:143], 0\n"
    "v_mov_b64 v[144:145], 0\n"
    "v_mov_b64 v[146:147], 0\n"
    "v_mov_b64 v[148:149], 0\n"
    "v_mov_b64 v[150:151], 0\n"
    "v_mov_b64 v[152:153], 0\n"
    "v_mov_b64 v[154:155], 0\n"
    "v_mov_b64 v[156:157], 0\n"
    "v_mov_b64 v[158:159], 0\n"
    "v_mov_b64 v[160:161], 0\n"
    "v_mov_b64 v[162:163], 0\n"
    "v_mov_b64 v[164:165], 0\n"
    "v_mov_b64 v[166:167], 0\n"
    "v_mov_b64 v[168:169], 0\n"
    "v_mov_b64 v[170:171], 0\n"
    "v_mov_b64 v[172:173], 0\n"
    "v_mov_b64 v[174:175], 0\n"
    "v_mov_b64 v[176:177], 0\n"
    "v_mov_b64 v[178:179], 0\n"
    "v_mov_b64 v[180:181], 0\n"
    "v_mov_b64 v[182:183], 0\n"
    "v_mov_b64 v[184:185], 0\n"
    "v_mov_b64 v[186:187], 0\n"
    "v_mov_b64 v[188:189], 0\n"
    "v_mov_b64 v[190:191], 0\n"
    "v_mov_b64 v[192:193], 0\n"
    "v_mov_b64 v[194:195], 0\n"
    "v_mov_b64 v[196:197], 0\n"
    "v_mov_b64 v[198:199], 0\n"
    "v_mov_b64 v[200:201], 0\n"
    "v_mov_b64 v[202:203], 0\n"
    "v_mov_b64 v[204:205], 0\n"
    "v_mov_b64 v[206:207], 0\n"
    "v_mov_b64 v[208:209], 0\n"
    "v_mov_b64 v[210:211], 0\n"
    "v_mov_b64 v[212:213], 0\n"
    "v_mov_b64 v[214:215], 0\n"
    "v_mov_b64 v[216:217], 0\n"
    "v_mov_b64 v[218:219], 0\n"
    "v_mov_b64 v[220:221], 0\n"
    "v_mov_b64 v[222:223], 0\n"
    "v_mov_b64 v[224:225], 0\n"
    "v_mov_b64 v[226:227], 0\n"
    "v_mov_b64 v[228:229], 0\n"
    "v_mov_b64 v[230:231], 0\n"
    "v_mov_b64 v[232:233], 0\n"
    "v_mov_b64 v[234:235], 0\n"
    "v_mov_b64 v[236:237], 0\n"
    "v_mov_b64 v[238:239], 0\n"
    "v_mov_b64 v[240:241], 0\n"
    "v_mov_b64 v[242:243], 0\n"
    "v_mov_b64 v[244:245], 0\n"
    "v_mov_b64 v[246:247], 0\n"
    "v_mov_b64 v[248:249], 0\n"
    "v_mov_b64 v[250:251], 0\n"
    "v_mov_b64 v[252:253], 0\n"
    "v_mov_b64 v[254:255], 0\n"
    "v_accvgpr_write_b32 a0, 0\n"
    "v_accvgpr_write_b32 a1, 0\n"
    "v_accvgpr_write_b32 a2, 0\n"
    "v_accvgpr_write_b32 a3, 0\n"
    "v_accvgpr_write_b32 a4, 0\n"
    "v_accvgpr_write_b32 a5, 0\n"
    "v_accvgpr_write_b32 a6, 0\n"
    "v_accvgpr_write_b32 a7, 0\n"
    "v_accvgpr_write_b32 a8, 0\n"
    "v_accvgpr_write_b32 a9, 0\n"
    "v_accvgpr_write_b32 a10, 0\n"
    "v_accvgpr_write_b32 a11, 0\n"
    "v_accvgpr_write_b32 a12, 0\n"
    "v_accvgpr_write_b32 a13, 0\n"
    "v_accvgpr_write_b32 a14, 0\n"
    "v_accvgpr_write_b32 a15, 0\n"
    "s_add_u32 m0, s70, 32768\n"
    "s_nop 0\n"
    "buffer_load_dwordx4 v6, s[44:47], s88 offen offset:0 lds\n"
    "buffer_load_dwordx4 v6, s[44:47], s89 offen offset:1024 lds\n"
    "buffer_load_dwordx4 v6, s[44:47], s90 offen offset:2048 lds\n"
    "buffer_load_dwordx4 v6, s[44:47], s91 offen offset:3072 lds\n"
    "s_add_u32 m0, s70, 36864\n"
    "s_nop 0\n"
    "buffer_load_dwordx4 v7, s[44:47], s92 offen offset:0 lds\n"
    "buffer_load_dwordx4 v7, s[44:47], s93 offen offset:1024 lds\n"
    "buffer_load_dwordx4 v7, s[44:47], s94 offen offset:2048 lds\n"
    "buffer_load_dwordx4 v7, s[44:47], s95 offen offset:3072 lds\n"
    "s_waitcnt vmcnt(9)\n"
    "s_barrier\n"
    "ds_read_b128 a[144:147], v8 offset:0\n"
    "ds_read_b128 a[148:151], v9 offset:0\n"
    "ds_read_b64_tr_b16 a[16:17], v12 offset:0\n"
    "ds_read_b64_tr_b16 a[18:19], v12 offset:2048\n"
    "ds_read_b64_tr_b16 a[20:21], v12 offset:16384\n"
    "ds_read_b64_tr_b16 a[22:23], v12 offset:18432\n"
    "ds_read_b64_tr_b16 a[24:25], v13 offset:0\n"
    "ds_read_b64_tr_b16 a[26:27], v13 offset:2048\n"
    "ds_read_b64_tr_b16 a[28:29], v13 offset:16384\n"
    "ds_read_b64_tr_b16 a[30:31], v13 offset:18432\n"
    "ds_read_b64_tr_b16 a[32:33], v12 offset:256\n"
    "ds_read_b64_tr_b16 a[34:35], v12 offset:2304\n"
    "ds_read_b64_tr_b16 a[36:37], v12 offset:16640\n"
    "ds_read_b64_tr_b16 a[38:39], v12 offset:18688\n"
    "ds_read_b64_tr_b16 a[40:41], v13 offset:256\n"
    "ds_read_b64_tr_b16 a[42:43], v13 offset:2304\n"
    "ds_read_b64_tr_b16 a[44:45], v13 offset:16640\n"
    "ds_read_b64_tr_b16 a[46:47], v13 offset:18688\n"
    "ds_read_b64_tr_b16 a[48:49], v12 offset:512\n"
    "ds_read_b64_tr_b16 a[50:51], v12 offset:2560\n"
    "ds_read_b64_tr_b16 a[52:53], v12 offset:16896\n"
    "ds_read_b64_tr_b16 a[54:55], v12 offset:18944\n"
    "ds_read_b64_tr_b16 a[56:57], v13 offset:512\n"
    "ds_read_b64_tr_b16 a[58:59], v13 offset:2560\n"
    "ds_read_b64_tr_b16 a[60:61], v13 offset:16896\n"
    "ds_read_b64_tr_b16 a[62:63], v13 offset:18944\n"
    "ds_read_b64_tr_b16 a[64:65], v12 offset:768\n"
    "ds_read_b64_tr_b16 a[66:67], v12 offset:2816\n"
    "ds_read_b64_tr_b16 a[68:69], v12 offset:17152\n"
    "ds_read_b64_tr_b16 a[70:71], v12 offset:19200\n"
    "ds_read_b64_tr_b16 a[72:73], v13 offset:768\n"
    "ds_read_b64_tr_b16 a[74:75], v13 offset:2816\n"
    "ds_read_b64_tr_b16 a[76:77], v13 offset:17152\n"
    "ds_read_b64_tr_b16 a[78:79], v13 offset:19200\n"
    "s_add_u32 m0, s68, 32768\n"
    "s_nop 0\n"
    "buffer_load_dwordx4 v2, s[36:39], s80 offen offset:0 lds\n"
    "ds_read_b128 a[152:155], v8 offset:2048\n"
    "buffer_load_dwordx4 v3, s[36:39], s81 offen offset:1024 lds\n"
    "buffer_load_dwordx4 v2, s[36:39], s82 offen offset:2048 lds\n"
    "ds_read_b128 a[156:159], v9 offset:2048\n"
    "ds_read_b128 a[160:163], v8 offset:4096\n"
    "ds_read_b128 a[164:167], v9 offset:4096\n"
    "ds_read_b128 a[168:171], v8 offset:6144\n"
    "ds_read_b128 a[172:175], v9 offset:6144\n"
    "s_waitcnt lgkmcnt(0)\n"
    "s_add_u32 s16, s16, 0x7f4a7c15\n"
    "s_addc_u32 s17, s17, 0x9e3779b9\n"
    "s_mov_b32 s96, 8\n"
    "s_cmp_eq_u32 s99, 0\n"
    "s_cbranch_scc0 .Lk1_loop_v\n"
    ".p2align 5\n"
    "s_nop 0\n"
    "s_nop 0\n"
    ".Lk1_loop_k:\n"
    "s_waitcnt lgkmcnt(15)\n"
    "v_mfma_f32_16x16x32_bf16 a[0:3], a[16:19], a[144:147], a[0:3]\n"
    "v_mfma_f32_16x16x32_bf16 a[0:3], a[20:23], a[148:151], a[0:3]\n"
    "buffer_load_dwordx4 v3, s[36:39], s83 offen offset:3072 lds\n"
    "v_mfma_f32_16x16x32_bf16 a[4:7], a[28:31], a[148:151], a[4:7]\n"
    "v_mfma_f32_16x16x32_bf16 a[4:7], a[24:27], a[144:147], a[4:7]\n"
    "v_mfma_f32_16x16x32_bf16 a[8:11], a[32:35], a[144:147], a[8:11]\n"
    "ds_read_b128 a[176:179], v8 offset:8192\n"
    "v_mfma_f32_16x16x32_bf16 a[8:11], a[36:39], a[148:151], a[8:11]\n"
    "s_add_u32 m0, s69, 32768\n"
    "v_mfma_f32_16x16x32_bf16 a[12:15], a[44:47], a[148:151], a[12:15]\n"
    "v_mfma_f32_16x16x32_bf16 a[12:15], a[40:43], a[144:147], a[12:15]\n"
    "v_mfma_f32_16x16x32_bf16 v[16:19], a[48:51], a[144:147], v[16:19]\n"
    "v_mfma_f32_16x16x32_bf16 v[16:19], a[52:55], a[148:151], v[16:19]\n"
    "s_waitcnt lgkmcnt(11)\n"
    "v_mfma_f32_16x16x32_bf16 v[20:23], a[60:63], a[148:151], v[20:23]\n"
    "ds_read_b128 a[180:183], v9 offset:8192\n"
    "v_mfma_f32_16x16x32_bf16 v[20:23], a[56:59], a[144:147], v[20:23]\n"
    "v_mfma_f32_16x16x32_bf16 v[24:27], a[64:67], a[144:147], v[24:27]\n"
    "buffer_load_dwordx4 v4, s[40:43], s84 offen offset:0 sc1 lds\n"
    "v_mfma_f32_16x16x32_bf16 v[24:27], a[68:71], a[148:151], v[24:27]\n"
    "s_waitcnt lgkmcnt(6)\n"
    "v_mfma_f32_16x16x32_bf16 v[28:31], a[76:79], a[148:151], v[28:31]\n"
    "buffer_load_dwordx4 v5, s[40:43], s85 offen offset:1024 sc1 lds\n"
    "v_mfma_f32_16x16x32_bf16 v[28:31], a[72:75], a[144:147], v[28:31]\n"
    "v_mfma_f32_16x16x32_bf16 v[32:35], a[16:19], a[152:155], v[32:35]\n"
    "ds_read_b128 a[184:187], v8 offset:10240\n"
    "v_mfma_f32_16x16x32_bf16 v[32:35], a[20:23], a[156:159], v[32:35]\n"
    "v_mfma_f32_16x16x32_bf16 v[36:39], a[28:31], a[156:159], v[36:39]\n"
    "v_mfma_f32_16x16x32_bf16 v[36:39], a[24:27], a[152:155], v[36:39]\n"
    "buffer_load_dwordx4 v4, s[40:43], s86 offen offset:2048 sc1 lds\n"
    "v_mfma_f32_16x16x32_bf16 v[40:43], a[32:35], a[152:155], v[40:43]\n"
    "v_mfma_f32_16x16x32_bf16 v[40:43], a[36:39], a[156:159], v[40:43]\n"
    "v_mfma_f32_16x16x32_bf16 v[44:47], a[44:47], a[156:159], v[44:47]\n"
    "ds_read_b128 a[188:191], v9 offset:10240\n"
    "v_mfma_f32_16x16x32_bf16 v[44:47], a[40:43], a[152:155], v[44:47]\n"
    "buffer_load_dwordx4 v0, s[76:79], s87 offen offset:3072 lds\n"
    "v_mfma_f32_16x16x32_bf16 v[48:51], a[48:51], a[152:155], v[48:51]\n"
    "v_mfma_f32_16x16x32_bf16 v[48:51], a[52:55], a[156:159], v[48:51]\n"
    "v_mfma_f32_16x16x32_bf16 v[52:55], a[60:63], a[156:159], v[52:55]\n"
    "v_mfma_f32_16x16x32_bf16 v[52:55], a[56:59], a[152:155], v[52:55]\n"
    "v_mfma_f32_16x16x32_bf16 v[56:59], a[64:67], a[152:155], v[56:59]\n"
    "ds_read_b128 a[192:195], v8 offset:12288\n"
    "v_mfma_f32_16x16x32_bf16 v[56:59], a[68:71], a[156:159], v[56:59]\n"
    "s_bitcmp1_b32 s66, s67\n"
    "s_cbranch_scc1 .Lk1_h0k\n"
    ".Lk1_h0k_ret:\n"
    "v_mfma_f32_16x16x32_bf16 v[60:63], a[76:79], a[156:159], v[60:63]\n"
    "global_load_dword a222, v[10:11], off\n"
    "v_mfma_f32_16x16x32_bf16 v[60:63], a[72:75], a[152:155], v[60:63]\n"
    "v_mov_b32 v10, s64\n"
    "s_waitcnt lgkmcnt(7)\n"
    "v_mfma_f32_16x16x32_bf16 v[64:67], a[16:19], a[160:163], v[64:67]\n"
    "v_mov_b32 v11, s65\n"
    "s_add_u32 s67, s67, 1\n"
    "v_mfma_f32_16x16x32_bf16 v[64:67], a[20:23], a[164:167], v[64:67]\n"
    "v_mfma_f32_16x16x32_bf16 v[68:71], a[28:31], a[164:167], v[68:71]\n"
    "ds_read_b128 a[196:199], v9 offset:12288\n"
    "v_add_u32 v2, 128, v2\n"
    "v_mfma_f32_16x16x32_bf16 v[68:71], a[24:27], a[160:163], v[68:71]\n"
    "v_mfma_f32_16x16x32_bf16 v[72:75], a[32:35], a[160:163], v[72:75]\n"
    "v_mfma_f32_16x16x32_bf16 v[72:75], a[36:39], a[164:167], v[72:75]\n"
    "v_add_u32 v3, 128, v3\n"
    "v_mfma_f32_16x16x32_bf16 v[76:79], a[44:47], a[164:167], v[76:79]\n"
    "v_mfma_f32_16x16x32_bf16 v[76:79], a[40:43], a[160:163], v[76:79]\n"
    "v_mfma_f32_16x16x32_bf16 v[80:83], a[48:51], a[160:163], v[80:83]\n"
    "ds_read_b128 a[200:203], v8 offset:14336\n"
    "v_mfma_f32_16x16x32_bf16 v[80:83], a[52:55], a[164:167], v[80:83]\n"
    "v_add_u32 v4, s74, v4\n"
    "v_mfma_f32_16x16x32_bf16 v[84:87], a[60:63], a[164:167], v[84:87]\n"
    "v_mfma_f32_16x16x32_bf16 v[84:87], a[56:59], a[160:163], v[84:87]\n"
    "v_mfma_f32_16x16x32_bf16 v[88:91], a[64:67], a[160:163], v[88:91]\n"
    "v_mfma_f32_16x16x32_bf16 v[88:91], a[68:71], a[164:167], v[88:91]\n"
    "v_add_u32 v5, s74, v5\n"
    "v_mfma_f32_16x16x32_bf16 v[92:95], a[76:79], a[164:167], v[92:95]\n"
    "ds_read_b128 a[204:207], v9 offset:14336\n"
    "v_mfma_f32_16x16x32_bf16 v[92:95], a[72:75], a[160:163], v[92:95]\n"
    "s_waitcnt lgkmcnt(8)\n"
    "v_mfma_f32_16x16x32_bf16 v[96:99], a[16:19], a[168:171], v[96:99]\n"
    "v_add_u32 v0, s74, v0\n"
    "v_mfma_f32_16x16x32_bf16 v[96:99], a[20:23], a[172:175], v[96:99]\n"
    "v_mfma_f32_16x16x32_bf16 v[100:103], a[28:31], a[172:175], v[100:103]\n"
    "v_mfma_f32_16x16x32_bf16 v[100:103], a[24:27], a[168:171], v[100:103]\n"
    "v_mfma_f32_16x16x32_bf16 v[104:107], a[32:35], a[168:171], v[104:107]\n"
    "v_add_u32 v6, 0x80000, v6\n"
    "v_mfma_f32_16x16x32_bf16 v[104:107], a[36:39], a[172:175], v[104:107]\n"
    "v_mfma_f32_16x16x32_bf16 v[108:111], a[44:47], a[172:175], v[108:111]\n"
    "v_mfma_f32_16x16x32_bf16 v[108:111], a[40:43], a[168:171], v[108:111]\n"
    "v_mfma_f32_16x16x32_bf16 v[112:115], a[48:51], a[168:171], v[112:115]\n"
    "v_add_u32 v7, 0x80000, v7\n"
    "v_mfma_f32_16x16x32_bf16 v[112:115], a[52:55], a[172:175], v[112:115]\n"
    "v_mfma_f32_16x16x32_bf16 v[116:119], a[60:63], a[172:175], v[116:119]\n"
    "v_mfma_f32_16x16x32_bf16 v[116:119], a[56:59], a[168:171], v[116:119]\n"
    "v_mfma_f32_16x16x32_bf16 v[120:123], a[64:67], a[168:171], v[120:123]\n"
    "v_mfma_f32_16x16x32_bf16 v[120:123], a[68:71], a[172:175], v[120:123]\n"
    "v_mfma_f32_16x16x32_bf16 v[124:127], a[76:79], a[172:175], v[124:127]\n"
    "v_mfma_f32_16x16x32_bf16 v[124:127], a[72:75], a[168:171], v[124:127]\n"
    "s_waitcnt lgkmcnt(6)\n"
    "v_mfma_f32_16x16x32_bf16 v[128:131], a[16:19], a[176:179], v[128:131]\n"
    "v_mfma_f32_16x16x32_bf16 v[128:131], a[20:23], a[180:183], v[128:131]\n"
    "s_cmp_eq_u32 s96, 1\n"
    "s_cbranch_scc1 .Lk1_swap_k\n"
    ".Lk1_swap_k_ret:\n"
    "v_mfma_f32_16x16x32_bf16 v[132:135], a[28:31], a[180:183], v[132:135]\n"
    "s_waitcnt vmcnt(1) lgkmcnt(0)\n"
    "s_barrier\n"
    "v_mfma_f32_16x16x32_bf16 v[132:135], a[24:27], a[176:179], v[132:135]\n"
    "ds_read_b128 a[144:147], v8 offset:32768\n"
    "v_mfma_f32_16x16x32_bf16 v[136:139], a[32:35], a[176:179], v[136:139]\n"
    "ds_read_b128 a[148:151], v9 offset:32768\n"
    "s_add_u32 m0, s70, 0\n"
    "v_mfma_f32_16x16x32_bf16 v[136:139], a[36:39], a[180:183], v[136:139]\n"
    "ds_read_b64_tr_b16 a[80:81], v12 offset:32768\n"
    "v_mfma_f32_16x16x32_bf16 v[140:143], a[44:47], a[180:183], v[140:143]\n"
    "v_mfma_f32_16x16x32_bf16 v[140:143], a[40:43], a[176:179], v[140:143]\n"
    "ds_read_b64_tr_b16 a[82:83], v12 offset:34816\n"
    "v_mfma_f32_16x16x32_bf16 v[144:147], a[48:51], a[176:179], v[144:147]\n"
    "ds_read_b64_tr_b16 a[84:85], v12 offset:49152\n"
    "v_mfma_f32_16x16x32_bf16 v[144:147], a[52:55], a[180:183], v[144:147]\n"
    "buffer_load_dwordx4 v6, s[44:47], s88 offen offset:0 lds\n"
    "v_mfma_f32_16x16x32_bf16 v[148:151], a[60:63], a[180:183], v[148:151]\n"
    "ds_read_b64_tr_b16 a[86:87], v12 offset:51200\n"
    "v_mfma_f32_16x16x32_bf16 v[148:151], a[56:59], a[176:179], v[148:151]\n"
    "ds_read_b64_tr_b16 a[88:89], v13 offset:32768\n"
    "v_mfma_f32_16x16x32_bf16 v[152:155], a[64:67], a[176:179], v[152:155]\n"
    "ds_read_b64_tr_b16 a[90:91], v13 offset:34816\n"
    "v_mfma_f32_16x16x32_bf16 v[152:155], a[68:71], a[180:183], v[152:155]\n"
    "buffer_load_dwordx4 v6, s[44:47], s89 offen offset:1024 lds\n"
    "v_mfma_f32_16x16x32_bf16 v[156:159], a[76:79], a[180:183], v[156:159]\n"
    "ds_read_b64_tr_b16 a[92:93], v13 offset:49152\n"
    "v_mfma_f32_16x16x32_bf16 v[156:159], a[72:75], a[176:179], v[156:159]\n"
    "ds_read_b64_tr_b16 a[94:95], v13 offset:51200\n"
    "v_mfma_f32_16x16x32_bf16 v[160:163], a[16:19], a[184:187], v[160:163]\n"
    "ds_read_b64_tr_b16 a[96:97], v12 offset:33024\n"
    "v_mfma_f32_16x16x32_bf16 v[160:163], a[20:23], a[188:191], v[160:163]\n"
    "buffer_load_dwordx4 v6, s[44:47], s90 offen offset:2048 lds\n"
    "v_mfma_f32_16x16x32_bf16 v[164:167], a[28:31], a[188:191], v[164:167]\n"
    "ds_read_b64_tr_b16 a[98:99], v12 offset:35072\n"
    "v_mfma_f32_16x16x32_bf16 v[164:167], a[24:27], a[184:187], v[164:167]\n"
    "ds_read_b64_tr_b16 a[100:101], v12 offset:49408\n"
    "v_mfma_f32_16x16x32_bf16 v[168:171], a[32:35], a[184:187], v[168:171]\n"
    "ds_read_b64_tr_b16 a[102:103], v12 offset:51456\n"
    "v_mfma_f32_16x16x32_bf16 v[168:171], a[36:39], a[188:191], v[168:171]\n"
    "buffer_load_dwordx4 v6, s[44:47], s91 offen offset:3072 lds\n"
    "v_mfma_f32_16x16x32_bf16 v[172:175], a[44:47], a[188:191], v[172:175]\n"
    "ds_read_b64_tr_b16 a[104:105], v13 offset:33024\n"
    "v_mfma_f32_16x16x32_bf16 v[172:175], a[40:43], a[184:187], v[172:175]\n"
    "ds_read_b64_tr_b16 a[106:107], v13 offset:35072\n"
    "v_mfma_f32_16x16x32_bf16 v[176:179], a[48:51], a[184:187], v[176:179]\n"
    "s_add_u32 m0, s70, 4096\n"
    "v_mfma_f32_16x16x32_bf16 v[176:179], a[52:55], a[188:191], v[176:179]\n"
    "ds_read_b64_tr_b16 a[108:109], v13 offset:49408\n"
    "v_mfma_f32_16x16x32_bf16 v[180:183], a[60:63], a[188:191], v[180:183]\n"
    "ds_read_b64_tr_b16 a[110:111], v13 offset:51456\n"
    "v_mfma_f32_16x16x32_bf16 v[180:183], a[56:59], a[184:187], v[180:183]\n"
    "ds_read_b64_tr_b16 a[112:113], v12 offset:33280\n"
    "v_mfma_f32_16x16x32_bf16 v[184:187], a[64:67], a[184:187], v[184:187]\n"
    "buffer_load_dwordx4 v7, s[44:47], s92 offen offset:0 lds\n"
    "v_mfma_f32_16x16x32_bf16 v[184:187], a[68:71], a[188:191], v[184:187]\n"
    "ds_read_b64_tr_b16 a[114:115], v12 offset:35328\n"
    "v_mfma_f32_16x16x32_bf16 v[188:191], a[76:79], a[188:191], v[188:191]\n"
    "ds_read_b64_tr_b16 a[116:117], v12 offset:49664\n"
    "v_mfma_f32_16x16x32_bf16 v[188:191], a[72:75], a[184:187], v[188:191]\n"
    "ds_read_b64_tr_b16 a[118:119], v12 offset:51712\n"
    "v_mfma_f32_16x16x32_bf16 v[192:195], a[16:19], a[192:195], v[192:195]\n"
    "v_mfma_f32_16x16x32_bf16 v[192:195], a[20:23], a[196:199], v[192:195]\n"
    "ds_read_b64_tr_b16 a[120:121], v13 offset:33280\n"
    "v_mfma_f32_16x16x32_bf16 v[196:199], a[28:31], a[196:199], v[196:199]\n"
    "buffer_load_dwordx4 v7, s[44:47], s93 offen offset:1024 lds\n"
    "v_mfma_f32_16x16x32_bf16 v[196:199], a[24:27], a[192:195], v[196:199]\n"
    "ds_read_b64_tr_b16 a[122:123], v13 offset:35328\n"
    "v_mfma_f32_16x16x32_bf16 v[200:203], a[32:35], a[192:195], v[200:203]\n"
    "ds_read_b64_tr_b16 a[124:125], v13 offset:49664\n"
    "v_mfma_f32_16x16x32_bf16 v[200:203], a[36:39], a[196:199], v[200:203]\n"
    "ds_read_b64_tr_b16 a[126:127], v13 offset:51712\n"
    "v_mfma_f32_16x16x32_bf16 v[204:207], a[44:47], a[196:199], v[204:207]\n"
    "buffer_load_dwordx4 v7, s[44:47], s94 offen offset:2048 lds\n"
    "v_mfma_f32_16x16x32_bf16 v[204:207], a[40:43], a[192:195], v[204:207]\n"
    "ds_read_b64_tr_b16 a[128:129], v12 offset:33536\n"
    "v_mfma_f32_16x16x32_bf16 v[208:211], a[48:51], a[192:195], v[208:211]\n"
    "ds_read_b64_tr_b16 a[130:131], v12 offset:35584\n"
    "v_mfma_f32_16x16x32_bf16 v[208:211], a[52:55], a[196:199], v[208:211]\n"
    "ds_read_b64_tr_b16 a[132:133], v12 offset:49920\n"
    "v_mfma_f32_16x16x32_bf16 v[212:215], a[60:63], a[196:199], v[212:215]\n"
    "buffer_load_dwordx4 v7, s[44:47], s95 offen offset:3072 lds\n"
    "v_mfma_f32_16x16x32_bf16 v[212:215], a[56:59], a[192:195], v[212:215]\n"
    "ds_read_b64_tr_b16 a[134:135], v12 offset:51968\n"
    "v_mfma_f32_16x16x32_bf16 v[216:219], a[64:67], a[192:195], v[216:219]\n"
    "ds_read_b64_tr_b16 a[136:137], v13 offset:33536\n"
    "v_mfma_f32_16x16x32_bf16 v[216:219], a[68:71], a[196:199], v[216:219]\n"
    "ds_read_b64_tr_b16 a[138:139], v13 offset:35584\n"
    "s_add_u32 m0, s68, 0\n"
    "v_mfma_f32_16x16x32_bf16 v[220:223], a[76:79], a[196:199], v[220:223]\n"
    "v_mfma_f32_16x16x32_bf16 v[220:223], a[72:75], a[192:195], v[220:223]\n"
    "ds_read_b64_tr_b16 a[140:141], v13 offset:49920\n"
    "v_mfma_f32_16x16x32_bf16 v[224:227], a[16:19], a[200:203], v[224:227]\n"
    "ds_read_b64_tr_b16 a[142:143], v13 offset:51968\n"
    "v_mfma_f32_16x16x32_bf16 v[224:227], a[20:23], a[204:207], v[224:227]\n"
    "ds_read_b128 a[152:155], v8 offset:34816\n"
    "v_mfma_f32_16x16x32_bf16 v[228:231], a[28:31], a[204:207], v[228:231]\n"
    "buffer_load_dwordx4 v2, s[36:39], s80 offen offset:0 lds\n"
    "v_mfma_f32_16x16x32_bf16 v[228:231], a[24:27], a[200:203], v[228:231]\n"
    "ds_read_b128 a[156:159], v9 offset:34816\n"
    "v_mfma_f32_16x16x32_bf16 v[232:235], a[32:35], a[200:203], v[232:235]\n"
    "ds_read_b128 a[160:163], v8 offset:36864\n"
    "v_mfma_f32_16x16x32_bf16 v[232:235], a[36:39], a[204:207], v[232:235]\n"
    "ds_read_b128 a[164:167], v9 offset:36864\n"
    "v_mfma_f32_16x16x32_bf16 v[236:239], a[44:47], a[204:207], v[236:239]\n"
    "buffer_load_dwordx4 v3, s[36:39], s81 offen offset:1024 lds\n"
    "v_mfma_f32_16x16x32_bf16 v[236:239], a[40:43], a[200:203], v[236:239]\n"
    "ds_read_b128 a[168:171], v8 offset:38912\n"
    "v_mfma_f32_16x16x32_bf16 v[240:243], a[48:51], a[200:203], v[240:243]\n"
    "ds_read_b128 a[172:175], v9 offset:38912\n"
    "v_mfma_f32_16x16x32_bf16 v[240:243], a[52:55], a[204:207], v[240:243]\n"
    "buffer_load_dwordx4 v2, s[36:39], s82 offen offset:2048 lds\n"
    "v_mfma_f32_16x16x32_bf16 v[244:247], a[60:63], a[204:207], v[244:247]\n"
    "v_mfma_f32_16x16x32_bf16 v[244:247], a[56:59], a[200:203], v[244:247]\n"
    "v_mfma_f32_16x16x32_bf16 v[248:251], a[64:67], a[200:203], v[248:251]\n"
    "v_mfma_f32_16x16x32_bf16 v[248:251], a[68:71], a[204:207], v[248:251]\n"
    "v_mfma_f32_16x16x32_bf16 v[252:255], a[76:79], a[204:207], v[252:255]\n"
    "v_mfma_f32_16x16x32_bf16 v[252:255], a[72:75], a[200:203], v[252:255]\n"
    "s_cmp_eq_u32 s96, 1\n"
    "s_cbranch_scc1 .Lk1_last_k\n"
    "s_waitcnt lgkmcnt(15)\n"
    "v_mfma_f32_16x16x32_bf16 a[0:3], a[80:83], a[144:147], a[0:3]\n"
    "v_mfma_f32_16x16x32_bf16 a[0:3], a[84:87], a[148:151], a[0:3]\n"
    "buffer_load_dwordx4 v3, s[36:39], s83 offen offset:3072 lds\n"
    "v_mfma_f32_16x16x32_bf16 a[4:7], a[92:95], a[148:151], a[4:7]\n"
    "v_mfma_f32_16x16x32_bf16 a[4:7], a[88:91], a[144:147], a[4:7]\n"
    "v_mfma_f32_16x16x32_bf16 a[8:11], a[96:99], a[144:147], a[8:11]\n"
    "ds_read_b128 a[176:179], v8 offset:40960\n"
    "v_mfma_f32_16x16x32_bf16 a[8:11], a[100:103], a[148:151], a[8:11]\n"
    "s_add_u32 m0, s69, 0\n"
    "v_mfma_f32_16x16x32_bf16 a[12:15], a[108:111], a[148:151], a[12:15]\n"
    "v_mfma_f32_16x16x32_bf16 a[12:15], a[104:107], a[144:147], a[12:15]\n"
    "v_mfma_f32_16x16x32_bf16 v[16:19], a[112:115], a[144:147], v[16:19]\n"
    "v_mfma_f32_16x16x32_bf16 v[16:19], a[116:119], a[148:151], v[16:19]\n"
    "s_waitcnt lgkmcnt(11)\n"
    "v_mfma_f32_16x16x32_bf16 v[20:23], a[124:127], a[148:151], v[20:23]\n"
    "ds_read_b128 a[180:183], v9 offset:40960\n"
    "v_mfma_f32_16x16x32_bf16 v[20:23], a[120:123], a[144:147], v[20:23]\n"
    "v_mfma_f32_16x16x32_bf16 v[24:27], a[128:131], a[144:147], v[24:27]\n"
    "buffer_load_dwordx4 v4, s[40:43], s84 offen offset:0 sc1 lds\n"
    "v_mfma_f32_16x16x32_bf16 v[24:27], a[132:135], a[148:151], v[24:27]\n"
    "s_waitcnt lgkmcnt(6)\n"
    "v_mfma_f32_16x16x32_bf16 v[28:31], a[140:143], a[148:151], v[28:31]\n"
    "buffer_load_dwordx4 v5, s[40:43], s85 offen offset:1024 sc1 lds\n"
    "v_mfma_f32_16x16x32_bf16 v[28:31], a[136:139], a[144:147], v[28:31]\n"
    "v_mfma_f32_16x16x32_bf16 v[32:35], a[80:83], a[152:155], v[32:35]\n"
    "ds_read_b128 a[184:187], v8 offset:43008\n"
    "v_mfma_f32_16x16x32_bf16 v[32:35], a[84:87], a[156:159], v[32:35]\n"
    "v_mfma_f32_16x16x32_bf16 v[36:39], a[92:95], a[156:159], v[36:39]\n"
    "v_mfma_f32_16x16x32_bf16 v[36:39], a[88:91], a[152:155], v[36:39]\n"
    "buffer_load_dwordx4 v4, s[40:43], s86 offen offset:2048 sc1 lds\n"
    "v_mfma_f32_16x16x32_bf16 v[40:43], a[96:99], a[152:155], v[40:43]\n"
    "v_mfma_f32_16x16x32_bf16 v[40:43], a[100:103], a[156:159], v[40:43]\n"
    "v_mfma_f32_16x16x32_bf16 v[44:47], a[108:111], a[156:159], v[44:47]\n"
    "ds_read_b128 a[188:191], v9 offset:43008\n"
    "v_mfma_f32_16x16x32_bf16 v[44:47], a[104:107], a[152:155], v[44:47]\n"
    "buffer_load_dwordx4 v0, s[76:79], s87 offen offset:3072 lds\n"
    "v_mfma_f32_16x16x32_bf16 v[48:51], a[112:115], a[152:155], v[48:51]\n"
    "v_mfma_f32_16x16x32_bf16 v[48:51], a[116:119], a[156:159], v[48:51]\n"
    "v_mfma_f32_16x16x32_bf16 v[52:55], a[124:127], a[156:159], v[52:55]\n"
    "v_mfma_f32_16x16x32_bf16 v[52:55], a[120:123], a[152:155], v[52:55]\n"
    "v_mfma_f32_16x16x32_bf16 v[56:59], a[128:131], a[152:155], v[56:59]\n"
    "ds_read_b128 a[192:195], v8 offset:45056\n"
    "v_mfma_f32_16x16x32_bf16 v[56:59], a[132:135], a[156:159], v[56:59]\n"
    "s_bitcmp1_b32 s66, s67\n"
    "s_cbranch_scc1 .Lk1_h1k\n"
    ".Lk1_h1k_ret:\n"
    "v_mfma_f32_16x16x32_bf16 v[60:63], a[140:143], a[156:159], v[60:63]\n"
    "global_load_dword a222, v[10:11], off\n"
    "v_mfma_f32_16x16x32_bf16 v[60:63], a[136:139], a[152:155], v[60:63]\n"
    "v_mov_b32 v10, s64\n"
    "s_waitcnt lgkmcnt(7)\n"
    "v_mfma_f32_16x16x32_bf16 v[64:67], a[80:83], a[160:163], v[64:67]\n"
    "v_mov_b32 v11, s65\n"
    "s_add_u32 s67, s67, 1\n"
    "v_mfma_f32_16x16x32_bf16 v[64:67], a[84:87], a[164:167], v[64:67]\n"
    "v_mfma_f32_16x16x32_bf16 v[68:71], a[92:95], a[164:167], v[68:71]\n"
    "ds_read_b128 a[196:199], v9 offset:45056\n"
    "v_add_u32 v2, 128, v2\n"
    "v_mfma_f32_16x16x32_bf16 v[68:71], a[88:91], a[160:163], v[68:71]\n"
    "v_mfma_f32_16x16x32_bf16 v[72:75], a[96:99], a[160:163], v[72:75]\n"
    "v_mfma_f32_16x16x32_bf16 v[72:75], a[100:103], a[164:167], v[72:75]\n"
    "v_add_u32 v3, 128, v3\n"
    "v_mfma_f32_16x16x32_bf16 v[76:79], a[108:111], a[164:167], v[76:79]\n"
    "v_mfma_f32_16x16x32_bf16 v[76:79], a[104:107], a[160:163], v[76:79]\n"
    "v_mfma_f32_16x16x32_bf16 v[80:83], a[112:115], a[160:163], v[80:83]\n"
    "ds_read_b128 a[200:203], v8 offset:47104\n"
    "v_mfma_f32_16x16x32_bf16 v[80:83], a[116:119], a[164:167], v[80:83]\n"
    "v_add_u32 v4, s74, v4\n"
    "v_mfma_f32_16x16x32_bf16 v[84:87], a[124:127], a[164:167], v[84:87]\n"
    "v_mfma_f32_16x16x32_bf16 v[84:87], a[120:123], a[160:163], v[84:87]\n"
    "v_mfma_f32_16x16x32_bf16 v[88:91], a[128:131], a[160:163], v[88:91]\n"
    "v_mfma_f32_16x16x32_bf16 v[88:91], a[132:135], a[164:167], v[88:91]\n"
    "v_add_u32 v5, s74, v5\n"
    "v_mfma_f32_16x16x32_bf16 v[92:95], a[140:143], a[164:167], v[92:95]\n"
    "ds_read_b128 a[204:207], v9 offset:47104\n"
    "v_mfma_f32_16x16x32_bf16 v[92:95], a[136:139], a[160:163], v[92:95]\n"
    "s_waitcnt lgkmcnt(8)\n"
    "v_mfma_f32_16x16x32_bf16 v[96:99], a[80:83], a[168:171], v[96:99]\n"
    "v_add_u32 v0, s74, v0\n"
    "v_mfma_f32_16x16x32_bf16 v[96:99], a[84:87], a[172:175], v[96:99]\n"
    "v_mfma_f32_16x16x32_bf16 v[100:103], a[92:95], a[172:175], v[100:103]\n"
    "v_mfma_f32_16x16x32_bf16 v[100:103], a[88:91], a[168:171], v[100:103]\n"
    "v_mfma_f32_16x16x32_bf16 v[104:107], a[96:99], a[168:171], v[104:107]\n"
    "v_add_u32 v6, 0x80000, v6\n"
    "v_mfma_f32_16x16x32_bf16 v[104:107], a[100:103], a[172:175], v[104:107]\n"
    "v_mfma_f32_16x16x32_bf16 v[108:111], a[108:111], a[172:175], v[108:111]\n"
    "v_mfma_f32_16x16x32_bf16 v[108:111], a[104:107], a[168:171], v[108:111]\n"
    "v_mfma_f32_16x16x32_bf16 v[112:115], a[112:115], a[168:171], v[112:115]\n"
    "v_add_u32 v7, 0x80000, v7\n"
    "v_mfma_f32_16x16x32_bf16 v[112:115], a[116:119], a[172:175], v[112:115]\n"
    "v_mfma_f32_16x16x32_bf16 v[116:119], a[124:127], a[172:175], v[116:119]\n"
    "v_mfma_f32_16x16x32_bf16 v[116:119], a[120:123], a[168:171], v[116:119]\n"
    "v_mfma_f32_16x16x32_bf16 v[120:123], a[128:131], a[168:171], v[120:123]\n"
    "v_mfma_f32_16x16x32_bf16 v[120:123], a[132:135], a[172:175], v[120:123]\n"
    "v_mfma_f32_16x16x32_bf16 v[124:127], a[140:143], a[172:175], v[124:127]\n"
    "v_mfma_f32_16x16x32_bf16 v[124:127], a[136:139], a[168:171], v[124:127]\n"
    "s_waitcnt lgkmcnt(6)\n"
    "v_mfma_f32_16x16x32_bf16 v[128:131], a[80:83], a[176:179], v[128:131]\n"
    "v_mfma_f32_16x16x32_bf16 v[128:131], a[84:87], a[180:183], v[128:131]\n"
    "v_mfma_f32_16x16x32_bf16 v[132:135], a[92:95], a[180:183], v[132:135]\n"
    "s_waitcnt vmcnt(1) lgkmcnt(0)\n"
    "s_barrier\n"
    "v_mfma_f32_16x16x32_bf16 v[132:135], a[88:91], a[176:179], v[132:135]\n"
    "s_bitcmp1_b32 0x1054, s67\n"
    "s_cbranch_scc1 .Lk1_h1kr\n"
    ".Lk1_h1kr_ret:\n"
    "v_mfma_f32_16x16x32_bf16 v[136:139], a[96:99], a[176:179], v[136:139]\n"
    "ds_read_b128 a[144:147], v8 offset:0\n"
    "s_add_u32 m0, s70, 32768\n"
    "v_mfma_f32_16x16x32_bf16 v[136:139], a[100:103], a[180:183], v[136:139]\n"
    "ds_read_b128 a[148:151], v9 offset:0\n"
    "v_mfma_f32_16x16x32_bf16 v[140:143], a[108:111], a[180:183], v[140:143]\n"
    "ds_read_b64_tr_b16 a[16:17], v12 offset:0\n"
    "v_mfma_f32_16x16x32_bf16 v[140:143], a[104:107], a[176:179], v[140:143]\n"
    "ds_read_b64_tr_b16 a[18:19], v12 offset:2048\n"
    "v_mfma_f32_16x16x32_bf16 v[144:147], a[112:115], a[176:179], v[144:147]\n"
    "ds_read_b64_tr_b16 a[20:21], v12 offset:16384\n"
    "v_mfma_f32_16x16x32_bf16 v[144:147], a[116:119], a[180:183], v[144:147]\n"
    "buffer_load_dwordx4 v6, s[44:47], s88 offen offset:0 lds\n"
    "v_mfma_f32_16x16x32_bf16 v[148:151], a[124:127], a[180:183], v[148:151]\n"
    "ds_read_b64_tr_b16 a[22:23], v12 offset:18432\n"
    "v_mfma_f32_16x16x32_bf16 v[148:151], a[120:123], a[176:179], v[148:151]\n"
    "ds_read_b64_tr_b16 a[24:25], v13 offset:0\n"
    "v_mfma_f32_16x16x32_bf16 v[152:155], a[128:131], a[176:179], v[152:155]\n"
    "ds_read_b64_tr_b16 a[26:27], v13 offset:2048\n"
    "v_mfma_f32_16x16x32_bf16 v[152:155], a[132:135], a[180:183], v[152:155]\n"
    "buffer_load_dwordx4 v6, s[44:47], s89 offen offset:1024 lds\n"
    "v_mfma_f32_16x16x32_bf16 v[156:159], a[140:143], a[180:183], v[156:159]\n"
    "ds_read_b64_tr_b16 a[28:29], v13 offset:16384\n"
    "v_mfma_f32_16x16x32_bf16 v[156:159], a[136:139], a[176:179], v[156:159]\n"
    "ds_read_b64_tr_b16 a[30:31], v13 offset:18432\n"
    "v_mfma_f32_16x16x32_bf16 v[160:163], a[80:83], a[184:187], v[160:163]\n"
    "ds_read_b64_tr_b16 a[32:33], v12 offset:256\n"
    "v_mfma_f32_16x16x32_bf16 v[160:163], a[84:87], a[188:191], v[160:163]\n"
    "buffer_load_dwordx4 v6, s[44:47], s90 offen offset:2048 lds\n"
    "v_mfma_f32_16x16x32_bf16 v[164:167], a[92:95], a[188:191], v[164:167]\n"
    "ds_read_b64_tr_b16 a[34:35], v12 offset:2304\n"
    "v_mfma_f32_16x16x32_bf16 v[164:167], a[88:91], a[184:187], v[164:167]\n"
    "ds_read_b64_tr_b16 a[36:37], v12 offset:16640\n"
    "v_mfma_f32_16x16x32_bf16 v[168:171], a[96:99], a[184:187], v[168:171]\n"
    "ds_read_b64_tr_b16 a[38:39], v12 offset:18688\n"
    "v_mfma_f32_16x16x32_bf16 v[168:171], a[100:103], a[188:191], v[168:171]\n"
    "buffer_load_dwordx4 v6, s[44:47], s91 offen offset:3072 lds\n"
    "v_mfma_f32_16x16x32_bf16 v[172:175], a[108:111], a[188:191], v[172:175]\n"
    "ds_read_b64_tr_b16 a[40:41], v13 offset:256\n"
    "v_mfma_f32_16x16x32_bf16 v[172:175], a[104:107], a[184:187], v[172:175]\n"
    "ds_read_b64_tr_b16 a[42:43], v13 offset:2304\n"
    "v_mfma_f32_16x16x32_bf16 v[176:179], a[112:115], a[184:187], v[176:179]\n"
    "s_add_u32 m0, s70, 36864\n"
    "v_mfma_f32_16x16x32_bf16 v[176:179], a[116:119], a[188:191], v[176:179]\n"
    "ds_read_b64_tr_b16 a[44:45], v13 offset:16640\n"
    "v_mfma_f32_16x16x32_bf16 v[180:183], a[124:127], a[188:191], v[180:183]\n"
    "ds_read_b64_tr_b16 a[46:47], v13 offset:18688\n"
    "v_mfma_f32_16x16x32_bf16 v[180:183], a[120:123], a[184:187], v[180:183]\n"
    "ds_read_b64_tr_b16 a[48:49], v12 offset:512\n"
    "v_mfma_f32_16x16x32_bf16 v[184:187], a[128:131], a[184:187], v[184:187]\n"
    "buffer_load_dwordx4 v7, s[44:47], s92 offen offset:0 lds\n"
    "v_mfma_f32_16x16x32_bf16 v[184:187], a[132:135], a[188:191], v[184:187]\n"
    "ds_read_b64_tr_b16 a[50:51], v12 offset:2560\n"
    "v_mfma_f32_16x16x32_bf16 v[188:191], a[140:143], a[188:191], v[188:191]\n"
    "ds_read_b64_tr_b16 a[52:53], v12 offset:16896\n"
    "v_mfma_f32_16x16x32_bf16 v[188:191], a[136:139], a[184:187], v[188:191]\n"
    "ds_read_b64_tr_b16 a[54:55], v12 offset:18944\n"
    "v_mfma_f32_16x16x32_bf16 v[192:195], a[80:83], a[192:195], v[192:195]\n"
    "v_mfma_f32_16x16x32_bf16 v[192:195], a[84:87], a[196:199], v[192:195]\n"
    "ds_read_b64_tr_b16 a[56:57], v13 offset:512\n"
    "v_mfma_f32_16x16x32_bf16 v[196:199], a[92:95], a[196:199], v[196:199]\n"
    "buffer_load_dwordx4 v7, s[44:47], s93 offen offset:1024 lds\n"
    "v_mfma_f32_16x16x32_bf16 v[196:199], a[88:91], a[192:195], v[196:199]\n"
    "ds_read_b64_tr_b16 a[58:59], v13 offset:2560\n"
    "v_mfma_f32_16x16x32_bf16 v[200:203], a[96:99], a[192:195], v[200:203]\n"
    "ds_read_b64_tr_b16 a[60:61], v13 offset:16896\n"
    "s_sub_u32 s96, s96, 1\n"
    "v_mfma_f32_16x16x32_bf16 v[200:203], a[100:103], a[196:199], v[200:203]\n"
    "ds_read_b64_tr_b16 a[62:63], v13 offset:18944\n"
    "v_mfma_f32_16x16x32_bf16 v[204:207], a[108:111], a[196:199], v[204:207]\n"
    "buffer_load_dwordx4 v7, s[44:47], s94 offen offset:2048 lds\n"
    "v_mfma_f32_16x16x32_bf16 v[204:207], a[104:107], a[192:195], v[204:207]\n"
    "ds_read_b64_tr_b16 a[64:65], v12 offset:768\n"
    "v_mfma_f32_16x16x32_bf16 v[208:211], a[112:115], a[192:195], v[208:211]\n"
    "ds_read_b64_tr_b16 a[66:67], v12 offset:2816\n"
    "v_mfma_f32_16x16x32_bf16 v[208:211], a[116:119], a[196:199], v[208:211]\n"
    "ds_read_b64_tr_b16 a[68:69], v12 offset:17152\n"
    "v_mfma_f32_16x16x32_bf16 v[212:215], a[124:127], a[196:199], v[212:215]\n"
    "buffer_load_dwordx4 v7, s[44:47], s95 offen offset:3072 lds\n"
    "v_mfma_f32_16x16x32_bf16 v[212:215], a[120:123], a[192:195], v[212:215]\n"
    "ds_read_b64_tr_b16 a[70:71], v12 offset:19200\n"
    "v_mfma_f32_16x16x32_bf16 v[216:219], a[128:131], a[192:195], v[216:219]\n"
    "ds_read_b64_tr_b16 a[72:73], v13 offset:768\n"
    "v_mfma_f32_16x16x32_bf16 v[216:219], a[132:135], a[196:199], v[216:219]\n"
    "ds_read_b64_tr_b16 a[74:75], v13 offset:2816\n"
    "s_add_u32 m0, s68, 32768\n"
    "v_mfma_f32_16x16x32_bf16 v[220:223], a[140:143], a[196:199], v[220:223]\n"
    "v_mfma_f32_16x16x32_bf16 v[220:223], a[136:139], a[192:195], v[220:223]\n"
    "ds_read_b64_tr_b16 a[76:77], v13 offset:17152\n"
    "v_mfma_f32_16x16x32_bf16 v[224:227], a[80:83], a[200:203], v[224:227]\n"
    "ds_read_b64_tr_b16 a[78:79], v13 offset:19200\n"
    "v_mfma_f32_16x16x32_bf16 v[224:227], a[84:87], a[204:207], v[224:227]\n"
    "ds_read_b128 a[152:155], v8 offset:2048\n"
    "v_mfma_f32_16x16x32_bf16 v[228:231], a[92:95], a[204:207], v[228:231]\n"
    "buffer_load_dwordx4 v2, s[36:39], s80 offen offset:0 lds\n"
    "v_mfma_f32_16x16x32_bf16 v[228:231], a[88:91], a[200:203], v[228:231]\n"
    "ds_read_b128 a[156:159], v9 offset:2048\n"
    "v_mfma_f32_16x16x32_bf16 v[232:235], a[96:99], a[200:203], v[232:235]\n"
    "ds_read_b128 a[160:163], v8 offset:4096\n"
    "v_mfma_f32_16x16x32_bf16 v[232:235], a[100:103], a[204:207], v[232:235]\n"
    "ds_read_b128 a[164:167], v9 offset:4096\n"
    "v_mfma_f32_16x16x32_bf16 v[236:239], a[108:111], a[204:207], v[236:239]\n"
    "buffer_load_dwordx4 v3, s[36:39], s81 offen offset:1024 lds\n"
    "v_mfma_f32_16x16x32_bf16 v[236:239], a[104:107], a[200:203], v[236:239]\n"
    "ds_read_b128 a[168:171], v8 offset:6144\n"
    "v_mfma_f32_16x16x32_bf16 v[240:243], a[112:115], a[200:203], v[240:243]\n"
    "ds_read_b128 a[172:175], v9 offset:6144\n"
    "v_mfma_f32_16x16x32_bf16 v[240:243], a[116:119], a[204:207], v[240:243]\n"
    "buffer_load_dwordx4 v2, s[36:39], s82 offen offset:2048 lds\n"
    "v_mfma_f32_16x16x32_bf16 v[244:247], a[124:127], a[204:207], v[244:247]\n"
    "v_mfma_f32_16x16x32_bf16 v[244:247], a[120:123], a[200:203], v[244:247]\n"
    "v_mfma_f32_16x16x32_bf16 v[248:251], a[128:131], a[200:203], v[248:251]\n"
    "v_mfma_f32_16x16x32_bf16 v[248:251], a[132:135], a[204:207], v[248:251]\n"
    "v_mfma_f32_16x16x32_bf16 v[252:255], a[140:143], a[204:207], v[252:255]\n"
    "v_mfma_f32_16x16x32_bf16 v[252:255], a[136:139], a[200:203], v[252:255]\n"
    "s_branch .Lk1_loop_k\n"
    ".Lk1_swap_k:\n"
    "s_call_b64 s[18:19], .Lk1_swap\n"
    "s_branch .Lk1_swap_k_ret\n"
    ".Lk1_h0k:\n"
    "v_accvgpr_read_b32 v10, a224\n"
    "v_accvgpr_read_b32 v11, a225\n"
    "v_accvgpr_mov_b32 a224, a226\n"
    "v_accvgpr_mov_b32 a225, a227\n"
    "v_accvgpr_mov_b32 a226, a228\n"
    "v_accvgpr_mov_b32 a227, a229\n"
    "v_accvgpr_mov_b32 a228, a230\n"
    "v_accvgpr_mov_b32 a229, a231\n"
    "s_branch .Lk1_h0k_ret\n"
    ".Lk1_h1k:\n"
    "v_accvgpr_read_b32 v10, a224\n"
    "v_accvgpr_read_b32 v11, a225\n"
    "v_accvgpr_mov_b32 a224, a226\n"
    "v_accvgpr_mov_b32 a225, a227\n"
    "v_accvgpr_mov_b32 a226, a228\n"
    "v_accvgpr_mov_b32 a227, a229\n"
    "v_accvgpr_mov_b32 a228, a230\n"
    "v_accvgpr_mov_b32 a229, a231\n"
    "s_branch .Lk1_h1k_ret\n"
    ".Lk1_h1kr:\n"
    "s_setpc_b64 s[34:35]\n"
    ".Lk1_rp0:\n"
    "v_accvgpr_read_b32 v10, a232\n"
    "v_accvgpr_read_b32 v11, a233\n"
    "buffer_load_dwordx4 a[208:211], v10, s[56:59], 0 offen\n"
    "buffer_load_dwordx4 a[212:215], v11, s[56:59], 0 offen\n"
    "s_add_u32 s34, s34, .Lk1_rp1-.Lk1_rp0\n"
    "s_addc_u32 s35, s35, 0\n"
    "v_mov_b32 v10, s64\n"
    "v_mov_b32 v11, s65\n"
    "s_setpc_b64 s[18:19]\n"
    ".Lk1_rp1:\n"
    "v_accvgpr_read_b32 v10, a234\n"
    "v_accvgpr_read_b32 v11, a235\n"
    "buffer_store_dwordx4 a[208:211], v10, s[48:51], 0 offen sc1\n"
    "buffer_store_dwordx4 a[212:215], v11, s[48:51], 0 offen sc1\n"
    "s_add_u32 s34, s34, .Lk1_rp2-.Lk1_rp1\n"
    "s_addc_u32 s35, s35, 0\n"
    "v_mov_b32 v10, s64\n"
    "v_mov_b32 v11, s65\n"
    "s_setpc_b64 s[18:19]\n"
    ".Lk1_rp2:\n"
    "v_mov_b32 v10, s16\n"
    "v_mov_b32 v11, s17\n"
    "v_accvgpr_write_b32 a208, v10\n"
    "v_accvgpr_write_b32 a209, v11\n"
    "v_accvgpr_read_b32 v10, a236\n"
    "buffer_store_dwordx2 a[208:209], v10, s[48:51], 0 offen sc1\n"
    "s_add_u32 s34, s34, .Lk1_rp3-.Lk1_rp2\n"
    "s_addc_u32 s35, s35, 0\n"
    "v_mov_b32 v10, s64\n"
    "v_mov_b32 v11, s65\n"
    "s_setpc_b64 s[18:19]\n"
    ".Lk1_rp3:\n"
    "buffer_load_dwordx2 a[208:209], v15, s[48:51], 0 offen sc1\n"
    "s_setpc_b64 s[18:19]\n"
    ".Lk1_swap:\n"
    "s_mov_b32 s97, 0\n"
    ".Lk1_sw0:\n"
    "v_accvgpr_read_b32 v10, a208\n"
    "v_accvgpr_read_b32 v11, a209\n"
    "v_cmp_eq_u64 vcc, s[16:17], v[10:11]\n"
    "s_nop 1\n"
    "s_cmp_eq_u64 vcc, -1\n"
    "s_cbranch_scc1 .Lk1_sw1\n"
    "s_add_u32 s97, s97, 1\n"
    "s_cmp_gt_u32 s97, 0x40000\n"
    "s_cbranch_scc1 .Lk1_sw1\n"
    "s_sleep 4\n"
    "buffer_load_dwordx2 a[208:209], v15, s[48:51], 0 offen sc1\n"
    "s_waitcnt vmcnt(0)\n"
    "s_branch .Lk1_sw0\n"
    ".Lk1_sw1:\n"
    "s_mov_b64 s[36:37], s[52:53]\n"
    "s_mov_b64 s[40:41], s[48:49]\n"
    "s_mov_b64 s[44:45], s[60:61]\n"
    "s_mov_b64 s[38:39], s[54:55]\n"
    "s_mov_b64 s[42:43], s[50:51]\n"
    "s_mov_b64 s[46:47], s[62:63]\n"
    "s_sub_u32 s76, s64, 3072\n"
    "s_subb_u32 s77, s65, 0\n"
    "s_and_b32 s77, s77, 0xffff\n"
    "s_mov_b32 s78, 3968\n"
    "s_mov_b32 s68, s72\n"
    "s_mov_b32 s69, s73\n"
    "s_mov_b32 s74, 12288\n"
    "s_mul_i32 s80, s75, 14336\n"
    "s_add_u32 s81, s80, 6144\n"
    "s_sub_u32 s82, 98304, s80\n"
    "s_add_u32 s83, s82, 6144\n"
    "s_mov_b32 s84, 0\n"
    "s_mov_b32 s85, 0\n"
    "s_mov_b32 s86, 0\n"
    "s_mov_b32 s87, 0\n"
    "v_accvgpr_read_b32 v2, a240\n"
    "v_accvgpr_read_b32 v3, a241\n"
    "v_accvgpr_read_b32 v4, a242\n"
    "v_accvgpr_read_b32 v5, a242\n"
    "v_mov_b32 v0, v1\n"
    "v_accvgpr_read_b32 v6, a250\n"
    "v_xor_b32 v7, 32, v6\n"
    "s_setpc_b64 s[18:19]\n"
    ".p2align 5\n"
    "s_nop 0\n"
    "s_nop 0\n"
    ".Lk1_loop_v:\n"
    "s_waitcnt lgkmcnt(15)\n"
    "v_mfma_f32_16x16x32_bf16 a[0:3], a[144:147], a[16:19], a[0:3]\n"
    "v_mfma_f32_16x16x32_bf16 a[0:3], a[148:151], a[20:23], a[0:3]\n"
    "buffer_load_dwordx4 v3, s[36:39], s83 offen offset:3072 lds\n"
    "v_mfma_f32_16x16x32_bf16 a[8:11], a[148:151], a[28:31], a[8:11]\n"
    "v_mfma_f32_16x16x32_bf16 a[8:11], a[144:147], a[24:27], a[8:11]\n"
    "v_mfma_f32_16x16x32_bf16 v[16:19], a[144:147], a[32:35], v[16:19]\n"
    "ds_read_b128 a[176:179], v8 offset:8192\n"
    "v_mfma_f32_16x16x32_bf16 v[16:19], a[148:151], a[36:39], v[16:19]\n"
    "s_add_u32 m0, s69, 32768\n"
    "v_mfma_f32_16x16x32_bf16 v[24:27], a[148:151], a[44:47], v[24:27]\n"
    "v_mfma_f32_16x16x32_bf16 v[24:27], a[144:147], a[40:43], v[24:27]\n"
    "v_mfma_f32_16x16x32_bf16 v[32:35], a[144:147], a[48:51], v[32:35]\n"
    "v_mfma_f32_16x16x32_bf16 v[32:35], a[148:151], a[52:55], v[32:35]\n"
    "s_waitcnt lgkmcnt(11)\n"
    "v_mfma_f32_16x16x32_bf16 v[40:43], a[148:151], a[60:63], v[40:43]\n"
    "ds_read_b128 a[180:183], v9 offset:8192\n"
    "v_mfma_f32_16x16x32_bf16 v[40:43], a[144:147], a[56:59], v[40:43]\n"
    "v_mfma_f32_16x16x32_bf16 v[48:51], a[144:147], a[64:67], v[48:51]\n"
    "buffer_load_dwordx4 v4, s[40:43], s84 offen offset:0 sc1 lds\n"
    "v_mfma_f32_16x16x32_bf16 v[48:51], a[148:151], a[68:71], v[48:51]\n"
    "s_waitcnt lgkmcnt(6)\n"
    "v_mfma_f32_16x16x32_bf16 v[56:59], a[148:151], a[76:79], v[56:59]\n"
    "buffer_load_dwordx4 v5, s[40:43], s85 offen offset:1024 sc1 lds\n"
    "v_mfma_f32_16x16x32_bf16 v[56:59], a[144:147], a[72:75], v[56:59]\n"
    "v_mfma_f32_16x16x32_bf16 a[4:7], a[152:155], a[16:19], a[4:7]\n"
    "ds_read_b128 a[184:187], v8 offset:10240\n"
    "v_mfma_f32_16x16x32_bf16 a[4:7], a[156:159], a[20:23], a[4:7]\n"
    "v_mfma_f32_16x16x32_bf16 a[12:15], a[156:159], a[28:31], a[12:15]\n"
    "v_mfma_f32_16x16x32_bf16 a[12:15], a[152:155], a[24:27], a[12:15]\n"
    "buffer_load_dwordx4 v4, s[40:43], s86 offen offset:2048 sc1 lds\n"
    "v_mfma_f32_16x16x32_bf16 v[20:23], a[152:155], a[32:35], v[20:23]\n"
    "v_mfma_f32_16x16x32_bf16 v[20:23], a[156:159], a[36:39], v[20:23]\n"
    "v_mfma_f32_16x16x32_bf16 v[28:31], a[156:159], a[44:47], v[28:31]\n"
    "ds_read_b128 a[188:191], v9 offset:10240\n"
    "v_mfma_f32_16x16x32_bf16 v[28:31], a[152:155], a[40:43], v[28:31]\n"
    "buffer_load_dwordx4 v0, s[76:79], s87 offen offset:3072 lds\n"
    "v_mfma_f32_16x16x32_bf16 v[36:39], a[152:155], a[48:51], v[36:39]\n"
    "v_mfma_f32_16x16x32_bf16 v[36:39], a[156:159], a[52:55], v[36:39]\n"
    "v_mfma_f32_16x16x32_bf16 v[44:47], a[156:159], a[60:63], v[44:47]\n"
    "v_mfma_f32_16x16x32_bf16 v[44:47], a[152:155], a[56:59], v[44:47]\n"
    "v_mfma_f32_16x16x32_bf16 v[52:55], a[152:155], a[64:67], v[52:55]\n"
    "ds_read_b128 a[192:195], v8 offset:12288\n"
    "v_mfma_f32_16x16x32_bf16 v[52:55], a[156:159], a[68:71], v[52:55]\n"
    "s_bitcmp1_b32 s66, s67\n"
    "s_cbranch_scc1 .Lk1_h0v\n"
    ".Lk1_h0v_ret:\n"
    "v_mfma_f32_16x16x32_bf16 v[60:63], a[156:159], a[76:79], v[60:63]\n"
    "global_load_dword a222, v[10:11], off\n"
    "v_mfma_f32_16x16x32_bf16 v[60:63], a[152:155], a[72:75], v[60:63]\n"
    "v_mov_b32 v10, s64\n"
    "s_waitcnt lgkmcnt(7)\n"
    "v_mfma_f32_16x16x32_bf16 v[64:67], a[160:163], a[16:19], v[64:67]\n"
    "v_mov_b32 v11, s65\n"
    "s_add_u32 s67, s67, 1\n"
    "v_mfma_f32_16x16x32_bf16 v[64:67], a[164:167], a[20:23], v[64:67]\n"
    "v_mfma_f32_16x16x32_bf16 v[72:75], a[164:167], a[28:31], v[72:75]\n"
    "ds_read_b128 a[196:199], v9 offset:12288\n"
    "v_add_u32 v2, 128, v2\n"
    "v_mfma_f32_16x16x32_bf16 v[72:75], a[160:163], a[24:27], v[72:75]\n"
    "v_mfma_f32_16x16x32_bf16 v[80:83], a[160:163], a[32:35], v[80:83]\n"
    "v_mfma_f32_16x16x32_bf16 v[80:83], a[164:167], a[36:39], v[80:83]\n"
    "v_add_u32 v3, 128, v3\n"
    "v_mfma_f32_16x16x32_bf16 v[88:91], a[164:167], a[44:47], v[88:91]\n"
    "v_mfma_f32_16x16x32_bf16 v[88:91], a[160:163], a[40:43], v[88:91]\n"
    "v_mfma_f32_16x16x32_bf16 v[96:99], a[160:163], a[48:51], v[96:99]\n"
    "ds_read_b128 a[200:203], v8 offset:14336\n"
    "v_mfma_f32_16x16x32_bf16 v[96:99], a[164:167], a[52:55], v[96:99]\n"
    "v_add_u32 v4, s74, v4\n"
    "v_mfma_f32_16x16x32_bf16 v[104:107], a[164:167], a[60:63], v[104:107]\n"
    "v_mfma_f32_16x16x32_bf16 v[104:107], a[160:163], a[56:59], v[104:107]\n"
    "v_mfma_f32_16x16x32_bf16 v[112:115], a[160:163], a[64:67], v[112:115]\n"
    "v_mfma_f32_16x16x32_bf16 v[112:115], a[164:167], a[68:71], v[112:115]\n"
    "v_add_u32 v5, s74, v5\n"
    "v_mfma_f32_16x16x32_bf16 v[120:123], a[164:167], a[76:79], v[120:123]\n"
    "ds_read_b128 a[204:207], v9 offset:14336\n"
    "v_mfma_f32_16x16x32_bf16 v[120:123], a[160:163], a[72:75], v[120:123]\n"
    "s_waitcnt lgkmcnt(8)\n"
    "v_mfma_f32_16x16x32_bf16 v[68:71], a[168:171], a[16:19], v[68:71]\n"
    "v_add_u32 v0, s74, v0\n"
    "v_mfma_f32_16x16x32_bf16 v[68:71], a[172:175], a[20:23], v[68:71]\n"
    "v_mfma_f32_16x16x32_bf16 v[76:79], a[172:175], a[28:31], v[76:79]\n"
    "v_mfma_f32_16x16x32_bf16 v[76:79], a[168:171], a[24:27], v[76:79]\n"
    "v_mfma_f32_16x16x32_bf16 v[84:87], a[168:171], a[32:35], v[84:87]\n"
    "v_add_u32 v6, 0x80000, v6\n"
    "v_mfma_f32_16x16x32_bf16 v[84:87], a[172:175], a[36:39], v[84:87]\n"
    "v_mfma_f32_16x16x32_bf16 v[92:95], a[172:175], a[44:47], v[92:95]\n"
    "v_mfma_f32_16x16x32_bf16 v[92:95], a[168:171], a[40:43], v[92:95]\n"
    "v_mfma_f32_16x16x32_bf16 v[100:103], a[168:171], a[48:51], v[100:103]\n"
    "v_add_u32 v7, 0x80000, v7\n"
    "v_mfma_f32_16x16x32_bf16 v[100:103], a[172:175], a[52:55], v[100:103]\n"
    "v_mfma_f32_16x16x32_bf16 v[108:111], a[172:175], a[60:63], v[108:111]\n"
    "v_mfma_f32_16x16x32_bf16 v[108:111], a[168:171], a[56:59], v[108:111]\n"
    "v_mfma_f32_16x16x32_bf16 v[116:119], a[168:171], a[64:67], v[116:119]\n"
    "v_mfma_f32_16x16x32_bf16 v[116:119], a[172:175], a[68:71], v[116:119]\n"
    "v_mfma_f32_16x16x32_bf16 v[124:127], a[172:175], a[76:79], v[124:127]\n"
    "v_mfma_f32_16x16x32_bf16 v[124:127], a[168:171], a[72:75], v[124:127]\n"
    "s_waitcnt lgkmcnt(6)\n"
    "v_mfma_f32_16x16x32_bf16 v[128:131], a[176:179], a[16:19], v[128:131]\n"
    "v_mfma_f32_16x16x32_bf16 v[128:131], a[180:183], a[20:23], v[128:131]\n"
    "s_cmp_eq_u32 s96, 1\n"
    "s_cbranch_scc1 .Lk1_swap_v\n"
    ".Lk1_swap_v_ret:\n"
    "v_mfma_f32_16x16x32_bf16 v[136:139], a[180:183], a[28:31], v[136:139]\n"
    "s_waitcnt vmcnt(1) lgkmcnt(0)\n"
    "s_barrier\n"
    "v_mfma_f32_16x16x32_bf16 v[136:139], a[176:179], a[24:27], v[136:139]\n"
    "ds_read_b128 a[144:147], v8 offset:32768\n"
    "v_mfma_f32_16x16x32_bf16 v[144:147], a[176:179], a[32:35], v[144:147]\n"
    "ds_read_b128 a[148:151], v9 offset:32768\n"
    "s_add_u32 m0, s70, 0\n"
    "v_mfma_f32_16x16x32_bf16 v[144:147], a[180:183], a[36:39], v[144:147]\n"
    "ds_read_b64_tr_b16 a[80:81], v12 offset:32768\n"
    "v_mfma_f32_16x16x32_bf16 v[152:155], a[180:183], a[44:47], v[152:155]\n"
    "v_mfma_f32_16x16x32_bf16 v[152:155], a[176:179], a[40:43], v[152:155]\n"
    "ds_read_b64_tr_b16 a[82:83], v12 offset:34816\n"
    "v_mfma_f32_16x16x32_bf16 v[160:163], a[176:179], a[48:51], v[160:163]\n"
    "ds_read_b64_tr_b16 a[84:85], v12 offset:49152\n"
    "v_mfma_f32_16x16x32_bf16 v[160:163], a[180:183], a[52:55], v[160:163]\n"
    "buffer_load_dwordx4 v6, s[44:47], s88 offen offset:0 lds\n"
    "v_mfma_f32_16x16x32_bf16 v[168:171], a[180:183], a[60:63], v[168:171]\n"
    "ds_read_b64_tr_b16 a[86:87], v12 offset:51200\n"
    "v_mfma_f32_16x16x32_bf16 v[168:171], a[176:179], a[56:59], v[168:171]\n"
    "ds_read_b64_tr_b16 a[88:89], v13 offset:32768\n"
    "v_mfma_f32_16x16x32_bf16 v[176:179], a[176:179], a[64:67], v[176:179]\n"
    "ds_read_b64_tr_b16 a[90:91], v13 offset:34816\n"
    "v_mfma_f32_16x16x32_bf16 v[176:179], a[180:183], a[68:71], v[176:179]\n"
    "buffer_load_dwordx4 v6, s[44:47], s89 offen offset:1024 lds\n"
    "v_mfma_f32_16x16x32_bf16 v[184:187], a[180:183], a[76:79], v[184:187]\n"
    "ds_read_b64_tr_b16 a[92:93], v13 offset:49152\n"
    "v_mfma_f32_16x16x32_bf16 v[184:187], a[176:179], a[72:75], v[184:187]\n"
    "ds_read_b64_tr_b16 a[94:95], v13 offset:51200\n"
    "v_mfma_f32_16x16x32_bf16 v[132:135], a[184:187], a[16:19], v[132:135]\n"
    "ds_read_b64_tr_b16 a[96:97], v12 offset:33024\n"
    "v_mfma_f32_16x16x32_bf16 v[132:135], a[188:191], a[20:23], v[132:135]\n"
    "buffer_load_dwordx4 v6, s[44:47], s90 offen offset:2048 lds\n"
    "v_mfma_f32_16x16x32_bf16 v[140:143], a[188:191], a[28:31], v[140:143]\n"
    "ds_read_b64_tr_b16 a[98:99], v12 offset:35072\n"
    "v_mfma_f32_16x16x32_bf16 v[140:143], a[184:187], a[24:27], v[140:143]\n"
    "ds_read_b64_tr_b16 a[100:101], v12 offset:49408\n"
    "v_mfma_f32_16x16x32_bf16 v[148:151], a[184:187], a[32:35], v[148:151]\n"
    "ds_read_b64_tr_b16 a[102:103], v12 offset:51456\n"
    "v_mfma_f32_16x16x32_bf16 v[148:151], a[188:191], a[36:39], v[148:151]\n"
    "buffer_load_dwordx4 v6, s[44:47], s91 offen offset:3072 lds\n"
    "v_mfma_f32_16x16x32_bf16 v[156:159], a[188:191], a[44:47], v[156:159]\n"
    "ds_read_b64_tr_b16 a[104:105], v13 offset:33024\n"
    "v_mfma_f32_16x16x32_bf16 v[156:159], a[184:187], a[40:43], v[156:159]\n"
    "ds_read_b64_tr_b16 a[106:107], v13 offset:35072\n"
    "v_mfma_f32_16x16x32_bf16 v[164:167], a[184:187], a[48:51], v[164:167]\n"
    "s_add_u32 m0, s70, 4096\n"
    "v_mfma_f32_16x16x32_bf16 v[164:167], a[188:191], a[52:55], v[164:167]\n"
    "ds_read_b64_tr_b16 a[108:109], v13 offset:49408\n"
    "v_mfma_f32_16x16x32_bf16 v[172:175], a[188:191], a[60:63], v[172:175]\n"
    "ds_read_b64_tr_b16 a[110:111], v13 offset:51456\n"
    "v_mfma_f32_16x16x32_bf16 v[172:175], a[184:187], a[56:59], v[172:175]\n"
    "ds_read_b64_tr_b16 a[112:113], v12 offset:33280\n"
    "v_mfma_f32_16x16x32_bf16 v[180:183], a[184:187], a[64:67], v[180:183]\n"
    "buffer_load_dwordx4 v7, s[44:47], s92 offen offset:0 lds\n"
    "v_mfma_f32_16x16x32_bf16 v[180:183], a[188:191], a[68:71], v[180:183]\n"
    "ds_read_b64_tr_b16 a[114:115], v12 offset:35328\n"
    "v_mfma_f32_16x16x32_bf16 v[188:191], a[188:191], a[76:79], v[188:191]\n"
    "ds_read_b64_tr_b16 a[116:117], v12 offset:49664\n"
    "v_mfma_f32_16x16x32_bf16 v[188:191], a[184:187], a[72:75], v[188:191]\n"
    "ds_read_b64_tr_b16 a[118:119], v12 offset:51712\n"
    "v_mfma_f32_16x16x32_bf16 v[192:195], a[192:195], a[16:19], v[192:195]\n"
    "v_mfma_f32_16x16x32_bf16 v[192:195], a[196:199], a[20:23], v[192:195]\n"
    "ds_read_b64_tr_b16 a[120:121], v13 offset:33280\n"
    "v_mfma_f32_16x16x32_bf16 v[200:203], a[196:199], a[28:31], v[200:203]\n"
    "buffer_load_dwordx4 v7, s[44:47], s93 offen offset:1024 lds\n"
    "v_mfma_f32_16x16x32_bf16 v[200:203], a[192:195], a[24:27], v[200:203]\n"
    "ds_read_b64_tr_b16 a[122:123], v13 offset:35328\n"
    "v_mfma_f32_16x16x32_bf16 v[208:211], a[192:195], a[32:35], v[208:211]\n"
    "ds_read_b64_tr_b16 a[124:125], v13 offset:49664\n"
    "v_mfma_f32_16x16x32_bf16 v[208:211], a[196:199], a[36:39], v[208:211]\n"
    "ds_read_b64_tr_b16 a[126:127], v13 offset:51712\n"
    "v_mfma_f32_16x16x32_bf16 v[216:219], a[196:199], a[44:47], v[216:219]\n"
    "buffer_load_dwordx4 v7, s[44:47], s94 offen offset:2048 lds\n"
    "v_mfma_f32_16x16x32_bf16 v[216:219], a[192:195], a[40:43], v[216:219]\n"
    "ds_read_b64_tr_b16 a[128:129], v12 offset:33536\n"
    "v_mfma_f32_16x16x32_bf16 v[224:227], a[192:195], a[48:51], v[224:227]\n"
    "ds_read_b64_tr_b16 a[130:131], v12 offset:35584\n"
    "v_mfma_f32_16x16x32_bf16 v[224:227], a[196:199], a[52:55], v[224:227]\n"
    "ds_read_b64_tr_b16 a[132:133], v12 offset:49920\n"
    "v_mfma_f32_16x16x32_bf16 v[232:235], a[196:199], a[60:63], v[232:235]\n"
    "buffer_load_dwordx4 v7, s[44:47], s95 offen offset:3072 lds\n"
    "v_mfma_f32_16x16x32_bf16 v[232:235], a[192:195], a[56:59], v[232:235]\n"
    "ds_read_b64_tr_b16 a[134:135], v12 offset:51968\n"
    "v_mfma_f32_16x16x32_bf16 v[240:243], a[192:195], a[64:67], v[240:243]\n"
    "ds_read_b64_tr_b16 a[136:137], v13 offset:33536\n"
    "v_mfma_f32_16x16x32_bf16 v[240:243], a[196:199], a[68:71], v[240:243]\n"
    "ds_read_b64_tr_b16 a[138:139], v13 offset:35584\n"
    "s_add_u32 m0, s68, 0\n"
    "v_mfma_f32_16x16x32_bf16 v[248:251], a[196:199], a[76:79], v[248:251]\n"
    "v_mfma_f32_16x16x32_bf16 v[248:251], a[192:195], a[72:75], v[248:251]\n"
    "ds_read_b64_tr_b16 a[140:141], v13 offset:49920\n"
    "v_mfma_f32_16x16x32_bf16 v[196:199], a[200:203], a[16:19], v[196:199]\n"
    "ds_read_b64_tr_b16 a[142:143], v13 offset:51968\n"
    "v_mfma_f32_16x16x32_bf16 v[196:199], a[204:207], a[20:23], v[196:199]\n"
    "ds_read_b128 a[152:155], v8 offset:34816\n"
    "v_mfma_f32_16x16x32_bf16 v[204:207], a[204:207], a[28:31], v[204:207]\n"
    "buffer_load_dwordx4 v2, s[36:39], s80 offen offset:0 lds\n"
    "v_mfma_f32_16x16x32_bf16 v[204:207], a[200:203], a[24:27], v[204:207]\n"
    "ds_read_b128 a[156:159], v9 offset:34816\n"
    "v_mfma_f32_16x16x32_bf16 v[212:215], a[200:203], a[32:35], v[212:215]\n"
    "ds_read_b128 a[160:163], v8 offset:36864\n"
    "v_mfma_f32_16x16x32_bf16 v[212:215], a[204:207], a[36:39], v[212:215]\n"
    "ds_read_b128 a[164:167], v9 offset:36864\n"
    "v_mfma_f32_16x16x32_bf16 v[220:223], a[204:207], a[44:47], v[220:223]\n"
    "buffer_load_dwordx4 v3, s[36:39], s81 offen offset:1024 lds\n"
    "v_mfma_f32_16x16x32_bf16 v[220:223], a[200:203], a[40:43], v[220:223]\n"
    "ds_read_b128 a[168:171], v8 offset:38912\n"
    "v_mfma_f32_16x16x32_bf16 v[228:231], a[200:203], a[48:51], v[228:231]\n"
    "ds_read_b128 a[172:175], v9 offset:38912\n"
    "v_mfma_f32_16x16x32_bf16 v[228:231], a[204:207], a[52:55], v[228:231]\n"
    "buffer_load_dwordx4 v2, s[36:39], s82 offen offset:2048 lds\n"
    "v_mfma_f32_16x16x32_bf16 v[236:239], a[204:207], a[60:63], v[236:239]\n"
    "v_mfma_f32_16x16x32_bf16 v[236:239], a[200:203], a[56:59], v[236:239]\n"
    "v_mfma_f32_16x16x32_bf16 v[244:247], a[200:203], a[64:67], v[244:247]\n"
    "v_mfma_f32_16x16x32_bf16 v[244:247], a[204:207], a[68:71], v[244:247]\n"
    "v_mfma_f32_16x16x32_bf16 v[252:255], a[204:207], a[76:79], v[252:255]\n"
    "v_mfma_f32_16x16x32_bf16 v[252:255], a[200:203], a[72:75], v[252:255]\n"
    "s_cmp_eq_u32 s96, 1\n"
    "s_cbranch_scc1 .Lk1_last_v\n"
    "s_waitcnt lgkmcnt(15)\n"
    "v_mfma_f32_16x16x32_bf16 a[0:3], a[144:147], a[80:83], a[0:3]\n"
    "v_mfma_f32_16x16x32_bf16 a[0:3], a[148:151], a[84:87], a[0:3]\n"
    "buffer_load_dwordx4 v3, s[36:39], s83 offen offset:3072 lds\n"
    "v_mfma_f32_16x16x32_bf16 a[8:11], a[148:151], a[92:95], a[8:11]\n"
    "v_mfma_f32_16x16x32_bf16 a[8:11], a[144:147], a[88:91], a[8:11]\n"
    "v_mfma_f32_16x16x32_bf16 v[16:19], a[144:147], a[96:99], v[16:19]\n"
    "ds_read_b128 a[176:179], v8 offset:40960\n"
    "v_mfma_f32_16x16x32_bf16 v[16:19], a[148:151], a[100:103], v[16:19]\n"
    "s_add_u32 m0, s69, 0\n"
    "v_mfma_f32_16x16x32_bf16 v[24:27], a[148:151], a[108:111], v[24:27]\n"
    "v_mfma_f32_16x16x32_bf16 v[24:27], a[144:147], a[104:107], v[24:27]\n"
    "v_mfma_f32_16x16x32_bf16 v[32:35], a[144:147], a[112:115], v[32:35]\n"
    "v_mfma_f32_16x16x32_bf16 v[32:35], a[148:151], a[116:119], v[32:35]\n"
    "s_waitcnt lgkmcnt(11)\n"
    "v_mfma_f32_16x16x32_bf16 v[40:43], a[148:151], a[124:127], v[40:43]\n"
    "ds_read_b128 a[180:183], v9 offset:40960\n"
    "v_mfma_f32_16x16x32_bf16 v[40:43], a[144:147], a[120:123], v[40:43]\n"
    "v_mfma_f32_16x16x32_bf16 v[48:51], a[144:147], a[128:131], v[48:51]\n"
    "buffer_load_dwordx4 v4, s[40:43], s84 offen offset:0 sc1 lds\n"
    "v_mfma_f32_16x16x32_bf16 v[48:51], a[148:151], a[132:135], v[48:51]\n"
    "s_waitcnt lgkmcnt(6)\n"
    "v_mfma_f32_16x16x32_bf16 v[56:59], a[148:151], a[140:143], v[56:59]\n"
    "buffer_load_dwordx4 v5, s[40:43], s85 offen offset:1024 sc1 lds\n"
    "v_mfma_f32_16x16x32_bf16 v[56:59], a[144:147], a[136:139], v[56:59]\n"
    "v_mfma_f32_16x16x32_bf16 a[4:7], a[152:155], a[80:83], a[4:7]\n"
    "ds_read_b128 a[184:187], v8 offset:43008\n"
    "v_mfma_f32_16x16x32_bf16 a[4:7], a[156:159], a[84:87], a[4:7]\n"
    "v_mfma_f32_16x16x32_bf16 a[12:15], a[156:159], a[92:95], a[12:15]\n"
    "v_mfma_f32_16x16x32_bf16 a[12:15], a[152:155], a[88:91], a[12:15]\n"
    "buffer_load_dwordx4 v4, s[40:43], s86 offen offset:2048 sc1 lds\n"
    "v_mfma_f32_16x16x32_bf16 v[20:23], a[152:155], a[96:99], v[20:23]\n"
    "v_mfma_f32_16x16x32_bf16 v[20:23], a[156:159], a[100:103], v[20:23]\n"
    "v_mfma_f32_16x16x32_bf16 v[28:31], a[156:159], a[108:111], v[28:31]\n"
    "ds_read_b128 a[188:191], v9 offset:43008\n"
    "v_mfma_f32_16x16x32_bf16 v[28:31], a[152:155], a[104:107], v[28:31]\n"
    "buffer_load_dwordx4 v0, s[76:79], s87 offen offset:3072 lds\n"
    "v_mfma_f32_16x16x32_bf16 v[36:39], a[152:155], a[112:115], v[36:39]\n"
    "v_mfma_f32_16x16x32_bf16 v[36:39], a[156:159], a[116:119], v[36:39]\n"
    "v_mfma_f32_16x16x32_bf16 v[44:47], a[156:159], a[124:127], v[44:47]\n"
    "v_mfma_f32_16x16x32_bf16 v[44:47], a[152:155], a[120:123], v[44:47]\n"
    "v_mfma_f32_16x16x32_bf16 v[52:55], a[152:155], a[128:131], v[52:55]\n"
    "ds_read_b128 a[192:195], v8 offset:45056\n"
    "v_mfma_f32_16x16x32_bf16 v[52:55], a[156:159], a[132:135], v[52:55]\n"
    "s_bitcmp1_b32 s66, s67\n"
    "s_cbranch_scc1 .Lk1_h1v\n"
    ".Lk1_h1v_ret:\n"
    "v_mfma_f32_16x16x32_bf16 v[60:63], a[156:159], a[140:143], v[60:63]\n"
    "global_load_dword a222, v[10:11], off\n"
    "v_mfma_f32_16x16x32_bf16 v[60:63], a[152:155], a[136:139], v[60:63]\n"
    "v_mov_b32 v10, s64\n"
    "s_waitcnt lgkmcnt(7)\n"
    "v_mfma_f32_16x16x32_bf16 v[64:67], a[160:163], a[80:83], v[64:67]\n"
    "v_mov_b32 v11, s65\n"
    "s_add_u32 s67, s67, 1\n"
    "v_mfma_f32_16x16x32_bf16 v[64:67], a[164:167], a[84:87], v[64:67]\n"
    "v_mfma_f32_16x16x32_bf16 v[72:75], a[164:167], a[92:95], v[72:75]\n"
    "ds_read_b128 a[196:199], v9 offset:45056\n"
    "v_add_u32 v2, 128, v2\n"
    "v_mfma_f32_16x16x32_bf16 v[72:75], a[160:163], a[88:91], v[72:75]\n"
    "v_mfma_f32_16x16x32_bf16 v[80:83], a[160:163], a[96:99], v[80:83]\n"
    "v_mfma_f32_16x16x32_bf16 v[80:83], a[164:167], a[100:103], v[80:83]\n"
    "v_add_u32 v3, 128, v3\n"
    "v_mfma_f32_16x16x32_bf16 v[88:91], a[164:167], a[108:111], v[88:91]\n"
    "v_mfma_f32_16x16x32_bf16 v[88:91], a[160:163], a[104:107], v[88:91]\n"
    "v_mfma_f32_16x16x32_bf16 v[96:99], a[160:163], a[112:115], v[96:99]\n"
    "ds_read_b128 a[200:203], v8 offset:47104\n"
    "v_mfma_f32_16x16x32_bf16 v[96:99], a[164:167], a[116:119], v[96:99]\n"
    "v_add_u32 v4, s74, v4\n"
    "v_mfma_f32_16x16x32_bf16 v[104:107], a[164:167], a[124:127], v[104:107]\n"
    "v_mfma_f32_16x16x32_bf16 v[104:107], a[160:163], a[120:123], v[104:107]\n"
    "v_mfma_f32_16x16x32_bf16 v[112:115], a[160:163], a[128:131], v[112:115]\n"
    "v_mfma_f32_16x16x32_bf16 v[112:115], a[164:167], a[132:135], v[112:115]\n"
    "v_add_u32 v5, s74, v5\n"
    "v_mfma_f32_16x16x32_bf16 v[120:123], a[164:167], a[140:143], v[120:123]\n"
    "ds_read_b128 a[204:207], v9 offset:47104\n"
    "v_mfma_f32_16x16x32_bf16 v[120:123], a[160:163], a[136:139], v[120:123]\n"
    "s_waitcnt lgkmcnt(8)\n"
    "v_mfma_f32_16x16x32_bf16 v[68:71], a[168:171], a[80:83], v[68:71]\n"
    "v_add_u32 v0, s74, v0\n"
    "v_mfma_f32_16x16x32_bf16 v[68:71], a[172:175], a[84:87], v[68:71]\n"
    "v_mfma_f32_16x16x32_bf16 v[76:79], a[172:175], a[92:95], v[76:79]\n"
    "v_mfma_f32_16x16x32_bf16 v[76:79], a[168:171], a[88:91], v[76:79]\n"
    "v_mfma_f32_16x16x32_bf16 v[84:87], a[168:171], a[96:99], v[84:87]\n"
    "v_add_u32 v6, 0x80000, v6\n"
    "v_mfma_f32_16x16x32_bf16 v[84:87], a[172:175], a[100:103], v[84:87]\n"
    "v_mfma_f32_16x16x32_bf16 v[92:95], a[172:175], a[108:111], v[92:95]\n"
    "v_mfma_f32_16x16x32_bf16 v[92:95], a[168:171], a[104:107], v[92:95]\n"
    "v_mfma_f32_16x16x32_bf16 v[100:103], a[168:171], a[112:115], v[100:103]\n"
    "v_add_u32 v7, 0x80000, v7\n"
    "v_mfma_f32_16x16x32_bf16 v[100:103], a[172:175], a[116:119], v[100:103]\n"
    "v_mfma_f32_16x16x32_bf16 v[108:111], a[172:175], a[124:127], v[108:111]\n"
    "v_mfma_f32_16x16x32_bf16 v[108:111], a[168:171], a[120:123], v[108:111]\n"
    "v_mfma_f32_16x16x32_bf16 v[116:119], a[168:171], a[128:131], v[116:119]\n"
    "v_mfma_f32_16x16x32_bf16 v[116:119], a[172:175], a[132:135], v[116:119]\n"
    "v_mfma_f32_16x16x32_bf16 v[124:127], a[172:175], a[140:143], v[124:127]\n"
    "v_mfma_f32_16x16x32_bf16 v[124:127], a[168:171], a[136:139], v[124:127]\n"
    "s_waitcnt lgkmcnt(6)\n"
    "v_mfma_f32_16x16x32_bf16 v[128:131], a[176:179], a[80:83], v[128:131]\n"
    "v_mfma_f32_16x16x32_bf16 v[128:131], a[180:183], a[84:87], v[128:131]\n"
    "v_mfma_f32_16x16x32_bf16 v[136:139], a[180:183], a[92:95], v[136:139]\n"
    "s_waitcnt vmcnt(1) lgkmcnt(0)\n"
    "s_barrier\n"
    "v_mfma_f32_16x16x32_bf16 v[136:139], a[176:179], a[88:91], v[136:139]\n"
    "s_bitcmp1_b32 0x1054, s67\n"
    "s_cbranch_scc1 .Lk1_h1vr\n"
    ".Lk1_h1vr_ret:\n"
    "v_mfma_f32_16x16x32_bf16 v[144:147], a[176:179], a[96:99], v[144:147]\n"
    "ds_read_b128 a[144:147], v8 offset:0\n"
    "s_add_u32 m0, s70, 32768\n"
    "v_mfma_f32_16x16x32_bf16 v[144:147], a[180:183], a[100:103], v[144:147]\n"
    "ds_read_b128 a[148:151], v9 offset:0\n"
    "v_mfma_f32_16x16x32_bf16 v[152:155], a[180:183], a[108:111], v[152:155]\n"
    "ds_read_b64_tr_b16 a[16:17], v12 offset:0\n"
    "v_mfma_f32_16x16x32_bf16 v[152:155], a[176:179], a[104:107], v[152:155]\n"
    "ds_read_b64_tr_b16 a[18:19], v12 offset:2048\n"
    "v_mfma_f32_16x16x32_bf16 v[160:163], a[176:179], a[112:115], v[160:163]\n"
    "ds_read_b64_tr_b16 a[20:21], v12 offset:16384\n"
    "v_mfma_f32_16x16x32_bf16 v[160:163], a[180:183], a[116:119], v[160:163]\n"
    "buffer_load_dwordx4 v6, s[44:47], s88 offen offset:0 lds\n"
    "v_mfma_f32_16x16x32_bf16 v[168:171], a[180:183], a[124:127], v[168:171]\n"
    "ds_read_b64_tr_b16 a[22:23], v12 offset:18432\n"
    "v_mfma_f32_16x16x32_bf16 v[168:171], a[176:179], a[120:123], v[168:171]\n"
    "ds_read_b64_tr_b16 a[24:25], v13 offset:0\n"
    "v_mfma_f32_16x16x32_bf16 v[176:179], a[176:179], a[128:131], v[176:179]\n"
    "ds_read_b64_tr_b16 a[26:27], v13 offset:2048\n"
    "v_mfma_f32_16x16x32_bf16 v[176:179], a[180:183], a[132:135], v[176:179]\n"
    "buffer_load_dwordx4 v6, s[44:47], s89 offen offset:1024 lds\n"
    "v_mfma_f32_16x16x32_bf16 v[184:187], a[180:183], a[140:143], v[184:187]\n"
    "ds_read_b64_tr_b16 a[28:29], v13 offset:16384\n"
    "v_mfma_f32_16x16x32_bf16 v[184:187], a[176:179], a[136:139], v[184:187]\n"
    "ds_read_b64_tr_b16 a[30:31], v13 offset:18432\n"
    "v_mfma_f32_16x16x32_bf16 v[132:135], a[184:187], a[80:83], v[132:135]\n"
    "ds_read_b64_tr_b16 a[32:33], v12 offset:256\n"
    "v_mfma_f32_16x16x32_bf16 v[132:135], a[188:191], a[84:87], v[132:135]\n"
    "buffer_load_dwordx4 v6, s[44:47], s90 offen offset:2048 lds\n"
    "v_mfma_f32_16x16x32_bf16 v[140:143], a[188:191], a[92:95], v[140:143]\n"
    "ds_read_b64_tr_b16 a[34:35], v12 offset:2304\n"
    "v_mfma_f32_16x16x32_bf16 v[140:143], a[184:187], a[88:91], v[140:143]\n"
    "ds_read_b64_tr_b16 a[36:37], v12 offset:16640\n"
    "v_mfma_f32_16x16x32_bf16 v[148:151], a[184:187], a[96:99], v[148:151]\n"
    "ds_read_b64_tr_b16 a[38:39], v12 offset:18688\n"
    "v_mfma_f32_16x16x32_bf16 v[148:151], a[188:191], a[100:103], v[148:151]\n"
    "buffer_load_dwordx4 v6, s[44:47], s91 offen offset:3072 lds\n"
    "v_mfma_f32_16x16x32_bf16 v[156:159], a[188:191], a[108:111], v[156:159]\n"
    "ds_read_b64_tr_b16 a[40:41], v13 offset:256\n"
    "v_mfma_f32_16x16x32_bf16 v[156:159], a[184:187], a[104:107], v[156:159]\n"
    "ds_read_b64_tr_b16 a[42:43], v13 offset:2304\n"
    "v_mfma_f32_16x16x32_bf16 v[164:167], a[184:187], a[112:115], v[164:167]\n"
    "s_add_u32 m0, s70, 36864\n"
    "v_mfma_f32_16x16x32_bf16 v[164:167], a[188:191], a[116:119], v[164:167]\n"
    "ds_read_b64_tr_b16 a[44:45], v13 offset:16640\n"
    "v_mfma_f32_16x16x32_bf16 v[172:175], a[188:191], a[124:127], v[172:175]\n"
    "ds_read_b64_tr_b16 a[46:47], v13 offset:18688\n"
    "v_mfma_f32_16x16x32_bf16 v[172:175], a[184:187], a[120:123], v[172:175]\n"
    "ds_read_b64_tr_b16 a[48:49], v12 offset:512\n"
    "v_mfma_f32_16x16x32_bf16 v[180:183], a[184:187], a[128:131], v[180:183]\n"
    "buffer_load_dwordx4 v7, s[44:47], s92 offen offset:0 lds\n"
    "v_mfma_f32_16x16x32_bf16 v[180:183], a[188:191], a[132:135], v[180:183]\n"
    "ds_read_b64_tr_b16 a[50:51], v12 offset:2560\n"
    "v_mfma_f32_16x16x32_bf16 v[188:191], a[188:191], a[140:143], v[188:191]\n"
    "ds_read_b64_tr_b16 a[52:53], v12 offset:16896\n"
    "v_mfma_f32_16x16x32_bf16 v[188:191], a[184:187], a[136:139], v[188:191]\n"
    "ds_read_b64_tr_b16 a[54:55], v12 offset:18944\n"
    "v_mfma_f32_16x16x32_bf16 v[192:195], a[192:195], a[80:83], v[192:195]\n"
    "v_mfma_f32_16x16x32_bf16 v[192:195], a[196:199], a[84:87], v[192:195]\n"
    "ds_read_b64_tr_b16 a[56:57], v13 offset:512\n"
    "v_mfma_f32_16x16x32_bf16 v[200:203], a[196:199], a[92:95], v[200:203]\n"
    "buffer_load_dwordx4 v7, s[44:47], s93 offen offset:1024 lds\n"
    "v_mfma_f32_16x16x32_bf16 v[200:203], a[192:195], a[88:91], v[200:203]\n"
    "ds_read_b64_tr_b16 a[58:59], v13 offset:2560\n"
    "v_mfma_f32_16x16x32_bf16 v[208:211], a[192:195], a[96:99], v[208:211]\n"
    "ds_read_b64_tr_b16 a[60:61], v13 offset:16896\n"
    "s_sub_u32 s96, s96, 1\n"
    "v_mfma_f32_16x16x32_bf16 v[208:211], a[196:199], a[100:103], v[208:211]\n"
    "ds_read_b64_tr_b16 a[62:63], v13 offset:18944\n"
    "v_mfma_f32_16x16x32_bf16 v[216:219], a[196:199], a[108:111], v[216:219]\n"
    "buffer_load_dwordx4 v7, s[44:47], s94 offen offset:2048 lds\n"
    "v_mfma_f32_16x16x32_bf16 v[216:219], a[192:195], a[104:107], v[216:219]\n"
    "ds_read_b64_tr_b16 a[64:65], v12 offset:768\n"
    "v_mfma_f32_16x16x32_bf16 v[224:227], a[192:195], a[112:115], v[224:227]\n"
    "ds_read_b64_tr_b16 a[66:67], v12 offset:2816\n"
    "v_mfma_f32_16x16x32_bf16 v[224:227], a[196:199], a[116:119], v[224:227]\n"
    "ds_read_b64_tr_b16 a[68:69], v12 offset:17152\n"
    "v_mfma_f32_16x16x32_bf16 v[232:235], a[196:199], a[124:127], v[232:235]\n"
    "buffer_load_dwordx4 v7, s[44:47], s95 offen offset:3072 lds\n"
    "v_mfma_f32_16x16x32_bf16 v[232:235], a[192:195], a[120:123], v[232:235]\n"
    "ds_read_b64_tr_b16 a[70:71], v12 offset:19200\n"
    "v_mfma_f32_16x16x32_bf16 v[240:243], a[192:195], a[128:131], v[240:243]\n"
    "ds_read_b64_tr_b16 a[72:73], v13 offset:768\n"
    "v_mfma_f32_16x16x32_bf16 v[240:243], a[196:199], a[132:135], v[240:243]\n"
    "ds_read_b64_tr_b16 a[74:75], v13 offset:2816\n"
    "s_add_u32 m0, s68, 32768\n"
    "v_mfma_f32_16x16x32_bf16 v[248:251], a[196:199], a[140:143], v[248:251]\n"
    "v_mfma_f32_16x16x32_bf16 v[248:251], a[192:195], a[136:139], v[248:251]\n"
    "ds_read_b64_tr_b16 a[76:77], v13 offset:17152\n"
    "v_mfma_f32_16x16x32_bf16 v[196:199], a[200:203], a[80:83], v[196:199]\n"
    "ds_read_b64_tr_b16 a[78:79], v13 offset:19200\n"
    "v_mfma_f32_16x16x32_bf16 v[196:199], a[204:207], a[84:87], v[196:199]\n"
    "ds_read_b128 a[152:155], v8 offset:2048\n"
    "v_mfma_f32_16x16x32_bf16 v[204:207], a[204:207], a[92:95], v[204:207]\n"
    "buffer_load_dwordx4 v2, s[36:39], s80 offen offset:0 lds\n"
    "v_mfma_f32_16x16x32_bf16 v[204:207], a[200:203], a[88:91], v[204:207]\n"
    "ds_read_b128 a[156:159], v9 offset:2048\n"
    "v_mfma_f32_16x16x32_bf16 v[212:215], a[200:203], a[96:99], v[212:215]\n"
    "ds_read_b128 a[160:163], v8 offset:4096\n"
    "v_mfma_f32_16x16x32_bf16 v[212:215], a[204:207], a[100:103], v[212:215]\n"
    "ds_read_b128 a[164:167], v9 offset:4096\n"
    "v_mfma_f32_16x16x32_bf16 v[220:223], a[204:207], a[108:111], v[220:223]\n"
    "buffer_load_dwordx4 v3, s[36:39], s81 offen offset:1024 lds\n"
    "v_mfma_f32_16x16x32_bf16 v[220:223], a[200:203], a[104:107], v[220:223]\n"
    "ds_read_b128 a[168:171], v8 offset:6144\n"
    "v_mfma_f32_16x16x32_bf16 v[228:231], a[200:203], a[112:115], v[228:231]\n"
    "ds_read_b128 a[172:175], v9 offset:6144\n"
    "v_mfma_f32_16x16x32_bf16 v[228:231], a[204:207], a[116:119], v[228:231]\n"
    "buffer_load_dwordx4 v2, s[36:39], s82 offen offset:2048 lds\n"
    "v_mfma_f32_16x16x32_bf16 v[236:239], a[204:207], a[124:127], v[236:239]\n"
    "v_mfma_f32_16x16x32_bf16 v[236:239], a[200:203], a[120:123], v[236:239]\n"
    "v_mfma_f32_16x16x32_bf16 v[244:247], a[200:203], a[128:131], v[244:247]\n"
    "v_mfma_f32_16x16x32_bf16 v[244:247], a[204:207], a[132:135], v[244:247]\n"
    "v_mfma_f32_16x16x32_bf16 v[252:255], a[204:207], a[140:143], v[252:255]\n"
    "v_mfma_f32_16x16x32_bf16 v[252:255], a[200:203], a[136:139], v[252:255]\n"
    "s_branch .Lk1_loop_v\n"
    ".Lk1_swap_v:\n"
    "s_call_b64 s[18:19], .Lk1_swap\n"
    "s_branch .Lk1_swap_v_ret\n"
    ".Lk1_h0v:\n"
    "v_accvgpr_read_b32 v10, a224\n"
    "v_accvgpr_read_b32 v11, a225\n"
    "v_accvgpr_mov_b32 a224, a226\n"
    "v_accvgpr_mov_b32 a225, a227\n"
    "v_accvgpr_mov_b32 a226, a228\n"
    "v_accvgpr_mov_b32 a227, a229\n"
    "v_accvgpr_mov_b32 a228, a230\n"
    "v_accvgpr_mov_b32 a229, a231\n"
    "s_branch .Lk1_h0v_ret\n"
    ".Lk1_h1v:\n"
    "v_accvgpr_read_b32 v10, a224\n"
    "v_accvgpr_read_b32 v11, a225\n"
    "v_accvgpr_mov_b32 a224, a226\n"
    "v_accvgpr_mov_b32 a225, a227\n"
    "v_accvgpr_mov_b32 a226, a228\n"
    "v_accvgpr_mov_b32 a227, a229\n"
    "v_accvgpr_mov_b32 a228, a230\n"
    "v_accvgpr_mov_b32 a229, a231\n"
    "s_branch .Lk1_h1v_ret\n"
    ".Lk1_h1vr:\n"
    "s_setpc_b64 s[34:35]\n"
    ".p2align 5\n"
    ".Lk1_last_k:\n"
    "s_waitcnt lgkmcnt(15)\n"
    "v_mfma_f32_16x16x32_bf16 a[0:3], a[80:83], a[144:147], a[0:3]\n"
    "buffer_load_dwordx4 v3, s[36:39], s83 offen offset:3072 lds\n"
    "v_mfma_f32_16x16x32_bf16 a[0:3], a[84:87], a[148:151], a[0:3]\n"
    "s_add_u32 m0, s69, 0\n"
    "v_mfma_f32_16x16x32_bf16 a[4:7], a[92:95], a[148:151], a[4:7]\n"
    "ds_read_b128 a[176:179], v8 offset:40960\n"
    "v_mfma_f32_16x16x32_bf16 a[4:7], a[88:91], a[144:147], a[4:7]\n"
    "v_mfma_f32_16x16x32_bf16 a[8:11], a[96:99], a[144:147], a[8:11]\n"
    "buffer_load_dwordx4 v4, s[40:43], s84 offen offset:0 sc1 lds\n"
    "v_mfma_f32_16x16x32_bf16 a[8:11], a[100:103], a[148:151], a[8:11]\n"
    "v_mfma_f32_16x16x32_bf16 a[12:15], a[108:111], a[148:151], a[12:15]\n"
    "v_mfma_f32_16x16x32_bf16 a[12:15], a[104:107], a[144:147], a[12:15]\n"
    "v_mfma_f32_16x16x32_bf16 v[16:19], a[112:115], a[144:147], v[16:19]\n"
    "buffer_load_dwordx4 v5, s[40:43], s85 offen offset:1024 sc1 lds\n"
    "v_mfma_f32_16x16x32_bf16 v[16:19], a[116:119], a[148:151], v[16:19]\n"
    "ds_read_b128 a[180:183], v9 offset:40960\n"
    "s_waitcnt lgkmcnt(12)\n"
    "v_mfma_f32_16x16x32_bf16 v[20:23], a[124:127], a[148:151], v[20:23]\n"
    "buffer_load_dwordx4 v4, s[40:43], s86 offen offset:2048 sc1 lds\n"
    "v_mfma_f32_16x16x32_bf16 v[20:23], a[120:123], a[144:147], v[20:23]\n"
    "v_mfma_f32_16x16x32_bf16 v[24:27], a[128:131], a[144:147], v[24:27]\n"
    "buffer_load_dwordx4 v0, s[76:79], s87 offen offset:3072 lds\n"
    "v_mfma_f32_16x16x32_bf16 v[24:27], a[132:135], a[148:151], v[24:27]\n"
    "s_waitcnt lgkmcnt(6)\n"
    "v_mfma_f32_16x16x32_bf16 v[28:31], a[140:143], a[148:151], v[28:31]\n"
    "ds_read_b128 a[184:187], v8 offset:43008\n"
    "v_mfma_f32_16x16x32_bf16 v[28:31], a[136:139], a[144:147], v[28:31]\n"
    "v_add_u32 v2, 128, v2\n"
    "v_mfma_f32_16x16x32_bf16 v[32:35], a[80:83], a[152:155], v[32:35]\n"
    "v_mfma_f32_16x16x32_bf16 v[32:35], a[84:87], a[156:159], v[32:35]\n"
    "v_mfma_f32_16x16x32_bf16 v[36:39], a[92:95], a[156:159], v[36:39]\n"
    "v_add_u32 v3, 128, v3\n"
    "v_mfma_f32_16x16x32_bf16 v[36:39], a[88:91], a[152:155], v[36:39]\n"
    "v_mfma_f32_16x16x32_bf16 v[40:43], a[96:99], a[152:155], v[40:43]\n"
    "ds_read_b128 a[188:191], v9 offset:43008\n"
    "v_mfma_f32_16x16x32_bf16 v[40:43], a[100:103], a[156:159], v[40:43]\n"
    "v_add_u32 v4, s74, v4\n"
    "v_mfma_f32_16x16x32_bf16 v[44:47], a[108:111], a[156:159], v[44:47]\n"
    "v_mfma_f32_16x16x32_bf16 v[44:47], a[104:107], a[152:155], v[44:47]\n"
    "v_mfma_f32_16x16x32_bf16 v[48:51], a[112:115], a[152:155], v[48:51]\n"
    "v_add_u32 v6, 0x80000, v6\n"
    "v_mfma_f32_16x16x32_bf16 v[48:51], a[116:119], a[156:159], v[48:51]\n"
    "v_mfma_f32_16x16x32_bf16 v[52:55], a[124:127], a[156:159], v[52:55]\n"
    "ds_read_b128 a[192:195], v8 offset:45056\n"
    "v_add_u32 v7, 0x80000, v7\n"
    "v_mfma_f32_16x16x32_bf16 v[52:55], a[120:123], a[152:155], v[52:55]\n"
    "v_cvt_pk_bf16_f32 v16, v16, v17\n"
    "v_cvt_pk_bf16_f32 v17, v18, v19\n"
    "v_mfma_f32_16x16x32_bf16 v[56:59], a[128:131], a[152:155], v[56:59]\n"
    "v_cvt_pk_bf16_f32 v18, v20, v21\n"
    "v_cvt_pk_bf16_f32 v19, v22, v23\n"
    "v_mfma_f32_16x16x32_bf16 v[56:59], a[132:135], a[156:159], v[56:59]\n"
    "v_accvgpr_read_b32 v10, a224\n"
    "v_cvt_pk_bf16_f32 v24, v24, v25\n"
    "v_mfma_f32_16x16x32_bf16 v[60:63], a[140:143], a[156:159], v[60:63]\n"
    "v_cvt_pk_bf16_f32 v25, v26, v27\n"
    "v_cvt_pk_bf16_f32 v26, v28, v29\n"
    "v_mfma_f32_16x16x32_bf16 v[60:63], a[136:139], a[152:155], v[60:63]\n"
    "v_cvt_pk_bf16_f32 v27, v30, v31\n"
    "s_waitcnt lgkmcnt(7)\n"
    "v_mfma_f32_16x16x32_bf16 v[64:67], a[80:83], a[160:163], v[64:67]\n"
    "ds_read_b128 a[196:199], v9 offset:45056\n"
    "v_accvgpr_read_b32 v11, a225\n"
    "v_mfma_f32_16x16x32_bf16 v[64:67], a[84:87], a[164:167], v[64:67]\n"
    "v_cvt_pk_bf16_f32 v32, v32, v33\n"
    "v_cvt_pk_bf16_f32 v33, v34, v35\n"
    "v_mfma_f32_16x16x32_bf16 v[68:71], a[92:95], a[164:167], v[68:71]\n"
    "v_cvt_pk_bf16_f32 v34, v36, v37\n"
    "v_cvt_pk_bf16_f32 v35, v38, v39\n"
    "v_mfma_f32_16x16x32_bf16 v[68:71], a[88:91], a[160:163], v[68:71]\n"
    "global_load_dword a222, v[10:11], off\n"
    "v_mfma_f32_16x16x32_bf16 v[72:75], a[96:99], a[160:163], v[72:75]\n"
    "v_cvt_pk_bf16_f32 v40, v40, v41\n"
    "v_cvt_pk_bf16_f32 v41, v42, v43\n"
    "v_mfma_f32_16x16x32_bf16 v[72:75], a[100:103], a[164:167], v[72:75]\n"
    "v_cvt_pk_bf16_f32 v42, v44, v45\n"
    "v_cvt_pk_bf16_f32 v43, v46, v47\n"
    "v_mfma_f32_16x16x32_bf16 v[76:79], a[108:111], a[164:167], v[76:79]\n"
    "ds_read_b128 a[200:203], v8 offset:47104\n"
    "v_accvgpr_read_b32 v20, a0\n"
    "v_mfma_f32_16x16x32_bf16 v[76:79], a[104:107], a[160:163], v[76:79]\n"
    "v_accvgpr_read_b32 v21, a1\n"
    "v_accvgpr_read_b32 v22, a2\n"
    "v_mfma_f32_16x16x32_bf16 v[80:83], a[112:115], a[160:163], v[80:83]\n"
    "v_accvgpr_read_b32 v23, a3\n"
    "v_accvgpr_read_b32 v28, a4\n"
    "v_mfma_f32_16x16x32_bf16 v[80:83], a[116:119], a[164:167], v[80:83]\n"
    "v_accvgpr_read_b32 v29, a5\n"
    "v_accvgpr_read_b32 v30, a6\n"
    "v_mfma_f32_16x16x32_bf16 v[84:87], a[124:127], a[164:167], v[84:87]\n"
    "v_accvgpr_read_b32 v31, a7\n"
    "v_cvt_pk_bf16_f32 v36, v20, v21\n"
    "v_mfma_f32_16x16x32_bf16 v[84:87], a[120:123], a[160:163], v[84:87]\n"
    "v_cvt_pk_bf16_f32 v37, v22, v23\n"
    "v_cvt_pk_bf16_f32 v38, v28, v29\n"
    "v_mfma_f32_16x16x32_bf16 v[88:91], a[128:131], a[160:163], v[88:91]\n"
    "ds_read_b128 a[204:207], v9 offset:47104\n"
    "v_accvgpr_read_b32 v8, a243\n"
    "v_mfma_f32_16x16x32_bf16 v[88:91], a[132:135], a[164:167], v[88:91]\n"
    "v_accvgpr_read_b32 v9, a244\n"
    "v_accvgpr_read_b32 v10, a245\n"
    "v_mfma_f32_16x16x32_bf16 v[92:95], a[140:143], a[164:167], v[92:95]\n"
    "v_accvgpr_read_b32 v11, a246\n"
    "v_accvgpr_read_b32 v15, a247\n"
    "v_mfma_f32_16x16x32_bf16 v[92:95], a[136:139], a[160:163], v[92:95]\n"
    "v_accvgpr_read_b32 v0, a251\n"
    "s_waitcnt lgkmcnt(8)\n"
    "v_mfma_f32_16x16x32_bf16 v[96:99], a[80:83], a[168:171], v[96:99]\n"
    "v_xor_b32 v5, 32, v15\n"
    "v_cvt_pk_bf16_f32 v39, v30, v31\n"
    "v_mfma_f32_16x16x32_bf16 v[96:99], a[84:87], a[172:175], v[96:99]\n"
    "v_accvgpr_read_b32 v20, a8\n"
    "v_accvgpr_read_b32 v21, a9\n"
    "v_mfma_f32_16x16x32_bf16 v[100:103], a[92:95], a[172:175], v[100:103]\n"
    "v_accvgpr_read_b32 v22, a10\n"
    "v_accvgpr_read_b32 v23, a11\n"
    "v_mfma_f32_16x16x32_bf16 v[100:103], a[88:91], a[168:171], v[100:103]\n"
    "v_accvgpr_read_b32 v28, a12\n"
    "v_accvgpr_read_b32 v29, a13\n"
    "v_mfma_f32_16x16x32_bf16 v[104:107], a[96:99], a[168:171], v[104:107]\n"
    "v_accvgpr_read_b32 v30, a14\n"
    "v_accvgpr_read_b32 v31, a15\n"
    "v_mfma_f32_16x16x32_bf16 v[104:107], a[100:103], a[172:175], v[104:107]\n"
    "v_cvt_pk_bf16_f32 v44, v20, v21\n"
    "v_cvt_pk_bf16_f32 v45, v22, v23\n"
    "v_mfma_f32_16x16x32_bf16 v[108:111], a[108:111], a[172:175], v[108:111]\n"
    "v_cvt_pk_bf16_f32 v46, v28, v29\n"
    "v_cvt_pk_bf16_f32 v47, v30, v31\n"
    "v_mfma_f32_16x16x32_bf16 v[108:111], a[104:107], a[168:171], v[108:111]\n"
    "s_mul_i32 s97, s99, 0\n"
    "v_cvt_pk_bf16_f32 v48, v48, v49\n"
    "v_mfma_f32_16x16x32_bf16 v[112:115], a[112:115], a[168:171], v[112:115]\n"
    "v_cvt_pk_bf16_f32 v49, v50, v51\n"
    "v_cvt_pk_bf16_f32 v50, v52, v53\n"
    "v_mfma_f32_16x16x32_bf16 v[112:115], a[116:119], a[172:175], v[112:115]\n"
    "s_add_u32 s97, s97, 0\n"
    "v_cvt_pk_bf16_f32 v51, v54, v55\n"
    "v_mfma_f32_16x16x32_bf16 v[116:119], a[124:127], a[172:175], v[116:119]\n"
    "v_cvt_pk_bf16_f32 v56, v56, v57\n"
    "v_cvt_pk_bf16_f32 v57, v58, v59\n"
    "v_mfma_f32_16x16x32_bf16 v[116:119], a[120:123], a[168:171], v[116:119]\n"
    "v_cvt_pk_bf16_f32 v58, v60, v61\n"
    "v_cvt_pk_bf16_f32 v59, v62, v63\n"
    "v_mfma_f32_16x16x32_bf16 v[120:123], a[128:131], a[168:171], v[120:123]\n"
    "v_cvt_pk_bf16_f32 v64, v64, v65\n"
    "v_cvt_pk_bf16_f32 v65, v66, v67\n"
    "v_mfma_f32_16x16x32_bf16 v[120:123], a[132:135], a[172:175], v[120:123]\n"
    "v_cvt_pk_bf16_f32 v66, v68, v69\n"
    "v_cvt_pk_bf16_f32 v67, v70, v71\n"
    "v_mfma_f32_16x16x32_bf16 v[124:127], a[140:143], a[172:175], v[124:127]\n"
    "v_cvt_pk_bf16_f32 v72, v72, v73\n"
    "v_cvt_pk_bf16_f32 v73, v74, v75\n"
    "v_mfma_f32_16x16x32_bf16 v[124:127], a[136:139], a[168:171], v[124:127]\n"
    "v_cvt_pk_bf16_f32 v74, v76, v77\n"
    "s_waitcnt lgkmcnt(6)\n"
    "v_mfma_f32_16x16x32_bf16 v[128:131], a[80:83], a[176:179], v[128:131]\n"
    "v_cvt_pk_bf16_f32 v75, v78, v79\n"
    "v_cvt_pk_bf16_f32 v80, v80, v81\n"
    "v_mfma_f32_16x16x32_bf16 v[128:131], a[84:87], a[180:183], v[128:131]\n"
    "v_cvt_pk_bf16_f32 v81, v82, v83\n"
    "v_cvt_pk_bf16_f32 v82, v84, v85\n"
    "v_mfma_f32_16x16x32_bf16 v[132:135], a[92:95], a[180:183], v[132:135]\n"
    "s_waitcnt vmcnt(1) lgkmcnt(0)\n"
    "s_barrier\n"
    "v_mfma_f32_16x16x32_bf16 v[132:135], a[88:91], a[176:179], v[132:135]\n"
    "ds_read_b128 a[16:19], v8 offset:0\n"
    "s_add_u32 m0, s68, 32768\n"
    "v_mfma_f32_16x16x32_bf16 v[136:139], a[96:99], a[176:179], v[136:139]\n"
    "ds_read_b64_tr_b16 a[32:33], v12 offset:0\n"
    "v_cvt_pk_bf16_f32 v83, v86, v87\n"
    "v_mfma_f32_16x16x32_bf16 v[136:139], a[100:103], a[180:183], v[136:139]\n"
    "buffer_load_dwordx4 v2, s[36:39], s80 offen offset:0 lds\n"
    "v_mfma_f32_16x16x32_bf16 v[140:143], a[108:111], a[180:183], v[140:143]\n"
    "ds_read_b64_tr_b16 a[34:35], v12 offset:2048\n"
    "v_cvt_pk_bf16_f32 v88, v88, v89\n"
    "v_mfma_f32_16x16x32_bf16 v[140:143], a[104:107], a[176:179], v[140:143]\n"
    "buffer_store_dwordx4 v[36:39], v14, s[48:51], s97 offen sc1\n"
    "v_mfma_f32_16x16x32_bf16 v[144:147], a[112:115], a[176:179], v[144:147]\n"
    "ds_read_b64_tr_b16 a[36:37], v13 offset:0\n"
    "v_cvt_pk_bf16_f32 v89, v90, v91\n"
    "v_mfma_f32_16x16x32_bf16 v[144:147], a[116:119], a[180:183], v[144:147]\n"
    "buffer_load_dwordx4 v3, s[36:39], s81 offen offset:1024 lds\n"
    "v_mfma_f32_16x16x32_bf16 v[148:151], a[124:127], a[180:183], v[148:151]\n"
    "ds_read_b64_tr_b16 a[38:39], v13 offset:2048\n"
    "v_cvt_pk_bf16_f32 v90, v92, v93\n"
    "v_mfma_f32_16x16x32_bf16 v[148:151], a[120:123], a[176:179], v[148:151]\n"
    "ds_read_b64_tr_b16 a[40:41], v12 offset:256\n"
    "v_cvt_pk_bf16_f32 v91, v94, v95\n"
    "v_mfma_f32_16x16x32_bf16 v[152:155], a[128:131], a[176:179], v[152:155]\n"
    "buffer_load_dwordx4 v2, s[36:39], s82 offen offset:2048 lds\n"
    "v_mfma_f32_16x16x32_bf16 v[152:155], a[132:135], a[180:183], v[152:155]\n"
    "ds_read_b64_tr_b16 a[42:43], v12 offset:2304\n"
    "v_cvt_pk_bf16_f32 v96, v96, v97\n"
    "v_mfma_f32_16x16x32_bf16 v[156:159], a[140:143], a[180:183], v[156:159]\n"
    "buffer_load_dwordx4 v3, s[36:39], s83 offen offset:3072 lds\n"
    "v_mfma_f32_16x16x32_bf16 v[156:159], a[136:139], a[176:179], v[156:159]\n"
    "ds_read_b64_tr_b16 a[44:45], v13 offset:256\n"
    "v_mfma_f32_16x16x32_bf16 v[160:163], a[80:83], a[184:187], v[160:163]\n"
    "ds_read_b64_tr_b16 a[46:47], v13 offset:2304\n"
    "s_add_u32 m0, s69, 32768\n"
    "v_mfma_f32_16x16x32_bf16 v[160:163], a[84:87], a[188:191], v[160:163]\n"
    "ds_read_b64_tr_b16 a[48:49], v12 offset:512\n"
    "v_cvt_pk_bf16_f32 v97, v98, v99\n"
    "v_mfma_f32_16x16x32_bf16 v[164:167], a[92:95], a[188:191], v[164:167]\n"
    "ds_read_b64_tr_b16 a[50:51], v12 offset:2560\n"
    "v_cvt_pk_bf16_f32 v98, v100, v101\n"
    "v_mfma_f32_16x16x32_bf16 v[164:167], a[88:91], a[184:187], v[164:167]\n"
    "buffer_load_dwordx4 v4, s[40:43], s84 offen offset:0 sc1 lds\n"
    "v_mfma_f32_16x16x32_bf16 v[168:171], a[96:99], a[184:187], v[168:171]\n"
    "ds_read_b64_tr_b16 a[52:53], v13 offset:512\n"
    "s_mul_i32 s97, s99, 0\n"
    "v_mfma_f32_16x16x32_bf16 v[168:171], a[100:103], a[188:191], v[168:171]\n"
    "buffer_load_dwordx4 v4, s[40:43], s85 offen offset:1024 sc1 lds\n"
    "v_mfma_f32_16x16x32_bf16 v[172:175], a[108:111], a[188:191], v[172:175]\n"
    "ds_read_b64_tr_b16 a[54:55], v13 offset:2560\n"
    "v_cvt_pk_bf16_f32 v99, v102, v103\n"
    "v_mfma_f32_16x16x32_bf16 v[172:175], a[104:107], a[184:187], v[172:175]\n"
    "ds_read_b64_tr_b16 a[56:57], v12 offset:768\n"
    "v_cvt_pk_bf16_f32 v104, v104, v105\n"
    "v_mfma_f32_16x16x32_bf16 v[176:179], a[112:115], a[184:187], v[176:179]\n"
    "buffer_load_dwordx4 v4, s[40:43], s86 offen offset:2048 sc1 lds\n"
    "v_mfma_f32_16x16x32_bf16 v[176:179], a[116:119], a[188:191], v[176:179]\n"
    "ds_read_b64_tr_b16 a[58:59], v12 offset:2816\n"
    "v_cvt_pk_bf16_f32 v105, v106, v107\n"
    "v_mfma_f32_16x16x32_bf16 v[180:183], a[124:127], a[188:191], v[180:183]\n"
    "ds_read_b64_tr_b16 a[60:61], v13 offset:768\n"
    "v_add_u32 v2, 128, v2\n"
    "v_mfma_f32_16x16x32_bf16 v[180:183], a[120:123], a[184:187], v[180:183]\n"
    "ds_read_b64_tr_b16 a[62:63], v13 offset:2816\n"
    "v_cvt_pk_bf16_f32 v106, v108, v109\n"
    "v_mfma_f32_16x16x32_bf16 v[184:187], a[128:131], a[184:187], v[184:187]\n"
    "v_add_u32 v3, 128, v3\n"
    "v_cvt_pk_bf16_f32 v107, v110, v111\n"
    "v_mfma_f32_16x16x32_bf16 v[184:187], a[132:135], a[188:191], v[184:187]\n"
    "ds_read_b128 a[20:23], v8 offset:2048\n"
    "v_cvt_pk_bf16_f32 v112, v112, v113\n"
    "v_mfma_f32_16x16x32_bf16 v[188:191], a[140:143], a[188:191], v[188:191]\n"
    "v_cvt_pk_bf16_f32 v113, v114, v115\n"
    "v_cvt_pk_bf16_f32 v114, v116, v117\n"
    "v_mfma_f32_16x16x32_bf16 v[188:191], a[136:139], a[184:187], v[188:191]\n"
    "ds_read_b128 a[24:27], v9 offset:0\n"
    "v_mfma_f32_16x16x32_bf16 v[192:195], a[80:83], a[192:195], v[192:195]\n"
    "ds_read_b128 a[28:31], v9 offset:2048\n"
    "v_add_u32 v4, s74, v4\n"
    "v_mfma_f32_16x16x32_bf16 v[192:195], a[84:87], a[196:199], v[192:195]\n"
    "s_add_u32 s97, s97, 1024\n"
    "v_cvt_pk_bf16_f32 v115, v118, v119\n"
    "v_mfma_f32_16x16x32_bf16 v[196:199], a[92:95], a[196:199], v[196:199]\n"
    "ds_read_b64_tr_b16 a[64:65], v15 offset:0\n"
    "s_add_u32 m0, s70, 32768\n"
    "v_mfma_f32_16x16x32_bf16 v[196:199], a[88:91], a[192:195], v[196:199]\n"
    "v_cvt_pk_bf16_f32 v120, v120, v121\n"
    "v_cvt_pk_bf16_f32 v121, v122, v123\n"
    "v_mfma_f32_16x16x32_bf16 v[200:203], a[96:99], a[192:195], v[200:203]\n"
    "ds_read_b64_tr_b16 a[66:67], v15 offset:256\n"
    "v_cvt_pk_bf16_f32 v122, v124, v125\n"
    "v_mfma_f32_16x16x32_bf16 v[200:203], a[100:103], a[196:199], v[200:203]\n"
    "ds_read_b64_tr_b16 a[68:69], v5 offset:0\n"
    "v_cvt_pk_bf16_f32 v123, v126, v127\n"
    "v_mfma_f32_16x16x32_bf16 v[204:207], a[108:111], a[196:199], v[204:207]\n"
    "v_cvt_pk_bf16_f32 v128, v128, v129\n"
    "v_cvt_pk_bf16_f32 v129, v130, v131\n"
    "v_mfma_f32_16x16x32_bf16 v[204:207], a[104:107], a[192:195], v[204:207]\n"
    "buffer_load_dwordx4 v6, s[44:47], s88 offen offset:0 lds\n"
    "v_mfma_f32_16x16x32_bf16 v[208:211], a[112:115], a[192:195], v[208:211]\n"
    "ds_read_b64_tr_b16 a[70:71], v5 offset:256\n"
    "v_cvt_pk_bf16_f32 v130, v132, v133\n"
    "v_mfma_f32_16x16x32_bf16 v[208:211], a[116:119], a[196:199], v[208:211]\n"
    "ds_read_b64_tr_b16 a[72:73], v15 offset:1024\n"
    "v_cvt_pk_bf16_f32 v131, v134, v135\n"
    "v_mfma_f32_16x16x32_bf16 v[212:215], a[124:127], a[196:199], v[212:215]\n"
    "ds_read_b64_tr_b16 a[74:75], v15 offset:1280\n"
    "v_cvt_pk_bf16_f32 v136, v136, v137\n"
    "v_mfma_f32_16x16x32_bf16 v[212:215], a[120:123], a[192:195], v[212:215]\n"
    "v_cvt_pk_bf16_f32 v137, v138, v139\n"
    "v_cvt_pk_bf16_f32 v138, v140, v141\n"
    "v_mfma_f32_16x16x32_bf16 v[216:219], a[128:131], a[192:195], v[216:219]\n"
    "buffer_load_dwordx4 v6, s[44:47], s89 offen offset:1024 lds\n"
    "v_mfma_f32_16x16x32_bf16 v[216:219], a[132:135], a[196:199], v[216:219]\n"
    "ds_read_b64_tr_b16 a[76:77], v5 offset:1024\n"
    "v_cvt_pk_bf16_f32 v139, v142, v143\n"
    "v_mfma_f32_16x16x32_bf16 v[220:223], a[140:143], a[196:199], v[220:223]\n"
    "buffer_store_dwordx4 v[44:47], v14, s[48:51], s97 offen sc1\n"
    "v_mfma_f32_16x16x32_bf16 v[220:223], a[136:139], a[192:195], v[220:223]\n"
    "ds_read_b64_tr_b16 a[78:79], v5 offset:1280\n"
    "v_mfma_f32_16x16x32_bf16 v[224:227], a[80:83], a[200:203], v[224:227]\n"
    "ds_read_b64_tr_b16 a[144:145], v15 offset:2048\n"
    "v_cvt_pk_bf16_f32 v144, v144, v145\n"
    "v_mfma_f32_16x16x32_bf16 v[224:227], a[84:87], a[204:207], v[224:227]\n"
    "buffer_load_dwordx4 v6, s[44:47], s90 offen offset:2048 lds\n"
    "v_mfma_f32_16x16x32_bf16 v[228:231], a[92:95], a[204:207], v[228:231]\n"
    "ds_read_b64_tr_b16 a[146:147], v15 offset:2304\n"
    "v_cvt_pk_bf16_f32 v145, v146, v147\n"
    "v_mfma_f32_16x16x32_bf16 v[228:231], a[88:91], a[200:203], v[228:231]\n"
    "ds_read_b64_tr_b16 a[148:149], v5 offset:2048\n"
    "v_cvt_pk_bf16_f32 v146, v148, v149\n"
    "v_mfma_f32_16x16x32_bf16 v[232:235], a[96:99], a[200:203], v[232:235]\n"
    "ds_read_b64_tr_b16 a[150:151], v5 offset:2304\n"
    "v_cvt_pk_bf16_f32 v147, v150, v151\n"
    "v_mfma_f32_16x16x32_bf16 v[232:235], a[100:103], a[204:207], v[232:235]\n"
    "v_cvt_pk_bf16_f32 v152, v152, v153\n"
    "v_cvt_pk_bf16_f32 v153, v154, v155\n"
    "v_mfma_f32_16x16x32_bf16 v[236:239], a[108:111], a[204:207], v[236:239]\n"
    "buffer_load_dwordx4 v6, s[44:47], s91 offen offset:3072 lds\n"
    "v_mfma_f32_16x16x32_bf16 v[236:239], a[104:107], a[200:203], v[236:239]\n"
    "ds_read_b128 a[152:155], v0 offset:0\n"
    "v_cvt_pk_bf16_f32 v154, v156, v157\n"
    "v_mfma_f32_16x16x32_bf16 v[240:243], a[112:115], a[200:203], v[240:243]\n"
    "v_cvt_pk_bf16_f32 v155, v158, v159\n"
    "v_cvt_pk_bf16_f32 v160, v160, v161\n"
    "v_mfma_f32_16x16x32_bf16 v[240:243], a[116:119], a[204:207], v[240:243]\n"
    "v_cvt_pk_bf16_f32 v161, v162, v163\n"
    "v_cvt_pk_bf16_f32 v162, v164, v165\n"
    "v_mfma_f32_16x16x32_bf16 v[244:247], a[124:127], a[204:207], v[244:247]\n"
    "v_cvt_pk_bf16_f32 v163, v166, v167\n"
    "v_cvt_pk_bf16_f32 v168, v168, v169\n"
    "v_mfma_f32_16x16x32_bf16 v[244:247], a[120:123], a[200:203], v[244:247]\n"
    "v_cvt_pk_bf16_f32 v169, v170, v171\n"
    "v_cvt_pk_bf16_f32 v170, v172, v173\n"
    "v_mfma_f32_16x16x32_bf16 v[248:251], a[128:131], a[200:203], v[248:251]\n"
    "v_cvt_pk_bf16_f32 v171, v174, v175\n"
    "v_cvt_pk_bf16_f32 v176, v176, v177\n"
    "v_mfma_f32_16x16x32_bf16 v[248:251], a[132:135], a[204:207], v[248:251]\n"
    "v_cvt_pk_bf16_f32 v177, v178, v179\n"
    "v_cvt_pk_bf16_f32 v178, v180, v181\n"
    "v_mfma_f32_16x16x32_bf16 v[252:255], a[140:143], a[204:207], v[252:255]\n"
    "v_cvt_pk_bf16_f32 v179, v182, v183\n"
    "v_cvt_pk_bf16_f32 v184, v184, v185\n"
    "v_mfma_f32_16x16x32_bf16 v[252:255], a[136:139], a[200:203], v[252:255]\n"
    "s_nop 7\n"
    "v_cvt_pk_bf16_f32 v185, v186, v187\n"
    "v_cvt_pk_bf16_f32 v186, v188, v189\n"
    "v_cvt_pk_bf16_f32 v187, v190, v191\n"
    "v_cvt_pk_bf16_f32 v192, v192, v193\n"
    "v_cvt_pk_bf16_f32 v193, v194, v195\n"
    "v_cvt_pk_bf16_f32 v194, v196, v197\n"
    "v_cvt_pk_bf16_f32 v195, v198, v199\n"
    "v_cvt_pk_bf16_f32 v200, v200, v201\n"
    "v_cvt_pk_bf16_f32 v201, v202, v203\n"
    "v_cvt_pk_bf16_f32 v202, v204, v205\n"
    "v_cvt_pk_bf16_f32 v203, v206, v207\n"
    "v_cvt_pk_bf16_f32 v208, v208, v209\n"
    "v_cvt_pk_bf16_f32 v209, v210, v211\n"
    "v_cvt_pk_bf16_f32 v210, v212, v213\n"
    "v_cvt_pk_bf16_f32 v211, v214, v215\n"
    "v_cvt_pk_bf16_f32 v216, v216, v217\n"
    "v_cvt_pk_bf16_f32 v217, v218, v219\n"
    "v_cvt_pk_bf16_f32 v218, v220, v221\n"
    "v_cvt_pk_bf16_f32 v219, v222, v223\n"
    "v_cvt_pk_bf16_f32 v224, v224, v225\n"
    "v_cvt_pk_bf16_f32 v225, v226, v227\n"
    "v_cvt_pk_bf16_f32 v226, v228, v229\n"
    "v_cvt_pk_bf16_f32 v227, v230, v231\n"
    "v_cvt_pk_bf16_f32 v232, v232, v233\n"
    "v_cvt_pk_bf16_f32 v233, v234, v235\n"
    "v_cvt_pk_bf16_f32 v234, v236, v237\n"
    "v_cvt_pk_bf16_f32 v235, v238, v239\n"
    "v_cvt_pk_bf16_f32 v240, v240, v241\n"
    "v_cvt_pk_bf16_f32 v241, v242, v243\n"
    "v_cvt_pk_bf16_f32 v242, v244, v245\n"
    "v_cvt_pk_bf16_f32 v243, v246, v247\n"
    "v_cvt_pk_bf16_f32 v248, v248, v249\n"
    "v_cvt_pk_bf16_f32 v249, v250, v251\n"
    "v_cvt_pk_bf16_f32 v250, v252, v253\n"
    "v_cvt_pk_bf16_f32 v251, v254, v255\n"
    "s_branch .Lk1_u2\n"
    ".p2align 5\n"
    ".Lk1_last_v:\n"
    "s_waitcnt lgkmcnt(15)\n"
    "v_mfma_f32_16x16x32_bf16 a[0:3], a[144:147], a[80:83], a[0:3]\n"
    "buffer_load_dwordx4 v3, s[36:39], s83 offen offset:3072 lds\n"
    "v_mfma_f32_16x16x32_bf16 a[0:3], a[148:151], a[84:87], a[0:3]\n"
    "s_add_u32 m0, s69, 0\n"
    "v_mfma_f32_16x16x32_bf16 a[8:11], a[148:151], a[92:95], a[8:11]\n"
    "ds_read_b128 a[176:179], v8 offset:40960\n"
    "v_mfma_f32_16x16x32_bf16 a[8:11], a[144:147], a[88:91], a[8:11]\n"
    "v_mfma_f32_16x16x32_bf16 v[16:19], a[144:147], a[96:99], v[16:19]\n"
    "buffer_load_dwordx4 v4, s[40:43], s84 offen offset:0 sc1 lds\n"
    "v_mfma_f32_16x16x32_bf16 v[16:19], a[148:151], a[100:103], v[16:19]\n"
    "v_mfma_f32_16x16x32_bf16 v[24:27], a[148:151], a[108:111], v[24:27]\n"
    "v_mfma_f32_16x16x32_bf16 v[24:27], a[144:147], a[104:107], v[24:27]\n"
    "v_mfma_f32_16x16x32_bf16 v[32:35], a[144:147], a[112:115], v[32:35]\n"
    "buffer_load_dwordx4 v5, s[40:43], s85 offen offset:1024 sc1 lds\n"
    "v_mfma_f32_16x16x32_bf16 v[32:35], a[148:151], a[116:119], v[32:35]\n"
    "ds_read_b128 a[180:183], v9 offset:40960\n"
    "s_waitcnt lgkmcnt(12)\n"
    "v_mfma_f32_16x16x32_bf16 v[40:43], a[148:151], a[124:127], v[40:43]\n"
    "buffer_load_dwordx4 v4, s[40:43], s86 offen offset:2048 sc1 lds\n"
    "v_mfma_f32_16x16x32_bf16 v[40:43], a[144:147], a[120:123], v[40:43]\n"
    "v_mfma_f32_16x16x32_bf16 v[48:51], a[144:147], a[128:131], v[48:51]\n"
    "buffer_load_dwordx4 v0, s[76:79], s87 offen offset:3072 lds\n"
    "v_mfma_f32_16x16x32_bf16 v[48:51], a[148:151], a[132:135], v[48:51]\n"
    "s_waitcnt lgkmcnt(6)\n"
    "v_mfma_f32_16x16x32_bf16 v[56:59], a[148:151], a[140:143], v[56:59]\n"
    "ds_read_b128 a[184:187], v8 offset:43008\n"
    "v_mfma_f32_16x16x32_bf16 v[56:59], a[144:147], a[136:139], v[56:59]\n"
    "v_add_u32 v2, 128, v2\n"
    "v_mfma_f32_16x16x32_bf16 a[4:7], a[152:155], a[80:83], a[4:7]\n"
    "v_mfma_f32_16x16x32_bf16 a[4:7], a[156:159], a[84:87], a[4:7]\n"
    "v_mfma_f32_16x16x32_bf16 a[12:15], a[156:159], a[92:95], a[12:15]\n"
    "v_add_u32 v3, 128, v3\n"
    "v_mfma_f32_16x16x32_bf16 a[12:15], a[152:155], a[88:91], a[12:15]\n"
    "v_mfma_f32_16x16x32_bf16 v[20:23], a[152:155], a[96:99], v[20:23]\n"
    "ds_read_b128 a[188:191], v9 offset:43008\n"
    "v_mfma_f32_16x16x32_bf16 v[20:23], a[156:159], a[100:103], v[20:23]\n"
    "v_add_u32 v4, s74, v4\n"
    "v_mfma_f32_16x16x32_bf16 v[28:31], a[156:159], a[108:111], v[28:31]\n"
    "v_mfma_f32_16x16x32_bf16 v[28:31], a[152:155], a[104:107], v[28:31]\n"
    "v_mfma_f32_16x16x32_bf16 v[36:39], a[152:155], a[112:115], v[36:39]\n"
    "v_add_u32 v6, 0x80000, v6\n"
    "v_mfma_f32_16x16x32_bf16 v[36:39], a[156:159], a[116:119], v[36:39]\n"
    "v_mfma_f32_16x16x32_bf16 v[44:47], a[156:159], a[124:127], v[44:47]\n"
    "ds_read_b128 a[192:195], v8 offset:45056\n"
    "v_add_u32 v7, 0x80000, v7\n"
    "v_mfma_f32_16x16x32_bf16 v[44:47], a[152:155], a[120:123], v[44:47]\n"
    "v_cvt_pk_bf16_f32 v16, v16, v17\n"
    "v_cvt_pk_bf16_f32 v17, v18, v19\n"
    "v_mfma_f32_16x16x32_bf16 v[52:55], a[152:155], a[128:131], v[52:55]\n"
    "v_cvt_pk_bf16_f32 v18, v20, v21\n"
    "v_cvt_pk_bf16_f32 v19, v22, v23\n"
    "v_mfma_f32_16x16x32_bf16 v[52:55], a[156:159], a[132:135], v[52:55]\n"
    "v_accvgpr_read_b32 v10, a224\n"
    "v_cvt_pk_bf16_f32 v24, v24, v25\n"
    "v_mfma_f32_16x16x32_bf16 v[60:63], a[156:159], a[140:143], v[60:63]\n"
    "v_cvt_pk_bf16_f32 v25, v26, v27\n"
    "v_cvt_pk_bf16_f32 v26, v28, v29\n"
    "v_mfma_f32_16x16x32_bf16 v[60:63], a[152:155], a[136:139], v[60:63]\n"
    "v_cvt_pk_bf16_f32 v27, v30, v31\n"
    "s_waitcnt lgkmcnt(7)\n"
    "v_mfma_f32_16x16x32_bf16 v[64:67], a[160:163], a[80:83], v[64:67]\n"
    "ds_read_b128 a[196:199], v9 offset:45056\n"
    "v_accvgpr_read_b32 v11, a225\n"
    "v_mfma_f32_16x16x32_bf16 v[64:67], a[164:167], a[84:87], v[64:67]\n"
    "v_cvt_pk_bf16_f32 v32, v32, v33\n"
    "v_cvt_pk_bf16_f32 v33, v34, v35\n"
    "v_mfma_f32_16x16x32_bf16 v[72:75], a[164:167], a[92:95], v[72:75]\n"
    "v_cvt_pk_bf16_f32 v34, v36, v37\n"
    "v_cvt_pk_bf16_f32 v35, v38, v39\n"
    "v_mfma_f32_16x16x32_bf16 v[72:75], a[160:163], a[88:91], v[72:75]\n"
    "global_load_dword a222, v[10:11], off\n"
    "v_mfma_f32_16x16x32_bf16 v[80:83], a[160:163], a[96:99], v[80:83]\n"
    "v_cvt_pk_bf16_f32 v40, v40, v41\n"
    "v_cvt_pk_bf16_f32 v41, v42, v43\n"
    "v_mfma_f32_16x16x32_bf16 v[80:83], a[164:167], a[100:103], v[80:83]\n"
    "v_cvt_pk_bf16_f32 v42, v44, v45\n"
    "v_cvt_pk_bf16_f32 v43, v46, v47\n"
    "v_mfma_f32_16x16x32_bf16 v[88:91], a[164:167], a[108:111], v[88:91]\n"
    "ds_read_b128 a[200:203], v8 offset:47104\n"
    "v_accvgpr_read_b32 v20, a0\n"
    "v_mfma_f32_16x16x32_bf16 v[88:91], a[160:163], a[104:107], v[88:91]\n"
    "v_accvgpr_read_b32 v21, a1\n"
    "v_accvgpr_read_b32 v22, a2\n"
    "v_mfma_f32_16x16x32_bf16 v[96:99], a[160:163], a[112:115], v[96:99]\n"
    "v_accvgpr_read_b32 v23, a3\n"
    "v_accvgpr_read_b32 v28, a4\n"
    "v_mfma_f32_16x16x32_bf16 v[96:99], a[164:167], a[116:119], v[96:99]\n"
    "v_accvgpr_read_b32 v29, a5\n"
    "v_accvgpr_read_b32 v30, a6\n"
    "v_mfma_f32_16x16x32_bf16 v[104:107], a[164:167], a[124:127], v[104:107]\n"
    "v_accvgpr_read_b32 v31, a7\n"
    "v_cvt_pk_bf16_f32 v36, v20, v21\n"
    "v_mfma_f32_16x16x32_bf16 v[104:107], a[160:163], a[120:123], v[104:107]\n"
    "v_cvt_pk_bf16_f32 v37, v22, v23\n"
    "v_cvt_pk_bf16_f32 v38, v28, v29\n"
    "v_mfma_f32_16x16x32_bf16 v[112:115], a[160:163], a[128:131], v[112:115]\n"
    "ds_read_b128 a[204:207], v9 offset:47104\n"
    "v_accvgpr_read_b32 v8, a243\n"
    "v_mfma_f32_16x16x32_bf16 v[112:115], a[164:167], a[132:135], v[112:115]\n"
    "v_accvgpr_read_b32 v9, a244\n"
    "v_accvgpr_read_b32 v10, a245\n"
    "v_mfma_f32_16x16x32_bf16 v[120:123], a[164:167], a[140:143], v[120:123]\n"
    "v_accvgpr_read_b32 v11, a246\n"
    "v_accvgpr_read_b32 v15, a247\n"
    "v_mfma_f32_16x16x32_bf16 v[120:123], a[160:163], a[136:139], v[120:123]\n"
    "v_accvgpr_read_b32 v0, a251\n"
    "s_waitcnt lgkmcnt(8)\n"
    "v_mfma_f32_16x16x32_bf16 v[68:71], a[168:171], a[80:83], v[68:71]\n"
    "v_xor_b32 v5, 32, v15\n"
    "v_cvt_pk_bf16_f32 v39, v30, v31\n"
    "v_mfma_f32_16x16x32_bf16 v[68:71], a[172:175], a[84:87], v[68:71]\n"
    "v_accvgpr_read_b32 v20, a8\n"
    "v_accvgpr_read_b32 v21, a9\n"
    "v_mfma_f32_16x16x32_bf16 v[76:79], a[172:175], a[92:95], v[76:79]\n"
    "v_accvgpr_read_b32 v22, a10\n"
    "v_accvgpr_read_b32 v23, a11\n"
    "v_mfma_f32_16x16x32_bf16 v[76:79], a[168:171], a[88:91], v[76:79]\n"
    "v_accvgpr_read_b32 v28, a12\n"
    "v_accvgpr_read_b32 v29, a13\n"
    "v_mfma_f32_16x16x32_bf16 v[84:87], a[168:171], a[96:99], v[84:87]\n"
    "v_accvgpr_read_b32 v30, a14\n"
    "v_accvgpr_read_b32 v31, a15\n"
    "v_mfma_f32_16x16x32_bf16 v[84:87], a[172:175], a[100:103], v[84:87]\n"
    "v_cvt_pk_bf16_f32 v44, v20, v21\n"
    "v_cvt_pk_bf16_f32 v45, v22, v23\n"
    "v_mfma_f32_16x16x32_bf16 v[92:95], a[172:175], a[108:111], v[92:95]\n"
    "v_cvt_pk_bf16_f32 v46, v28, v29\n"
    "v_cvt_pk_bf16_f32 v47, v30, v31\n"
    "v_mfma_f32_16x16x32_bf16 v[92:95], a[168:171], a[104:107], v[92:95]\n"
    "s_mul_i32 s97, s99, 0\n"
    "v_cvt_pk_bf16_f32 v48, v48, v49\n"
    "v_mfma_f32_16x16x32_bf16 v[100:103], a[168:171], a[112:115], v[100:103]\n"
    "v_cvt_pk_bf16_f32 v49, v50, v51\n"
    "v_cvt_pk_bf16_f32 v50, v52, v53\n"
    "v_mfma_f32_16x16x32_bf16 v[100:103], a[172:175], a[116:119], v[100:103]\n"
    "s_add_u32 s97, s97, 0\n"
    "v_cvt_pk_bf16_f32 v51, v54, v55\n"
    "v_mfma_f32_16x16x32_bf16 v[108:111], a[172:175], a[124:127], v[108:111]\n"
    "v_cvt_pk_bf16_f32 v56, v56, v57\n"
    "v_cvt_pk_bf16_f32 v57, v58, v59\n"
    "v_mfma_f32_16x16x32_bf16 v[108:111], a[168:171], a[120:123], v[108:111]\n"
    "v_cvt_pk_bf16_f32 v58, v60, v61\n"
    "v_cvt_pk_bf16_f32 v59, v62, v63\n"
    "v_mfma_f32_16x16x32_bf16 v[116:119], a[168:171], a[128:131], v[116:119]\n"
    "v_cvt_pk_bf16_f32 v64, v64, v65\n"
    "v_cvt_pk_bf16_f32 v65, v66, v67\n"
    "v_mfma_f32_16x16x32_bf16 v[116:119], a[172:175], a[132:135], v[116:119]\n"
    "v_cvt_pk_bf16_f32 v66, v68, v69\n"
    "v_cvt_pk_bf16_f32 v67, v70, v71\n"
    "v_mfma_f32_16x16x32_bf16 v[124:127], a[172:175], a[140:143], v[124:127]\n"
    "v_cvt_pk_bf16_f32 v72, v72, v73\n"
    "v_cvt_pk_bf16_f32 v73, v74, v75\n"
    "v_mfma_f32_16x16x32_bf16 v[124:127], a[168:171], a[136:139], v[124:127]\n"
    "v_cvt_pk_bf16_f32 v74, v76, v77\n"
    "s_waitcnt lgkmcnt(6)\n"
    "v_mfma_f32_16x16x32_bf16 v[128:131], a[176:179], a[80:83], v[128:131]\n"
    "v_cvt_pk_bf16_f32 v75, v78, v79\n"
    "v_cvt_pk_bf16_f32 v80, v80, v81\n"
    "v_mfma_f32_16x16x32_bf16 v[128:131], a[180:183], a[84:87], v[128:131]\n"
    "v_cvt_pk_bf16_f32 v81, v82, v83\n"
    "v_cvt_pk_bf16_f32 v82, v84, v85\n"
    "v_mfma_f32_16x16x32_bf16 v[136:139], a[180:183], a[92:95], v[136:139]\n"
    "s_waitcnt vmcnt(1) lgkmcnt(0)\n"
    "s_barrier\n"
    "v_mfma_f32_16x16x32_bf16 v[136:139], a[176:179], a[88:91], v[136:139]\n"
    "ds_read_b128 a[16:19], v8 offset:0\n"
    "s_add_u32 m0, s68, 32768\n"
    "v_mfma_f32_16x16x32_bf16 v[144:147], a[176:179], a[96:99], v[144:147]\n"
    "ds_read_b64_tr_b16 a[32:33], v12 offset:0\n"
    "v_cvt_pk_bf16_f32 v83, v86, v87\n"
    "v_mfma_f32_16x16x32_bf16 v[144:147], a[180:183], a[100:103], v[144:147]\n"
    "buffer_load_dwordx4 v2, s[36:39], s80 offen offset:0 lds\n"
    "v_mfma_f32_16x16x32_bf16 v[152:155], a[180:183], a[108:111], v[152:155]\n"
    "ds_read_b64_tr_b16 a[34:35], v12 offset:2048\n"
    "v_cvt_pk_bf16_f32 v88, v88, v89\n"
    "v_mfma_f32_16x16x32_bf16 v[152:155], a[176:179], a[104:107], v[152:155]\n"
    "buffer_store_dwordx4 v[36:39], v14, s[48:51], s97 offen sc1\n"
    "v_mfma_f32_16x16x32_bf16 v[160:163], a[176:179], a[112:115], v[160:163]\n"
    "ds_read_b64_tr_b16 a[36:37], v13 offset:0\n"
    "v_cvt_pk_bf16_f32 v89, v90, v91\n"
    "v_mfma_f32_16x16x32_bf16 v[160:163], a[180:183], a[116:119], v[160:163]\n"
    "buffer_load_dwordx4 v3, s[36:39], s81 offen offset:1024 lds\n"
    "v_mfma_f32_16x16x32_bf16 v[168:171], a[180:183], a[124:127], v[168:171]\n"
    "ds_read_b64_tr_b16 a[38:39], v13 offset:2048\n"
    "v_cvt_pk_bf16_f32 v90, v92, v93\n"
    "v_mfma_f32_16x16x32_bf16 v[168:171], a[176:179], a[120:123], v[168:171]\n"
    "ds_read_b64_tr_b16 a[40:41], v12 offset:256\n"
    "v_cvt_pk_bf16_f32 v91, v94, v95\n"
    "v_mfma_f32_16x16x32_bf16 v[176:179], a[176:179], a[128:131], v[176:179]\n"
    "buffer_load_dwordx4 v2, s[36:39], s82 offen offset:2048 lds\n"
    "v_mfma_f32_16x16x32_bf16 v[176:179], a[180:183], a[132:135], v[176:179]\n"
    "ds_read_b64_tr_b16 a[42:43], v12 offset:2304\n"
    "v_cvt_pk_bf16_f32 v96, v96, v97\n"
    "v_mfma_f32_16x16x32_bf16 v[184:187], a[180:183], a[140:143], v[184:187]\n"
    "buffer_load_dwordx4 v3, s[36:39], s83 offen offset:3072 lds\n"
    "v_mfma_f32_16x16x32_bf16 v[184:187], a[176:179], a[136:139], v[184:187]\n"
    "ds_read_b64_tr_b16 a[44:45], v13 offset:256\n"
    "v_mfma_f32_16x16x32_bf16 v[132:135], a[184:187], a[80:83], v[132:135]\n"
    "ds_read_b64_tr_b16 a[46:47], v13 offset:2304\n"
    "s_add_u32 m0, s69, 32768\n"
    "v_mfma_f32_16x16x32_bf16 v[132:135], a[188:191], a[84:87], v[132:135]\n"
    "ds_read_b64_tr_b16 a[48:49], v12 offset:512\n"
    "v_cvt_pk_bf16_f32 v97, v98, v99\n"
    "v_mfma_f32_16x16x32_bf16 v[140:143], a[188:191], a[92:95], v[140:143]\n"
    "ds_read_b64_tr_b16 a[50:51], v12 offset:2560\n"
    "v_cvt_pk_bf16_f32 v98, v100, v101\n"
    "v_mfma_f32_16x16x32_bf16 v[140:143], a[184:187], a[88:91], v[140:143]\n"
    "buffer_load_dwordx4 v4, s[40:43], s84 offen offset:0 sc1 lds\n"
    "v_mfma_f32_16x16x32_bf16 v[148:151], a[184:187], a[96:99], v[148:151]\n"
    "ds_read_b64_tr_b16 a[52:53], v13 offset:512\n"
    "s_mul_i32 s97, s99, 0\n"
    "v_mfma_f32_16x16x32_bf16 v[148:151], a[188:191], a[100:103], v[148:151]\n"
    "buffer_load_dwordx4 v4, s[40:43], s85 offen offset:1024 sc1 lds\n"
    "v_mfma_f32_16x16x32_bf16 v[156:159], a[188:191], a[108:111], v[156:159]\n"
    "ds_read_b64_tr_b16 a[54:55], v13 offset:2560\n"
    "v_cvt_pk_bf16_f32 v99, v102, v103\n"
    "v_mfma_f32_16x16x32_bf16 v[156:159], a[184:187], a[104:107], v[156:159]\n"
    "ds_read_b64_tr_b16 a[56:57], v12 offset:768\n"
    "v_cvt_pk_bf16_f32 v104, v104, v105\n"
    "v_mfma_f32_16x16x32_bf16 v[164:167], a[184:187], a[112:115], v[164:167]\n"
    "buffer_load_dwordx4 v4, s[40:43], s86 offen offset:2048 sc1 lds\n"
    "v_mfma_f32_16x16x32_bf16 v[164:167], a[188:191], a[116:119], v[164:167]\n"
    "ds_read_b64_tr_b16 a[58:59], v12 offset:2816\n"
    "v_cvt_pk_bf16_f32 v105, v106, v107\n"
    "v_mfma_f32_16x16x32_bf16 v[172:175], a[188:191], a[124:127], v[172:175]\n"
    "ds_read_b64_tr_b16 a[60:61], v13 offset:768\n"
    "v_add_u32 v2, 128, v2\n"
    "v_mfma_f32_16x16x32_bf16 v[172:175], a[184:187], a[120:123], v[172:175]\n"
    "ds_read_b64_tr_b16 a[62:63], v13 offset:2816\n"
    "v_cvt_pk_bf16_f32 v106, v108, v109\n"
    "v_mfma_f32_16x16x32_bf16 v[180:183], a[184:187], a[128:131], v[180:183]\n"
    "v_add_u32 v3, 128, v3\n"
    "v_cvt_pk_bf16_f32 v107, v110, v111\n"
    "v_mfma_f32_16x16x32_bf16 v[180:183], a[188:191], a[132:135], v[180:183]\n"
    "ds_read_b128 a[20:23], v8 offset:2048\n"
    "v_cvt_pk_bf16_f32 v112, v112, v113\n"
    "v_mfma_f32_16x16x32_bf16 v[188:191], a[188:191], a[140:143], v[188:191]\n"
    "v_cvt_pk_bf16_f32 v113, v114, v115\n"
    "v_cvt_pk_bf16_f32 v114, v116, v117\n"
    "v_mfma_f32_16x16x32_bf16 v[188:191], a[184:187], a[136:139], v[188:191]\n"
    "ds_read_b128 a[24:27], v9 offset:0\n"
    "v_mfma_f32_16x16x32_bf16 v[192:195], a[192:195], a[80:83], v[192:195]\n"
    "ds_read_b128 a[28:31], v9 offset:2048\n"
    "v_add_u32 v4, s74, v4\n"
    "v_mfma_f32_16x16x32_bf16 v[192:195], a[196:199], a[84:87], v[192:195]\n"
    "s_add_u32 s97, s97, 1024\n"
    "v_cvt_pk_bf16_f32 v115, v118, v119\n"
    "v_mfma_f32_16x16x32_bf16 v[200:203], a[196:199], a[92:95], v[200:203]\n"
    "ds_read_b64_tr_b16 a[64:65], v15 offset:0\n"
    "s_add_u32 m0, s70, 32768\n"
    "v_mfma_f32_16x16x32_bf16 v[200:203], a[192:195], a[88:91], v[200:203]\n"
    "v_cvt_pk_bf16_f32 v120, v120, v121\n"
    "v_cvt_pk_bf16_f32 v121, v122, v123\n"
    "v_mfma_f32_16x16x32_bf16 v[208:211], a[192:195], a[96:99], v[208:211]\n"
    "ds_read_b64_tr_b16 a[66:67], v15 offset:256\n"
    "v_cvt_pk_bf16_f32 v122, v124, v125\n"
    "v_mfma_f32_16x16x32_bf16 v[208:211], a[196:199], a[100:103], v[208:211]\n"
    "ds_read_b64_tr_b16 a[68:69], v5 offset:0\n"
    "v_cvt_pk_bf16_f32 v123, v126, v127\n"
    "v_mfma_f32_16x16x32_bf16 v[216:219], a[196:199], a[108:111], v[216:219]\n"
    "v_cvt_pk_bf16_f32 v128, v128, v129\n"
    "v_cvt_pk_bf16_f32 v129, v130, v131\n"
    "v_mfma_f32_16x16x32_bf16 v[216:219], a[192:195], a[104:107], v[216:219]\n"
    "buffer_load_dwordx4 v6, s[44:47], s88 offen offset:0 lds\n"
    "v_mfma_f32_16x16x32_bf16 v[224:227], a[192:195], a[112:115], v[224:227]\n"
    "ds_read_b64_tr_b16 a[70:71], v5 offset:256\n"
    "v_cvt_pk_bf16_f32 v130, v132, v133\n"
    "v_mfma_f32_16x16x32_bf16 v[224:227], a[196:199], a[116:119], v[224:227]\n"
    "ds_read_b64_tr_b16 a[72:73], v15 offset:1024\n"
    "v_cvt_pk_bf16_f32 v131, v134, v135\n"
    "v_mfma_f32_16x16x32_bf16 v[232:235], a[196:199], a[124:127], v[232:235]\n"
    "ds_read_b64_tr_b16 a[74:75], v15 offset:1280\n"
    "v_cvt_pk_bf16_f32 v136, v136, v137\n"
    "v_mfma_f32_16x16x32_bf16 v[232:235], a[192:195], a[120:123], v[232:235]\n"
    "v_cvt_pk_bf16_f32 v137, v138, v139\n"
    "v_cvt_pk_bf16_f32 v138, v140, v141\n"
    "v_mfma_f32_16x16x32_bf16 v[240:243], a[192:195], a[128:131], v[240:243]\n"
    "buffer_load_dwordx4 v6, s[44:47], s89 offen offset:1024 lds\n"
    "v_mfma_f32_16x16x32_bf16 v[240:243], a[196:199], a[132:135], v[240:243]\n"
    "ds_read_b64_tr_b16 a[76:77], v5 offset:1024\n"
    "v_cvt_pk_bf16_f32 v139, v142, v143\n"
    "v_mfma_f32_16x16x32_bf16 v[248:251], a[196:199], a[140:143], v[248:251]\n"
    "buffer_store_dwordx4 v[44:47], v14, s[48:51], s97 offen sc1\n"
    "v_mfma_f32_16x16x32_bf16 v[248:251], a[192:195], a[136:139], v[248:251]\n"
    "ds_read_b64_tr_b16 a[78:79], v5 offset:1280\n"
    "v_mfma_f32_16x16x32_bf16 v[196:199], a[200:203], a[80:83], v[196:199]\n"
    "ds_read_b64_tr_b16 a[144:145], v15 offset:2048\n"
    "v_cvt_pk_bf16_f32 v144, v144, v145\n"
    "v_mfma_f32_16x16x32_bf16 v[196:199], a[204:207], a[84:87], v[196:199]\n"
    "buffer_load_dwordx4 v6, s[44:47], s90 offen offset:2048 lds\n"
    "v_mfma_f32_16x16x32_bf16 v[204:207], a[204:207], a[92:95], v[204:207]\n"
    "ds_read_b64_tr_b16 a[146:147], v15 offset:2304\n"
    "v_cvt_pk_bf16_f32 v145, v146, v147\n"
    "v_mfma_f32_16x16x32_bf16 v[204:207], a[200:203], a[88:91], v[204:207]\n"
    "ds_read_b64_tr_b16 a[148:149], v5 offset:2048\n"
    "v_cvt_pk_bf16_f32 v146, v148, v149\n"
    "v_mfma_f32_16x16x32_bf16 v[212:215], a[200:203], a[96:99], v[212:215]\n"
    "ds_read_b64_tr_b16 a[150:151], v5 offset:2304\n"
    "v_cvt_pk_bf16_f32 v147, v150, v151\n"
    "v_mfma_f32_16x16x32_bf16 v[212:215], a[204:207], a[100:103], v[212:215]\n"
    "v_cvt_pk_bf16_f32 v152, v152, v153\n"
    "v_cvt_pk_bf16_f32 v153, v154, v155\n"
    "v_mfma_f32_16x16x32_bf16 v[220:223], a[204:207], a[108:111], v[220:223]\n"
    "buffer_load_dwordx4 v6, s[44:47], s91 offen offset:3072 lds\n"
    "v_mfma_f32_16x16x32_bf16 v[220:223], a[200:203], a[104:107], v[220:223]\n"
    "ds_read_b128 a[152:155], v0 offset:0\n"
    "v_cvt_pk_bf16_f32 v154, v156, v157\n"
    "v_mfma_f32_16x16x32_bf16 v[228:231], a[200:203], a[112:115], v[228:231]\n"
    "v_cvt_pk_bf16_f32 v155, v158, v159\n"
    "v_cvt_pk_bf16_f32 v160, v160, v161\n"
    "v_mfma_f32_16x16x32_bf16 v[228:231], a[204:207], a[116:119], v[228:231]\n"
    "v_cvt_pk_bf16_f32 v161, v162, v163\n"
    "v_cvt_pk_bf16_f32 v162, v164, v165\n"
    "v_mfma_f32_16x16x32_bf16 v[236:239], a[204:207], a[124:127], v[236:239]\n"
    "v_cvt_pk_bf16_f32 v163, v166, v167\n"
    "v_cvt_pk_bf16_f32 v168, v168, v169\n"
    "v_mfma_f32_16x16x32_bf16 v[236:239], a[200:203], a[120:123], v[236:239]\n"
    "v_cvt_pk_bf16_f32 v169, v170, v171\n"
    "v_cvt_pk_bf16_f32 v170, v172, v173\n"
    "v_mfma_f32_16x16x32_bf16 v[244:247], a[200:203], a[128:131], v[244:247]\n"
    "v_cvt_pk_bf16_f32 v171, v174, v175\n"
    "v_cvt_pk_bf16_f32 v176, v176, v177\n"
    "v_mfma_f32_16x16x32_bf16 v[244:247], a[204:207], a[132:135], v[244:247]\n"
    "v_cvt_pk_bf16_f32 v177, v178, v179\n"
    "v_cvt_pk_bf16_f32 v178, v180, v181\n"
    "v_mfma_f32_16x16x32_bf16 v[252:255], a[204:207], a[140:143], v[252:255]\n"
    "v_cvt_pk_bf16_f32 v179, v182, v183\n"
    "v_cvt_pk_bf16_f32 v184, v184, v185\n"
    "v_mfma_f32_16x16x32_bf16 v[252:255], a[200:203], a[136:139], v[252:255]\n"
    "s_nop 7\n"
    "v_cvt_pk_bf16_f32 v185, v186, v187\n"
    "v_cvt_pk_bf16_f32 v186, v188, v189\n"
    "v_cvt_pk_bf16_f32 v187, v190, v191\n"
    "v_cvt_pk_bf16_f32 v192, v192, v193\n"
    "v_cvt_pk_bf16_f32 v193, v194, v195\n"
    "v_cvt_pk_bf16_f32 v194, v196, v197\n"
    "v_cvt_pk_bf16_f32 v195, v198, v199\n"
    "v_cvt_pk_bf16_f32 v200, v200, v201\n"
    "v_cvt_pk_bf16_f32 v201, v202, v203\n"
    "v_cvt_pk_bf16_f32 v202, v204, v205\n"
    "v_cvt_pk_bf16_f32 v203, v206, v207\n"
    "v_cvt_pk_bf16_f32 v208, v208, v209\n"
    "v_cvt_pk_bf16_f32 v209, v210, v211\n"
    "v_cvt_pk_bf16_f32 v210, v212, v213\n"
    "v_cvt_pk_bf16_f32 v211, v214, v215\n"
    "v_cvt_pk_bf16_f32 v216, v216, v217\n"
    "v_cvt_pk_bf16_f32 v217, v218, v219\n"
    "v_cvt_pk_bf16_f32 v218, v220, v221\n"
    "v_cvt_pk_bf16_f32 v219, v222, v223\n"
    "v_cvt_pk_bf16_f32 v224, v224, v225\n"
    "v_cvt_pk_bf16_f32 v225, v226, v227\n"
    "v_cvt_pk_bf16_f32 v226, v228, v229\n"
    "v_cvt_pk_bf16_f32 v227, v230, v231\n"
    "v_cvt_pk_bf16_f32 v232, v232, v233\n"
    "v_cvt_pk_bf16_f32 v233, v234, v235\n"
    "v_cvt_pk_bf16_f32 v234, v236, v237\n"
    "v_cvt_pk_bf16_f32 v235, v238, v239\n"
    "v_cvt_pk_bf16_f32 v240, v240, v241\n"
    "v_cvt_pk_bf16_f32 v241, v242, v243\n"
    "v_cvt_pk_bf16_f32 v242, v244, v245\n"
    "v_cvt_pk_bf16_f32 v243, v246, v247\n"
    "v_cvt_pk_bf16_f32 v248, v248, v249\n"
    "v_cvt_pk_bf16_f32 v249, v250, v251\n"
    "v_cvt_pk_bf16_f32 v250, v252, v253\n"
    "v_cvt_pk_bf16_f32 v251, v254, v255\n"
    ".p2align 3\n"
    ".Lk1_u2:\n"
    "s_nop 1\n"
    "s_waitcnt lgkmcnt(15)\n"
    "v_mfma_f32_16x16x32_bf16 v[20:23], a[32:35], a[16:19], 0\n"
    "ds_read_b128 a[80:83], v10 offset:0\n"
    "v_mfma_f32_16x16x32_bf16 v[28:31], a[36:39], a[16:19], 0\n"
    "ds_read_b64_tr_b16 a[96:97], v12 offset:16384\n"
    "s_add_u32 m0, s70, 36864\n"
    "v_mfma_f32_16x16x32_bf16 v[36:39], a[40:43], a[16:19], 0\n"
    "ds_read_b64_tr_b16 a[98:99], v12 offset:18432\n"
    "v_mfma_f32_16x16x32_bf16 v[44:47], a[44:47], a[16:19], 0\n"
    "ds_read_b64_tr_b16 a[100:101], v13 offset:16384\n"
    "v_mfma_f32_16x16x32_bf16 v[52:55], a[48:51], a[16:19], 0\n"
    "buffer_load_dwordx4 v7, s[44:47], s92 offen offset:0 lds\n"
    "v_mfma_f32_16x16x32_bf16 v[60:63], a[52:55], a[16:19], 0\n"
    "ds_read_b64_tr_b16 a[102:103], v13 offset:18432\n"
    "v_mfma_f32_16x16x32_bf16 v[68:71], a[56:59], a[16:19], 0\n"
    "buffer_load_dwordx4 v7, s[44:47], s93 offen offset:1024 lds\n"
    "v_mfma_f32_16x16x32_bf16 v[76:79], a[60:63], a[16:19], 0\n"
    "ds_read_b64_tr_b16 a[104:105], v12 offset:16640\n"
    "v_mfma_f32_16x16x32_bf16 v[84:87], a[32:35], a[20:23], 0\n"
    "ds_read_b64_tr_b16 a[106:107], v12 offset:18688\n"
    "v_mfma_f32_16x16x32_bf16 v[92:95], a[36:39], a[20:23], 0\n"
    "ds_read_b64_tr_b16 a[108:109], v13 offset:16640\n"
    "v_mfma_f32_16x16x32_bf16 v[100:103], a[40:43], a[20:23], 0\n"
    "buffer_load_dwordx4 v7, s[44:47], s94 offen offset:2048 lds\n"
    "v_mfma_f32_16x16x32_bf16 v[108:111], a[44:47], a[20:23], 0\n"
    "ds_read_b64_tr_b16 a[110:111], v13 offset:18688\n"
    "v_mfma_f32_16x16x32_bf16 v[116:119], a[48:51], a[20:23], 0\n"
    "buffer_load_dwordx4 v7, s[44:47], s95 offen offset:3072 lds\n"
    "v_mfma_f32_16x16x32_bf16 v[124:127], a[52:55], a[20:23], 0\n"
    "ds_read_b64_tr_b16 a[112:113], v12 offset:16896\n"
    "v_add_u32 v6, 0x80000, v6\n"
    "v_mfma_f32_16x16x32_bf16 v[132:135], a[56:59], a[20:23], 0\n"
    "ds_read_b64_tr_b16 a[114:115], v12 offset:18944\n"
    "v_mfma_f32_16x16x32_bf16 v[140:143], a[60:63], a[20:23], 0\n"
    "ds_read_b64_tr_b16 a[116:117], v13 offset:16896\n"
    "s_waitcnt lgkmcnt(15)\n"
    "v_mfma_f32_16x16x32_bf16 v[148:151], a[32:35], a[24:27], 0\n"
    "ds_read_b64_tr_b16 a[118:119], v13 offset:18944\n"
    "v_add_u32 v7, 0x80000, v7\n"
    "v_mfma_f32_16x16x32_bf16 v[156:159], a[36:39], a[24:27], 0\n"
    "ds_read_b64_tr_b16 a[120:121], v12 offset:17152\n"
    "s_add_u32 m0, s70, 65536\n"
    "v_mfma_f32_16x16x32_bf16 v[164:167], a[40:43], a[24:27], 0\n"
    "ds_read_b64_tr_b16 a[122:123], v12 offset:19200\n"
    "v_mfma_f32_16x16x32_bf16 v[172:175], a[44:47], a[24:27], 0\n"
    "ds_read_b64_tr_b16 a[124:125], v13 offset:17152\n"
    "v_mfma_f32_16x16x32_bf16 v[180:183], a[48:51], a[24:27], 0\n"
    "buffer_load_dwordx4 v6, s[44:47], s88 offen offset:0 lds\n"
    "v_mfma_f32_16x16x32_bf16 v[188:191], a[52:55], a[24:27], 0\n"
    "ds_read_b64_tr_b16 a[126:127], v13 offset:19200\n"
    "s_mul_i32 s97, s99, 0\n"
    "v_mfma_f32_16x16x32_bf16 v[196:199], a[56:59], a[24:27], 0\n"
    "ds_read_b128 a[84:87], v10 offset:2048\n"
    "s_add_u32 s97, s97, 2048\n"
    "v_mfma_f32_16x16x32_bf16 v[204:207], a[60:63], a[24:27], 0\n"
    "ds_read_b128 a[88:91], v11 offset:0\n"
    "v_mfma_f32_16x16x32_bf16 v[212:215], a[32:35], a[28:31], 0\n"
    "buffer_load_dwordx4 v6, s[44:47], s89 offen offset:1024 lds\n"
    "v_mfma_f32_16x16x32_bf16 v[220:223], a[36:39], a[28:31], 0\n"
    "buffer_store_dwordx4 v[16:19], v14, s[48:51], s97 offen sc1\n"
    "v_mfma_f32_16x16x32_bf16 v[228:231], a[40:43], a[28:31], 0\n"
    "ds_read_b128 a[92:95], v11 offset:2048\n"
    "v_mfma_f32_16x16x32_bf16 v[236:239], a[44:47], a[28:31], 0\n"
    "buffer_load_dwordx4 v6, s[44:47], s90 offen offset:2048 lds\n"
    "v_mfma_f32_16x16x32_bf16 v[244:247], a[48:51], a[28:31], 0\n"
    "ds_read_b64_tr_b16 a[128:129], v15 offset:8192\n"
    "v_mfma_f32_16x16x32_bf16 v[252:255], a[52:55], a[28:31], 0\n"
    "ds_read_b64_tr_b16 a[130:131], v15 offset:8448\n"
    "v_mfma_f32_16x16x32_bf16 a[0:3], a[56:59], a[28:31], 0\n"
    "buffer_load_dwordx4 v6, s[44:47], s91 offen offset:3072 lds\n"
    "v_mfma_f32_16x16x32_bf16 a[4:7], a[60:63], a[28:31], 0\n"
    "ds_read_b64_tr_b16 a[132:133], v5 offset:8192\n"
    "v_mfma_f32_16x16x32_bf16 a[168:171], a[64:67], a[16:19], 0\n"
    "ds_read_b64_tr_b16 a[134:135], v5 offset:8448\n"
    "s_add_u32 m0, s70, 69632\n"
    "v_mfma_f32_16x16x32_bf16 a[172:175], a[68:71], a[16:19], 0\n"
    "ds_read_b64_tr_b16 a[136:137], v15 offset:9216\n"
    "v_mfma_f32_16x16x32_bf16 a[176:179], a[64:67], a[20:23], 0\n"
    "ds_read_b64_tr_b16 a[138:139], v15 offset:9472\n"
    "v_mfma_f32_16x16x32_bf16 a[180:183], a[68:71], a[20:23], 0\n"
    "buffer_load_dwordx4 v7, s[44:47], s92 offen offset:0 lds\n"
    "v_mfma_f32_16x16x32_bf16 a[184:187], a[16:19], a[72:75], 0\n"
    "ds_read_b64_tr_b16 a[140:141], v5 offset:9216\n"
    "v_mfma_f32_16x16x32_bf16 a[188:191], a[20:23], a[72:75], 0\n"
    "ds_read_b64_tr_b16 a[142:143], v5 offset:9472\n"
    "v_mfma_f32_16x16x32_bf16 a[192:195], a[16:19], a[76:79], 0\n"
    "buffer_load_dwordx4 v7, s[44:47], s93 offen offset:1024 lds\n"
    "v_mfma_f32_16x16x32_bf16 a[196:199], a[20:23], a[76:79], 0\n"
    "ds_read_b64_tr_b16 a[156:157], v15 offset:10240\n"
    "v_mfma_f32_16x16x32_bf16 a[200:203], a[16:19], a[144:147], 0\n"
    "ds_read_b64_tr_b16 a[158:159], v15 offset:10496\n"
    "v_mfma_f32_16x16x32_bf16 a[204:207], a[20:23], a[144:147], 0\n"
    "buffer_load_dwordx4 v7, s[44:47], s94 offen offset:2048 lds\n"
    "s_waitcnt lgkmcnt(15)\n"
    "v_mfma_f32_16x16x32_bf16 a[208:211], a[16:19], a[148:151], 0\n"
    "ds_read_b64_tr_b16 a[160:161], v5 offset:10240\n"
    "v_mfma_f32_16x16x32_bf16 a[212:215], a[20:23], a[148:151], 0\n"
    "buffer_load_dwordx4 v7, s[44:47], s95 offen offset:3072 lds\n"
    "v_mfma_f32_16x16x32_bf16 a[8:11], a[152:155], a[16:19], 0\n"
    "ds_read_b64_tr_b16 a[162:163], v5 offset:10496\n"
    "v_add_u32 v6, 0x80000, v6\n"
    "v_mfma_f32_16x16x32_bf16 a[12:15], a[152:155], a[20:23], 0\n"
    "ds_read_b128 a[164:167], v0 offset:64\n"
    "v_mfma_f32_16x16x32_bf16 v[20:23], a[96:99], a[80:83], v[20:23]\n"
    "v_add_u32 v7, 0x80000, v7\n"
    "v_mfma_f32_16x16x32_bf16 v[28:31], a[100:103], a[80:83], v[28:31]\n"
    "v_mfma_f32_16x16x32_bf16 v[36:39], a[104:107], a[80:83], v[36:39]\n"
    "s_waitcnt vmcnt(9) lgkmcnt(0)\n"
    "s_barrier\n"
    "v_mfma_f32_16x16x32_bf16 v[44:47], a[108:111], a[80:83], v[44:47]\n"
    "ds_read_b128 a[16:19], v8 offset:32768\n"
    "v_mfma_f32_16x16x32_bf16 v[52:55], a[112:115], a[80:83], v[52:55]\n"
    "ds_read_b64_tr_b16 a[32:33], v12 offset:32768\n"
    "s_add_u32 m0, s68, 0\n"
    "v_mfma_f32_16x16x32_bf16 v[60:63], a[116:119], a[80:83], v[60:63]\n"
    "ds_read_b64_tr_b16 a[34:35], v12 offset:34816\n"
    "v_mfma_f32_16x16x32_bf16 v[68:71], a[120:123], a[80:83], v[68:71]\n"
    "ds_read_b64_tr_b16 a[36:37], v13 offset:32768\n"
    "v_mfma_f32_16x16x32_bf16 v[76:79], a[124:127], a[80:83], v[76:79]\n"
    "ds_read_b64_tr_b16 a[38:39], v13 offset:34816\n"
    "v_mfma_f32_16x16x32_bf16 v[84:87], a[96:99], a[84:87], v[84:87]\n"
    "buffer_load_dwordx4 v2, s[36:39], s80 offen offset:0 lds\n"
    "v_mfma_f32_16x16x32_bf16 v[92:95], a[100:103], a[84:87], v[92:95]\n"
    "ds_read_b64_tr_b16 a[40:41], v12 offset:33024\n"
    "v_mfma_f32_16x16x32_bf16 v[100:103], a[104:107], a[84:87], v[100:103]\n"
    "ds_read_b64_tr_b16 a[42:43], v12 offset:35072\n"
    "v_mfma_f32_16x16x32_bf16 v[108:111], a[108:111], a[84:87], v[108:111]\n"
    "buffer_load_dwordx4 v3, s[36:39], s81 offen offset:1024 lds\n"
    "v_mfma_f32_16x16x32_bf16 v[116:119], a[112:115], a[84:87], v[116:119]\n"
    "ds_read_b64_tr_b16 a[44:45], v13 offset:33024\n"
    "v_mfma_f32_16x16x32_bf16 v[124:127], a[116:119], a[84:87], v[124:127]\n"
    "ds_read_b64_tr_b16 a[46:47], v13 offset:35072\n"
    "v_mfma_f32_16x16x32_bf16 v[132:135], a[120:123], a[84:87], v[132:135]\n"
    "buffer_load_dwordx4 v2, s[36:39], s82 offen offset:2048 lds\n"
    "v_mfma_f32_16x16x32_bf16 v[140:143], a[124:127], a[84:87], v[140:143]\n"
    "ds_read_b64_tr_b16 a[48:49], v12 offset:33280\n"
    "v_mfma_f32_16x16x32_bf16 v[148:151], a[96:99], a[88:91], v[148:151]\n"
    "ds_read_b64_tr_b16 a[50:51], v12 offset:35328\n"
    "v_mfma_f32_16x16x32_bf16 v[156:159], a[100:103], a[88:91], v[156:159]\n"
    "ds_read_b64_tr_b16 a[52:53], v13 offset:33280\n"
    "v_mfma_f32_16x16x32_bf16 v[164:167], a[104:107], a[88:91], v[164:167]\n"
    "buffer_load_dwordx4 v3, s[36:39], s83 offen offset:3072 lds\n"
    "v_mfma_f32_16x16x32_bf16 v[172:175], a[108:111], a[88:91], v[172:175]\n"
    "ds_read_b64_tr_b16 a[54:55], v13 offset:35328\n"
    "s_add_u32 m0, s69, 0\n"
    "v_mfma_f32_16x16x32_bf16 v[180:183], a[112:115], a[88:91], v[180:183]\n"
    "ds_read_b64_tr_b16 a[56:57], v12 offset:33536\n"
    "v_mfma_f32_16x16x32_bf16 v[188:191], a[116:119], a[88:91], v[188:191]\n"
    "ds_read_b64_tr_b16 a[58:59], v12 offset:35584\n"
    "s_mul_i32 s97, s99, 0\n"
    "v_mfma_f32_16x16x32_bf16 v[196:199], a[120:123], a[88:91], v[196:199]\n"
    "ds_read_b64_tr_b16 a[60:61], v13 offset:33536\n"
    "s_add_u32 s97, s97, 3072\n"
    "v_mfma_f32_16x16x32_bf16 v[204:207], a[124:127], a[88:91], v[204:207]\n"
    "ds_read_b64_tr_b16 a[62:63], v13 offset:35584\n"
    "v_mfma_f32_16x16x32_bf16 v[212:215], a[96:99], a[92:95], v[212:215]\n"
    "buffer_load_dwordx4 v4, s[40:43], s84 offen offset:0 sc1 lds\n"
    "v_mfma_f32_16x16x32_bf16 v[220:223], a[100:103], a[92:95], v[220:223]\n"
    "buffer_store_dwordx4 v[24:27], v14, s[48:51], s97 offen sc1\n"
    "v_mfma_f32_16x16x32_bf16 v[228:231], a[104:107], a[92:95], v[228:231]\n"
    "ds_read_b128 a[20:23], v8 offset:34816\n"
    "v_mfma_f32_16x16x32_bf16 v[236:239], a[108:111], a[92:95], v[236:239]\n"
    "ds_read_b128 a[24:27], v9 offset:32768\n"
    "v_mfma_f32_16x16x32_bf16 v[244:247], a[112:115], a[92:95], v[244:247]\n"
    "buffer_load_dwordx4 v4, s[40:43], s85 offen offset:1024 sc1 lds\n"
    "v_mfma_f32_16x16x32_bf16 v[252:255], a[116:119], a[92:95], v[252:255]\n"
    "ds_read_b128 a[28:31], v9 offset:34816\n"
    "v_mfma_f32_16x16x32_bf16 a[0:3], a[120:123], a[92:95], a[0:3]\n"
    "ds_read_b64_tr_b16 a[64:65], v15 offset:32768\n"
    "v_mfma_f32_16x16x32_bf16 a[4:7], a[124:127], a[92:95], a[4:7]\n"
    "ds_read_b64_tr_b16 a[66:67], v15 offset:33024\n"
    "v_mfma_f32_16x16x32_bf16 a[168:171], a[128:131], a[80:83], a[168:171]\n"
    "buffer_load_dwordx4 v4, s[40:43], s86 offen offset:2048 sc1 lds\n"
    "v_mfma_f32_16x16x32_bf16 a[172:175], a[132:135], a[80:83], a[172:175]\n"
    "ds_read_b64_tr_b16 a[68:69], v5 offset:32768\n"
    "v_add_u32 v2, 128, v2\n"
    "v_mfma_f32_16x16x32_bf16 a[176:179], a[128:131], a[84:87], a[176:179]\n"
    "ds_read_b64_tr_b16 a[70:71], v5 offset:33024\n"
    "v_add_u32 v3, 128, v3\n"
    "v_mfma_f32_16x16x32_bf16 a[180:183], a[132:135], a[84:87], a[180:183]\n"
    "ds_read_b64_tr_b16 a[72:73], v15 offset:33792\n"
    "v_mfma_f32_16x16x32_bf16 a[184:187], a[80:83], a[136:139], a[184:187]\n"
    "ds_read_b64_tr_b16 a[74:75], v15 offset:34048\n"
    "v_mfma_f32_16x16x32_bf16 a[188:191], a[84:87], a[136:139], a[188:191]\n"
    "ds_read_b64_tr_b16 a[76:77], v5 offset:33792\n"
    "v_add_u32 v4, s74, v4\n"
    "v_mfma_f32_16x16x32_bf16 a[192:195], a[80:83], a[140:143], a[192:195]\n"
    "ds_read_b64_tr_b16 a[78:79], v5 offset:34048\n"
    "v_mfma_f32_16x16x32_bf16 a[196:199], a[84:87], a[140:143], a[196:199]\n"
    "ds_read_b64_tr_b16 a[144:145], v15 offset:34816\n"
    "v_mfma_f32_16x16x32_bf16 a[200:203], a[80:83], a[156:159], a[200:203]\n"
    "ds_read_b64_tr_b16 a[146:147], v15 offset:35072\n"
    "s_add_u32 m0, s70, 0\n"
    "v_mfma_f32_16x16x32_bf16 a[204:207], a[84:87], a[156:159], a[204:207]\n"
    "ds_read_b64_tr_b16 a[148:149], v5 offset:34816\n"
    "v_mfma_f32_16x16x32_bf16 a[208:211], a[80:83], a[160:163], a[208:211]\n"
    "ds_read_b64_tr_b16 a[150:151], v5 offset:35072\n"
    "v_mfma_f32_16x16x32_bf16 a[212:215], a[84:87], a[160:163], a[212:215]\n"
    "ds_read_b128 a[152:155], v0 offset:128\n"
    "v_mfma_f32_16x16x32_bf16 a[8:11], a[164:167], a[80:83], a[8:11]\n"
    "buffer_load_dwordx4 v6, s[44:47], s88 offen offset:0 lds\n"
    "v_mfma_f32_16x16x32_bf16 a[12:15], a[164:167], a[84:87], a[12:15]\n"
    "s_waitcnt lgkmcnt(15)\n"
    "v_mfma_f32_16x16x32_bf16 v[20:23], a[32:35], a[16:19], v[20:23]\n"
    "ds_read_b128 a[80:83], v10 offset:32768\n"
    "v_mfma_f32_16x16x32_bf16 v[28:31], a[36:39], a[16:19], v[28:31]\n"
    "ds_read_b64_tr_b16 a[96:97], v12 offset:49152\n"
    "v_mfma_f32_16x16x32_bf16 v[36:39], a[40:43], a[16:19], v[36:39]\n"
    "ds_read_b64_tr_b16 a[98:99], v12 offset:51200\n"
    "v_mfma_f32_16x16x32_bf16 v[44:47], a[44:47], a[16:19], v[44:47]\n"
    "buffer_load_dwordx4 v6, s[44:47], s89 offen offset:1024 lds\n"
    "v_mfma_f32_16x16x32_bf16 v[52:55], a[48:51], a[16:19], v[52:55]\n"
    "ds_read_b64_tr_b16 a[100:101], v13 offset:49152\n"
    "v_mfma_f32_16x16x32_bf16 v[60:63], a[52:55], a[16:19], v[60:63]\n"
    "ds_read_b64_tr_b16 a[102:103], v13 offset:51200\n"
    "v_mfma_f32_16x16x32_bf16 v[68:71], a[56:59], a[16:19], v[68:71]\n"
    "ds_read_b64_tr_b16 a[104:105], v12 offset:49408\n"
    "v_mfma_f32_16x16x32_bf16 v[76:79], a[60:63], a[16:19], v[76:79]\n"
    "ds_read_b64_tr_b16 a[106:107], v12 offset:51456\n"
    "v_mfma_f32_16x16x32_bf16 v[84:87], a[32:35], a[20:23], v[84:87]\n"
    "buffer_load_dwordx4 v6, s[44:47], s90 offen offset:2048 lds\n"
    "v_mfma_f32_16x16x32_bf16 v[92:95], a[36:39], a[20:23], v[92:95]\n"
    "ds_read_b64_tr_b16 a[108:109], v13 offset:49408\n"
    "v_mfma_f32_16x16x32_bf16 v[100:103], a[40:43], a[20:23], v[100:103]\n"
    "ds_read_b64_tr_b16 a[110:111], v13 offset:51456\n"
    "v_mfma_f32_16x16x32_bf16 v[108:111], a[44:47], a[20:23], v[108:111]\n"
    "ds_read_b64_tr_b16 a[112:113], v12 offset:49664\n"
    "v_mfma_f32_16x16x32_bf16 v[116:119], a[48:51], a[20:23], v[116:119]\n"
    "buffer_load_dwordx4 v6, s[44:47], s91 offen offset:3072 lds\n"
    "v_mfma_f32_16x16x32_bf16 v[124:127], a[52:55], a[20:23], v[124:127]\n"
    "ds_read_b64_tr_b16 a[114:115], v12 offset:51712\n"
    "v_mfma_f32_16x16x32_bf16 v[132:135], a[56:59], a[20:23], v[132:135]\n"
    "ds_read_b64_tr_b16 a[116:117], v13 offset:49664\n"
    "v_mfma_f32_16x16x32_bf16 v[140:143], a[60:63], a[20:23], v[140:143]\n"
    "ds_read_b64_tr_b16 a[118:119], v13 offset:51712\n"
    "s_waitcnt lgkmcnt(15)\n"
    "v_mfma_f32_16x16x32_bf16 v[148:151], a[32:35], a[24:27], v[148:151]\n"
    "ds_read_b64_tr_b16 a[120:121], v12 offset:49920\n"
    "s_add_u32 m0, s70, 4096\n"
    "v_mfma_f32_16x16x32_bf16 v[156:159], a[36:39], a[24:27], v[156:159]\n"
    "ds_read_b64_tr_b16 a[122:123], v12 offset:51968\n"
    "v_mfma_f32_16x16x32_bf16 v[164:167], a[40:43], a[24:27], v[164:167]\n"
    "v_mfma_f32_16x16x32_bf16 v[172:175], a[44:47], a[24:27], v[172:175]\n"
    "ds_read_b64_tr_b16 a[124:125], v13 offset:49920\n"
    "v_mfma_f32_16x16x32_bf16 v[180:183], a[48:51], a[24:27], v[180:183]\n"
    "ds_read_b64_tr_b16 a[126:127], v13 offset:51968\n"
    "v_mfma_f32_16x16x32_bf16 v[188:191], a[52:55], a[24:27], v[188:191]\n"
    "buffer_load_dwordx4 v7, s[44:47], s92 offen offset:0 lds\n"
    "v_mfma_f32_16x16x32_bf16 v[196:199], a[56:59], a[24:27], v[196:199]\n"
    "ds_read_b128 a[84:87], v10 offset:34816\n"
    "s_mul_i32 s97, s99, -4096\n"
    "v_mfma_f32_16x16x32_bf16 v[204:207], a[60:63], a[24:27], v[204:207]\n"
    "s_add_u32 s97, s97, 8192\n"
    "v_mfma_f32_16x16x32_bf16 v[212:215], a[32:35], a[28:31], v[212:215]\n"
    "ds_read_b128 a[88:91], v11 offset:32768\n"
    "v_mfma_f32_16x16x32_bf16 v[220:223], a[36:39], a[28:31], v[220:223]\n"
    "buffer_store_dwordx4 v[32:35], v14, s[48:51], s97 offen sc1\n"
    "v_mfma_f32_16x16x32_bf16 v[228:231], a[40:43], a[28:31], v[228:231]\n"
    "ds_read_b128 a[92:95], v11 offset:34816\n"
    "v_mfma_f32_16x16x32_bf16 v[236:239], a[44:47], a[28:31], v[236:239]\n"
    "buffer_load_dwordx4 v7, s[44:47], s93 offen offset:1024 lds\n"
    "v_mfma_f32_16x16x32_bf16 v[244:247], a[48:51], a[28:31], v[244:247]\n"
    "ds_read_b64_tr_b16 a[128:129], v15 offset:40960\n"
    "v_mfma_f32_16x16x32_bf16 v[252:255], a[52:55], a[28:31], v[252:255]\n"
    "ds_read_b64_tr_b16 a[130:131], v15 offset:41216\n"
    "v_mfma_f32_16x16x32_bf16 a[0:3], a[56:59], a[28:31], a[0:3]\n"
    "ds_read_b64_tr_b16 a[132:133], v5 offset:40960\n"
    "v_mfma_f32_16x16x32_bf16 a[4:7], a[60:63], a[28:31], a[4:7]\n"
    "ds_read_b64_tr_b16 a[134:135], v5 offset:41216\n"
    "v_mfma_f32_16x16x32_bf16 a[168:171], a[64:67], a[16:19], a[168:171]\n"
    "buffer_load_dwordx4 v7, s[44:47], s94 offen offset:2048 lds\n"
    "v_mfma_f32_16x16x32_bf16 a[172:175], a[68:71], a[16:19], a[172:175]\n"
    "ds_read_b64_tr_b16 a[136:137], v15 offset:41984\n"
    "v_mfma_f32_16x16x32_bf16 a[176:179], a[64:67], a[20:23], a[176:179]\n"
    "ds_read_b64_tr_b16 a[138:139], v15 offset:42240\n"
    "v_mfma_f32_16x16x32_bf16 a[180:183], a[68:71], a[20:23], a[180:183]\n"
    "ds_read_b64_tr_b16 a[140:141], v5 offset:41984\n"
    "v_mfma_f32_16x16x32_bf16 a[184:187], a[16:19], a[72:75], a[184:187]\n"
    "buffer_load_dwordx4 v7, s[44:47], s95 offen offset:3072 lds\n"
    "v_mfma_f32_16x16x32_bf16 a[188:191], a[20:23], a[72:75], a[188:191]\n"
    "ds_read_b64_tr_b16 a[142:143], v5 offset:42240\n"
    "v_add_u32 v6, 0x80000, v6\n"
    "v_mfma_f32_16x16x32_bf16 a[192:195], a[16:19], a[76:79], a[192:195]\n"
    "ds_read_b64_tr_b16 a[156:157], v15 offset:43008\n"
    "v_mfma_f32_16x16x32_bf16 a[196:199], a[20:23], a[76:79], a[196:199]\n"
    "ds_read_b64_tr_b16 a[158:159], v15 offset:43264\n"
    "v_mfma_f32_16x16x32_bf16 a[200:203], a[16:19], a[144:147], a[200:203]\n"
    "ds_read_b64_tr_b16 a[160:161], v5 offset:43008\n"
    "v_mfma_f32_16x16x32_bf16 a[204:207], a[20:23], a[144:147], a[204:207]\n"
    "ds_read_b64_tr_b16 a[162:163], v5 offset:43264\n"
    "v_add_u32 v7, 0x80000, v7\n"
    "s_waitcnt lgkmcnt(15)\n"
    "v_mfma_f32_16x16x32_bf16 a[208:211], a[16:19], a[148:151], a[208:211]\n"
    "ds_read_b128 a[164:167], v0 offset:192\n"
    "v_mfma_f32_16x16x32_bf16 a[212:215], a[20:23], a[148:151], a[212:215]\n"
    "v_mfma_f32_16x16x32_bf16 a[8:11], a[152:155], a[16:19], a[8:11]\n"
    "v_mfma_f32_16x16x32_bf16 a[12:15], a[152:155], a[20:23], a[12:15]\n"
    "v_mfma_f32_16x16x32_bf16 v[20:23], a[96:99], a[80:83], v[20:23]\n"
    "v_mfma_f32_16x16x32_bf16 v[28:31], a[100:103], a[80:83], v[28:31]\n"
    "v_mfma_f32_16x16x32_bf16 v[36:39], a[104:107], a[80:83], v[36:39]\n"
    "s_waitcnt vmcnt(9) lgkmcnt(0)\n"
    "s_barrier\n"
    "v_mfma_f32_16x16x32_bf16 v[44:47], a[108:111], a[80:83], v[44:47]\n"
    "ds_read_b128 a[16:19], v8 offset:0\n"
    "v_mfma_f32_16x16x32_bf16 v[52:55], a[112:115], a[80:83], v[52:55]\n"
    "v_add_u32 v12, 0x10000, v12\n"
    "s_add_u32 m0, s68, 32768\n"
    "v_mfma_f32_16x16x32_bf16 v[60:63], a[116:119], a[80:83], v[60:63]\n"
    "v_add_u32 v13, 0x10000, v13\n"
    "v_mfma_f32_16x16x32_bf16 v[68:71], a[120:123], a[80:83], v[68:71]\n"
    "ds_read_b64_tr_b16 a[32:33], v12 offset:0\n"
    "v_mfma_f32_16x16x32_bf16 v[76:79], a[124:127], a[80:83], v[76:79]\n"
    "ds_read_b64_tr_b16 a[34:35], v12 offset:2048\n"
    "v_mfma_f32_16x16x32_bf16 v[84:87], a[96:99], a[84:87], v[84:87]\n"
    "buffer_load_dwordx4 v2, s[36:39], s80 offen offset:0 lds\n"
    "v_mfma_f32_16x16x32_bf16 v[92:95], a[100:103], a[84:87], v[92:95]\n"
    "ds_read_b64_tr_b16 a[36:37], v13 offset:0\n"
    "v_mfma_f32_16x16x32_bf16 v[100:103], a[104:107], a[84:87], v[100:103]\n"
    "ds_read_b64_tr_b16 a[38:39], v13 offset:2048\n"
    "v_mfma_f32_16x16x32_bf16 v[108:111], a[108:111], a[84:87], v[108:111]\n"
    "buffer_load_dwordx4 v3, s[36:39], s81 offen offset:1024 lds\n"
    "v_mfma_f32_16x16x32_bf16 v[116:119], a[112:115], a[84:87], v[116:119]\n"
    "ds_read_b64_tr_b16 a[40:41], v12 offset:256\n"
    "v_mfma_f32_16x16x32_bf16 v[124:127], a[116:119], a[84:87], v[124:127]\n"
    "ds_read_b64_tr_b16 a[42:43], v12 offset:2304\n"
    "v_mfma_f32_16x16x32_bf16 v[132:135], a[120:123], a[84:87], v[132:135]\n"
    "ds_read_b64_tr_b16 a[44:45], v13 offset:256\n"
    "v_mfma_f32_16x16x32_bf16 v[140:143], a[124:127], a[84:87], v[140:143]\n"
    "ds_read_b64_tr_b16 a[46:47], v13 offset:2304\n"
    "v_mfma_f32_16x16x32_bf16 v[148:151], a[96:99], a[88:91], v[148:151]\n"
    "buffer_load_dwordx4 v2, s[36:39], s82 offen offset:2048 lds\n"
    "v_mfma_f32_16x16x32_bf16 v[156:159], a[100:103], a[88:91], v[156:159]\n"
    "ds_read_b64_tr_b16 a[48:49], v12 offset:512\n"
    "v_mfma_f32_16x16x32_bf16 v[164:167], a[104:107], a[88:91], v[164:167]\n"
    "buffer_load_dwordx4 v3, s[36:39], s83 offen offset:3072 lds\n"
    "v_mfma_f32_16x16x32_bf16 v[172:175], a[108:111], a[88:91], v[172:175]\n"
    "ds_read_b64_tr_b16 a[50:51], v12 offset:2560\n"
    "s_add_u32 m0, s69, 32768\n"
    "v_mfma_f32_16x16x32_bf16 v[180:183], a[112:115], a[88:91], v[180:183]\n"
    "ds_read_b64_tr_b16 a[52:53], v13 offset:512\n"
    "v_mfma_f32_16x16x32_bf16 v[188:191], a[116:119], a[88:91], v[188:191]\n"
    "ds_read_b64_tr_b16 a[54:55], v13 offset:2560\n"
    "s_mul_i32 s97, s99, -4096\n"
    "v_mfma_f32_16x16x32_bf16 v[196:199], a[120:123], a[88:91], v[196:199]\n"
    "ds_read_b64_tr_b16 a[56:57], v12 offset:768\n"
    "s_add_u32 s97, s97, 9216\n"
    "v_mfma_f32_16x16x32_bf16 v[204:207], a[124:127], a[88:91], v[204:207]\n"
    "ds_read_b64_tr_b16 a[58:59], v12 offset:2816\n"
    "v_mfma_f32_16x16x32_bf16 v[212:215], a[96:99], a[92:95], v[212:215]\n"
    "ds_read_b64_tr_b16 a[60:61], v13 offset:768\n"
    "v_mfma_f32_16x16x32_bf16 v[220:223], a[100:103], a[92:95], v[220:223]\n"
    "buffer_load_dwordx4 v4, s[40:43], s84 offen offset:0 sc1 lds\n"
    "v_mfma_f32_16x16x32_bf16 v[228:231], a[104:107], a[92:95], v[228:231]\n"
    "buffer_store_dwordx4 v[40:43], v14, s[48:51], s97 offen sc1\n"
    "v_mfma_f32_16x16x32_bf16 v[236:239], a[108:111], a[92:95], v[236:239]\n"
    "ds_read_b64_tr_b16 a[62:63], v13 offset:2816\n"
    "v_mfma_f32_16x16x32_bf16 v[244:247], a[112:115], a[92:95], v[244:247]\n"
    "ds_read_b128 a[20:23], v8 offset:2048\n"
    "v_mfma_f32_16x16x32_bf16 v[252:255], a[116:119], a[92:95], v[252:255]\n"
    "buffer_load_dwordx4 v4, s[40:43], s85 offen offset:1024 sc1 lds\n"
    "v_mfma_f32_16x16x32_bf16 a[0:3], a[120:123], a[92:95], a[0:3]\n"
    "ds_read_b128 a[24:27], v9 offset:0\n"
    "v_mfma_f32_16x16x32_bf16 a[4:7], a[124:127], a[92:95], a[4:7]\n"
    "ds_read_b128 a[28:31], v9 offset:2048\n"
    "v_mfma_f32_16x16x32_bf16 a[168:171], a[128:131], a[80:83], a[168:171]\n"
    "ds_read_b64_tr_b16 a[64:65], v15 offset:0\n"
    "v_mfma_f32_16x16x32_bf16 a[172:175], a[132:135], a[80:83], a[172:175]\n"
    "buffer_load_dwordx4 v4, s[40:43], s86 offen offset:2048 sc1 lds\n"
    "v_mfma_f32_16x16x32_bf16 a[176:179], a[128:131], a[84:87], a[176:179]\n"
    "ds_read_b64_tr_b16 a[66:67], v15 offset:256\n"
    "v_add_u32 v2, 128, v2\n"
    "v_mfma_f32_16x16x32_bf16 a[180:183], a[132:135], a[84:87], a[180:183]\n"
    "ds_read_b64_tr_b16 a[68:69], v5 offset:0\n"
    "v_add_u32 v3, 128, v3\n"
    "v_mfma_f32_16x16x32_bf16 a[184:187], a[80:83], a[136:139], a[184:187]\n"
    "ds_read_b64_tr_b16 a[70:71], v5 offset:256\n"
    "v_mfma_f32_16x16x32_bf16 a[188:191], a[84:87], a[136:139], a[188:191]\n"
    "ds_read_b64_tr_b16 a[72:73], v15 offset:1024\n"
    "v_add_u32 v4, s74, v4\n"
    "v_mfma_f32_16x16x32_bf16 a[192:195], a[80:83], a[140:143], a[192:195]\n"
    "ds_read_b64_tr_b16 a[74:75], v15 offset:1280\n"
    "v_mfma_f32_16x16x32_bf16 a[196:199], a[84:87], a[140:143], a[196:199]\n"
    "ds_read_b64_tr_b16 a[76:77], v5 offset:1024\n"
    "v_mfma_f32_16x16x32_bf16 a[200:203], a[80:83], a[156:159], a[200:203]\n"
    "ds_read_b64_tr_b16 a[78:79], v5 offset:1280\n"
    "s_add_u32 m0, s70, 32768\n"
    "v_mfma_f32_16x16x32_bf16 a[204:207], a[84:87], a[156:159], a[204:207]\n"
    "ds_read_b64_tr_b16 a[144:145], v15 offset:2048\n"
    "v_mfma_f32_16x16x32_bf16 a[208:211], a[80:83], a[160:163], a[208:211]\n"
    "ds_read_b64_tr_b16 a[146:147], v15 offset:2304\n"
    "v_mfma_f32_16x16x32_bf16 a[212:215], a[84:87], a[160:163], a[212:215]\n"
    "ds_read_b64_tr_b16 a[148:149], v5 offset:2048\n"
    "v_mfma_f32_16x16x32_bf16 a[8:11], a[164:167], a[80:83], a[8:11]\n"
    "ds_read_b64_tr_b16 a[150:151], v5 offset:2304\n"
    "v_mfma_f32_16x16x32_bf16 a[12:15], a[164:167], a[84:87], a[12:15]\n"
    "ds_read_b128 a[152:155], v0 offset:256\n"
    "s_waitcnt lgkmcnt(15)\n"
    "v_mfma_f32_16x16x32_bf16 v[20:23], a[32:35], a[16:19], v[20:23]\n"
    "buffer_load_dwordx4 v6, s[44:47], s88 offen offset:0 lds\n"
    "v_mfma_f32_16x16x32_bf16 v[28:31], a[36:39], a[16:19], v[28:31]\n"
    "ds_read_b128 a[80:83], v10 offset:0\n"
    "v_mfma_f32_16x16x32_bf16 v[36:39], a[40:43], a[16:19], v[36:39]\n"
    "ds_read_b64_tr_b16 a[96:97], v12 offset:16384\n"
    "v_mfma_f32_16x16x32_bf16 v[44:47], a[44:47], a[16:19], v[44:47]\n"
    "ds_read_b64_tr_b16 a[98:99], v12 offset:18432\n"
    "v_mfma_f32_16x16x32_bf16 v[52:55], a[48:51], a[16:19], v[52:55]\n"
    "buffer_load_dwordx4 v6, s[44:47], s89 offen offset:1024 lds\n"
    "v_mfma_f32_16x16x32_bf16 v[60:63], a[52:55], a[16:19], v[60:63]\n"
    "ds_read_b64_tr_b16 a[100:101], v13 offset:16384\n"
    "v_mfma_f32_16x16x32_bf16 v[68:71], a[56:59], a[16:19], v[68:71]\n"
    "ds_read_b64_tr_b16 a[102:103], v13 offset:18432\n"
    "v_mfma_f32_16x16x32_bf16 v[76:79], a[60:63], a[16:19], v[76:79]\n"
    "ds_read_b64_tr_b16 a[104:105], v12 offset:16640\n"
    "v_mfma_f32_16x16x32_bf16 v[84:87], a[32:35], a[20:23], v[84:87]\n"
    "ds_read_b64_tr_b16 a[106:107], v12 offset:18688\n"
    "v_mfma_f32_16x16x32_bf16 v[92:95], a[36:39], a[20:23], v[92:95]\n"
    "buffer_load_dwordx4 v6, s[44:47], s90 offen offset:2048 lds\n"
    "v_mfma_f32_16x16x32_bf16 v[100:103], a[40:43], a[20:23], v[100:103]\n"
    "ds_read_b64_tr_b16 a[108:109], v13 offset:16640\n"
    "v_mfma_f32_16x16x32_bf16 v[108:111], a[44:47], a[20:23], v[108:111]\n"
    "ds_read_b64_tr_b16 a[110:111], v13 offset:18688\n"
    "v_mfma_f32_16x16x32_bf16 v[116:119], a[48:51], a[20:23], v[116:119]\n"
    "ds_read_b64_tr_b16 a[112:113], v12 offset:16896\n"
    "v_mfma_f32_16x16x32_bf16 v[124:127], a[52:55], a[20:23], v[124:127]\n"
    "buffer_load_dwordx4 v6, s[44:47], s91 offen offset:3072 lds\n"
    "v_mfma_f32_16x16x32_bf16 v[132:135], a[56:59], a[20:23], v[132:135]\n"
    "ds_read_b64_tr_b16 a[114:115], v12 offset:18944\n"
    "v_mfma_f32_16x16x32_bf16 v[140:143], a[60:63], a[20:23], v[140:143]\n"
    "ds_read_b64_tr_b16 a[116:117], v13 offset:16896\n"
    "s_waitcnt lgkmcnt(15)\n"
    "v_mfma_f32_16x16x32_bf16 v[148:151], a[32:35], a[24:27], v[148:151]\n"
    "ds_read_b64_tr_b16 a[118:119], v13 offset:18944\n"
    "s_add_u32 m0, s70, 36864\n"
    "v_mfma_f32_16x16x32_bf16 v[156:159], a[36:39], a[24:27], v[156:159]\n"
    "ds_read_b64_tr_b16 a[120:121], v12 offset:17152\n"
    "v_mfma_f32_16x16x32_bf16 v[164:167], a[40:43], a[24:27], v[164:167]\n"
    "ds_read_b64_tr_b16 a[122:123], v12 offset:19200\n"
    "v_mfma_f32_16x16x32_bf16 v[172:175], a[44:47], a[24:27], v[172:175]\n"
    "ds_read_b64_tr_b16 a[124:125], v13 offset:17152\n"
    "v_mfma_f32_16x16x32_bf16 v[180:183], a[48:51], a[24:27], v[180:183]\n"
    "ds_read_b64_tr_b16 a[126:127], v13 offset:19200\n"
    "v_mfma_f32_16x16x32_bf16 v[188:191], a[52:55], a[24:27], v[188:191]\n"
    "buffer_load_dwordx4 v7, s[44:47], s92 offen offset:0 lds\n"
    "v_mfma_f32_16x16x32_bf16 v[196:199], a[56:59], a[24:27], v[196:199]\n"
    "ds_read_b128 a[84:87], v10 offset:2048\n"
    "s_mul_i32 s97, s99, -4096\n"
    "v_mfma_f32_16x16x32_bf16 v[204:207], a[60:63], a[24:27], v[204:207]\n"
    "s_add_u32 s97, s97, 10240\n"
    "v_mfma_f32_16x16x32_bf16 v[212:215], a[32:35], a[28:31], v[212:215]\n"
    "ds_read_b128 a[88:91], v11 offset:0\n"
    "v_mfma_f32_16x16x32_bf16 v[220:223], a[36:39], a[28:31], v[220:223]\n"
    "buffer_store_dwordx4 v[48:51], v14, s[48:51], s97 offen sc1\n"
    "v_mfma_f32_16x16x32_bf16 v[228:231], a[40:43], a[28:31], v[228:231]\n"
    "ds_read_b128 a[92:95], v11 offset:2048\n"
    "v_mfma_f32_16x16x32_bf16 v[236:239], a[44:47], a[28:31], v[236:239]\n"
    "buffer_load_dwordx4 v7, s[44:47], s93 offen offset:1024 lds\n"
    "v_mfma_f32_16x16x32_bf16 v[244:247], a[48:51], a[28:31], v[244:247]\n"
    "ds_read_b64_tr_b16 a[128:129], v15 offset:8192\n"
    "v_mfma_f32_16x16x32_bf16 v[252:255], a[52:55], a[28:31], v[252:255]\n"
    "ds_read_b64_tr_b16 a[130:131], v15 offset:8448\n"
    "v_mfma_f32_16x16x32_bf16 a[0:3], a[56:59], a[28:31], a[0:3]\n"
    "ds_read_b64_tr_b16 a[132:133], v5 offset:8192\n"
    "v_mfma_f32_16x16x32_bf16 a[4:7], a[60:63], a[28:31], a[4:7]\n"
    "ds_read_b64_tr_b16 a[134:135], v5 offset:8448\n"
    "v_mfma_f32_16x16x32_bf16 a[168:171], a[64:67], a[16:19], a[168:171]\n"
    "buffer_load_dwordx4 v7, s[44:47], s94 offen offset:2048 lds\n"
    "v_mfma_f32_16x16x32_bf16 a[172:175], a[68:71], a[16:19], a[172:175]\n"
    "ds_read_b64_tr_b16 a[136:137], v15 offset:9216\n"
    "v_mfma_f32_16x16x32_bf16 a[176:179], a[64:67], a[20:23], a[176:179]\n"
    "ds_read_b64_tr_b16 a[138:139], v15 offset:9472\n"
    "v_mfma_f32_16x16x32_bf16 a[180:183], a[68:71], a[20:23], a[180:183]\n"
    "ds_read_b64_tr_b16 a[140:141], v5 offset:9216\n"
    "v_mfma_f32_16x16x32_bf16 a[184:187], a[16:19], a[72:75], a[184:187]\n"
    "buffer_load_dwordx4 v7, s[44:47], s95 offen offset:3072 lds\n"
    "v_mfma_f32_16x16x32_bf16 a[188:191], a[20:23], a[72:75], a[188:191]\n"
    "ds_read_b64_tr_b16 a[142:143], v5 offset:9472\n"
    "v_add_u32 v6, 0x80000, v6\n"
    "v_mfma_f32_16x16x32_bf16 a[192:195], a[16:19], a[76:79], a[192:195]\n"
    "ds_read_b64_tr_b16 a[156:157], v15 offset:10240\n"
    "v_mfma_f32_16x16x32_bf16 a[196:199], a[20:23], a[76:79], a[196:199]\n"
    "ds_read_b64_tr_b16 a[158:159], v15 offset:10496\n"
    "v_mfma_f32_16x16x32_bf16 a[200:203], a[16:19], a[144:147], a[200:203]\n"
    "ds_read_b64_tr_b16 a[160:161], v5 offset:10240\n"
    "v_mfma_f32_16x16x32_bf16 a[204:207], a[20:23], a[144:147], a[204:207]\n"
    "ds_read_b64_tr_b16 a[162:163], v5 offset:10496\n"
    "v_add_u32 v7, 0x80000, v7\n"
    "s_waitcnt lgkmcnt(15)\n"
    "v_mfma_f32_16x16x32_bf16 a[208:211], a[16:19], a[148:151], a[208:211]\n"
    "ds_read_b128 a[164:167], v0 offset:320\n"
    "v_mfma_f32_16x16x32_bf16 a[212:215], a[20:23], a[148:151], a[212:215]\n"
    "v_mfma_f32_16x16x32_bf16 a[8:11], a[152:155], a[16:19], a[8:11]\n"
    "v_mfma_f32_16x16x32_bf16 a[12:15], a[152:155], a[20:23], a[12:15]\n"
    "v_mfma_f32_16x16x32_bf16 v[20:23], a[96:99], a[80:83], v[20:23]\n"
    "v_mfma_f32_16x16x32_bf16 v[28:31], a[100:103], a[80:83], v[28:31]\n"
    "v_mfma_f32_16x16x32_bf16 v[36:39], a[104:107], a[80:83], v[36:39]\n"
    "s_waitcnt vmcnt(9) lgkmcnt(0)\n"
    "s_barrier\n"
    "v_mfma_f32_16x16x32_bf16 v[44:47], a[108:111], a[80:83], v[44:47]\n"
    "ds_read_b128 a[16:19], v8 offset:32768\n"
    "v_mfma_f32_16x16x32_bf16 v[52:55], a[112:115], a[80:83], v[52:55]\n"
    "v_subrev_u32 v12, 0x10000, v12\n"
    "s_add_u32 m0, s68, 0\n"
    "v_mfma_f32_16x16x32_bf16 v[60:63], a[116:119], a[80:83], v[60:63]\n"
    "v_subrev_u32 v13, 0x10000, v13\n"
    "v_mfma_f32_16x16x32_bf16 v[68:71], a[120:123], a[80:83], v[68:71]\n"
    "ds_read_b64_tr_b16 a[32:33], v12 offset:0\n"
    "v_mfma_f32_16x16x32_bf16 v[76:79], a[124:127], a[80:83], v[76:79]\n"
    "ds_read_b64_tr_b16 a[34:35], v12 offset:2048\n"
    "v_mfma_f32_16x16x32_bf16 v[84:87], a[96:99], a[84:87], v[84:87]\n"
    "buffer_load_dwordx4 v2, s[36:39], s80 offen offset:0 lds\n"
    "v_mfma_f32_16x16x32_bf16 v[92:95], a[100:103], a[84:87], v[92:95]\n"
    "ds_read_b64_tr_b16 a[36:37], v13 offset:0\n"
    "v_mfma_f32_16x16x32_bf16 v[100:103], a[104:107], a[84:87], v[100:103]\n"
    "ds_read_b64_tr_b16 a[38:39], v13 offset:2048\n"
    "v_mfma_f32_16x16x32_bf16 v[108:111], a[108:111], a[84:87], v[108:111]\n"
    "buffer_load_dwordx4 v3, s[36:39], s81 offen offset:1024 lds\n"
    "v_mfma_f32_16x16x32_bf16 v[116:119], a[112:115], a[84:87], v[116:119]\n"
    "ds_read_b64_tr_b16 a[40:41], v12 offset:256\n"
    "v_mfma_f32_16x16x32_bf16 v[124:127], a[116:119], a[84:87], v[124:127]\n"
    "ds_read_b64_tr_b16 a[42:43], v12 offset:2304\n"
    "v_mfma_f32_16x16x32_bf16 v[132:135], a[120:123], a[84:87], v[132:135]\n"
    "ds_read_b64_tr_b16 a[44:45], v13 offset:256\n"
    "v_mfma_f32_16x16x32_bf16 v[140:143], a[124:127], a[84:87], v[140:143]\n"
    "ds_read_b64_tr_b16 a[46:47], v13 offset:2304\n"
    "v_mfma_f32_16x16x32_bf16 v[148:151], a[96:99], a[88:91], v[148:151]\n"
    "buffer_load_dwordx4 v2, s[36:39], s82 offen offset:2048 lds\n"
    "v_mfma_f32_16x16x32_bf16 v[156:159], a[100:103], a[88:91], v[156:159]\n"
    "ds_read_b64_tr_b16 a[48:49], v12 offset:512\n"
    "v_mfma_f32_16x16x32_bf16 v[164:167], a[104:107], a[88:91], v[164:167]\n"
    "buffer_load_dwordx4 v3, s[36:39], s83 offen offset:3072 lds\n"
    "v_mfma_f32_16x16x32_bf16 v[172:175], a[108:111], a[88:91], v[172:175]\n"
    "ds_read_b64_tr_b16 a[50:51], v12 offset:2560\n"
    "s_add_u32 m0, s69, 0\n"
    "v_mfma_f32_16x16x32_bf16 v[180:183], a[112:115], a[88:91], v[180:183]\n"
    "ds_read_b64_tr_b16 a[52:53], v13 offset:512\n"
    "v_mfma_f32_16x16x32_bf16 v[188:191], a[116:119], a[88:91], v[188:191]\n"
    "ds_read_b64_tr_b16 a[54:55], v13 offset:2560\n"
    "s_mul_i32 s97, s99, -4096\n"
    "v_mfma_f32_16x16x32_bf16 v[196:199], a[120:123], a[88:91], v[196:199]\n"
    "ds_read_b64_tr_b16 a[56:57], v12 offset:768\n"
    "s_add_u32 s97, s97, 11264\n"
    "v_mfma_f32_16x16x32_bf16 v[204:207], a[124:127], a[88:91], v[204:207]\n"
    "ds_read_b64_tr_b16 a[58:59], v12 offset:2816\n"
    "v_mfma_f32_16x16x32_bf16 v[212:215], a[96:99], a[92:95], v[212:215]\n"
    "ds_read_b64_tr_b16 a[60:61], v13 offset:768\n"
    "v_mfma_f32_16x16x32_bf16 v[220:223], a[100:103], a[92:95], v[220:223]\n"
    "buffer_load_dwordx4 v4, s[40:43], s84 offen offset:0 sc1 lds\n"
    "v_mfma_f32_16x16x32_bf16 v[228:231], a[104:107], a[92:95], v[228:231]\n"
    "buffer_store_dwordx4 v[56:59], v14, s[48:51], s97 offen sc1\n"
    "v_mfma_f32_16x16x32_bf16 v[236:239], a[108:111], a[92:95], v[236:239]\n"
    "ds_read_b64_tr_b16 a[62:63], v13 offset:2816\n"
    "v_mfma_f32_16x16x32_bf16 v[244:247], a[112:115], a[92:95], v[244:247]\n"
    "ds_read_b128 a[20:23], v8 offset:34816\n"
    "v_mfma_f32_16x16x32_bf16 v[252:255], a[116:119], a[92:95], v[252:255]\n"
    "buffer_load_dwordx4 v4, s[40:43], s85 offen offset:1024 sc1 lds\n"
    "v_mfma_f32_16x16x32_bf16 a[0:3], a[120:123], a[92:95], a[0:3]\n"
    "ds_read_b128 a[24:27], v9 offset:32768\n"
    "v_mfma_f32_16x16x32_bf16 a[4:7], a[124:127], a[92:95], a[4:7]\n"
    "ds_read_b128 a[28:31], v9 offset:34816\n"
    "v_mfma_f32_16x16x32_bf16 a[168:171], a[128:131], a[80:83], a[168:171]\n"
    "ds_read_b64_tr_b16 a[64:65], v15 offset:32768\n"
    "v_mfma_f32_16x16x32_bf16 a[172:175], a[132:135], a[80:83], a[172:175]\n"
    "buffer_load_dwordx4 v4, s[40:43], s86 offen offset:2048 sc1 lds\n"
    "v_mfma_f32_16x16x32_bf16 a[176:179], a[128:131], a[84:87], a[176:179]\n"
    "ds_read_b64_tr_b16 a[66:67], v15 offset:33024\n"
    "v_add_u32 v2, 128, v2\n"
    "v_mfma_f32_16x16x32_bf16 a[180:183], a[132:135], a[84:87], a[180:183]\n"
    "ds_read_b64_tr_b16 a[68:69], v5 offset:32768\n"
    "v_add_u32 v3, 128, v3\n"
    "v_mfma_f32_16x16x32_bf16 a[184:187], a[80:83], a[136:139], a[184:187]\n"
    "ds_read_b64_tr_b16 a[70:71], v5 offset:33024\n"
    "v_mfma_f32_16x16x32_bf16 a[188:191], a[84:87], a[136:139], a[188:191]\n"
    "ds_read_b64_tr_b16 a[72:73], v15 offset:33792\n"
    "v_add_u32 v4, s74, v4\n"
    "v_mfma_f32_16x16x32_bf16 a[192:195], a[80:83], a[140:143], a[192:195]\n"
    "ds_read_b64_tr_b16 a[74:75], v15 offset:34048\n"
    "v_mfma_f32_16x16x32_bf16 a[196:199], a[84:87], a[140:143], a[196:199]\n"
    "ds_read_b64_tr_b16 a[76:77], v5 offset:33792\n"
    "v_mfma_f32_16x16x32_bf16 a[200:203], a[80:83], a[156:159], a[200:203]\n"
    "ds_read_b64_tr_b16 a[78:79], v5 offset:34048\n"
    "s_add_u32 m0, s70, 65536\n"
    "v_mfma_f32_16x16x32_bf16 a[204:207], a[84:87], a[156:159], a[204:207]\n"
    "ds_read_b64_tr_b16 a[144:145], v15 offset:34816\n"
    "v_mfma_f32_16x16x32_bf16 a[208:211], a[80:83], a[160:163], a[208:211]\n"
    "ds_read_b64_tr_b16 a[146:147], v15 offset:35072\n"
    "v_mfma_f32_16x16x32_bf16 a[212:215], a[84:87], a[160:163], a[212:215]\n"
    "ds_read_b64_tr_b16 a[148:149], v5 offset:34816\n"
    "v_mfma_f32_16x16x32_bf16 a[8:11], a[164:167], a[80:83], a[8:11]\n"
    "ds_read_b64_tr_b16 a[150:151], v5 offset:35072\n"
    "v_mfma_f32_16x16x32_bf16 a[12:15], a[164:167], a[84:87], a[12:15]\n"
    "ds_read_b128 a[152:155], v0 offset:384\n"
    "s_waitcnt lgkmcnt(15)\n"
    "v_mfma_f32_16x16x32_bf16 v[20:23], a[32:35], a[16:19], v[20:23]\n"
    "buffer_load_dwordx4 v6, s[44:47], s88 offen offset:0 lds\n"
    "v_mfma_f32_16x16x32_bf16 v[28:31], a[36:39], a[16:19], v[28:31]\n"
    "ds_read_b128 a[80:83], v10 offset:32768\n"
    "v_mfma_f32_16x16x32_bf16 v[36:39], a[40:43], a[16:19], v[36:39]\n"
    "ds_read_b64_tr_b16 a[96:97], v12 offset:16384\n"
    "v_mfma_f32_16x16x32_bf16 v[44:47], a[44:47], a[16:19], v[44:47]\n"
    "ds_read_b64_tr_b16 a[98:99], v12 offset:18432\n"
    "v_mfma_f32_16x16x32_bf16 v[52:55], a[48:51], a[16:19], v[52:55]\n"
    "buffer_load_dwordx4 v6, s[44:47], s89 offen offset:1024 lds\n"
    "v_mfma_f32_16x16x32_bf16 v[60:63], a[52:55], a[16:19], v[60:63]\n"
    "ds_read_b64_tr_b16 a[100:101], v13 offset:16384\n"
    "v_mfma_f32_16x16x32_bf16 v[68:71], a[56:59], a[16:19], v[68:71]\n"
    "ds_read_b64_tr_b16 a[102:103], v13 offset:18432\n"
    "v_mfma_f32_16x16x32_bf16 v[76:79], a[60:63], a[16:19], v[76:79]\n"
    "ds_read_b64_tr_b16 a[104:105], v12 offset:16640\n"
    "v_mfma_f32_16x16x32_bf16 v[84:87], a[32:35], a[20:23], v[84:87]\n"
    "ds_read_b64_tr_b16 a[106:107], v12 offset:18688\n"
    "v_mfma_f32_16x16x32_bf16 v[92:95], a[36:39], a[20:23], v[92:95]\n"
    "buffer_load_dwordx4 v6, s[44:47], s90 offen offset:2048 lds\n"
    "v_mfma_f32_16x16x32_bf16 v[100:103], a[40:43], a[20:23], v[100:103]\n"
    "ds_read_b64_tr_b16 a[108:109], v13 offset:16640\n"
    "s_mul_i32 s97, s99, 0\n"
    "v_mfma_f32_16x16x32_bf16 v[108:111], a[44:47], a[20:23], v[108:111]\n"
    "ds_read_b64_tr_b16 a[110:111], v13 offset:18688\n"
    "s_add_u32 s97, s97, 16384\n"
    "v_mfma_f32_16x16x32_bf16 v[116:119], a[48:51], a[20:23], v[116:119]\n"
    "buffer_store_dwordx4 v[64:67], v14, s[48:51], s97 offen sc1\n"
    "v_mfma_f32_16x16x32_bf16 v[124:127], a[52:55], a[20:23], v[124:127]\n"
    "ds_read_b64_tr_b16 a[112:113], v12 offset:16896\n"
    "v_mfma_f32_16x16x32_bf16 v[132:135], a[56:59], a[20:23], v[132:135]\n"
    "buffer_load_dwordx4 v6, s[44:47], s91 offen offset:3072 lds\n"
    "v_mfma_f32_16x16x32_bf16 v[140:143], a[60:63], a[20:23], v[140:143]\n"
    "ds_read_b64_tr_b16 a[114:115], v12 offset:18944\n"
    "s_waitcnt lgkmcnt(15)\n"
    "v_mfma_f32_16x16x32_bf16 v[148:151], a[32:35], a[24:27], v[148:151]\n"
    "ds_read_b64_tr_b16 a[116:117], v13 offset:16896\n"
    "s_add_u32 m0, s70, 69632\n"
    "v_mfma_f32_16x16x32_bf16 v[156:159], a[36:39], a[24:27], v[156:159]\n"
    "ds_read_b64_tr_b16 a[118:119], v13 offset:18944\n"
    "v_mfma_f32_16x16x32_bf16 v[164:167], a[40:43], a[24:27], v[164:167]\n"
    "ds_read_b64_tr_b16 a[120:121], v12 offset:17152\n"
    "v_mfma_f32_16x16x32_bf16 v[172:175], a[44:47], a[24:27], v[172:175]\n"
    "ds_read_b64_tr_b16 a[122:123], v12 offset:19200\n"
    "v_mfma_f32_16x16x32_bf16 v[180:183], a[48:51], a[24:27], v[180:183]\n"
    "ds_read_b64_tr_b16 a[124:125], v13 offset:17152\n"
    "v_mfma_f32_16x16x32_bf16 v[188:191], a[52:55], a[24:27], v[188:191]\n"
    "ds_read_b64_tr_b16 a[126:127], v13 offset:19200\n"
    "v_mfma_f32_16x16x32_bf16 v[196:199], a[56:59], a[24:27], v[196:199]\n"
    "buffer_load_dwordx4 v7, s[44:47], s92 offen offset:0 lds\n"
    "v_mfma_f32_16x16x32_bf16 v[204:207], a[60:63], a[24:27], v[204:207]\n"
    "ds_read_b128 a[84:87], v10 offset:34816\n"
    "v_mfma_f32_16x16x32_bf16 v[212:215], a[32:35], a[28:31], v[212:215]\n"
    "ds_read_b128 a[88:91], v11 offset:32768\n"
    "v_mfma_f32_16x16x32_bf16 v[220:223], a[36:39], a[28:31], v[220:223]\n"
    "ds_read_b128 a[92:95], v11 offset:34816\n"
    "v_mfma_f32_16x16x32_bf16 v[228:231], a[40:43], a[28:31], v[228:231]\n"
    "buffer_load_dwordx4 v7, s[44:47], s93 offen offset:1024 lds\n"
    "v_mfma_f32_16x16x32_bf16 v[236:239], a[44:47], a[28:31], v[236:239]\n"
    "ds_read_b64_tr_b16 a[128:129], v15 offset:40960\n"
    "v_mfma_f32_16x16x32_bf16 v[244:247], a[48:51], a[28:31], v[244:247]\n"
    "ds_read_b64_tr_b16 a[130:131], v15 offset:41216\n"
    "v_mfma_f32_16x16x32_bf16 v[252:255], a[52:55], a[28:31], v[252:255]\n"
    "ds_read_b64_tr_b16 a[132:133], v5 offset:40960\n"
    "v_mfma_f32_16x16x32_bf16 a[0:3], a[56:59], a[28:31], a[0:3]\n"
    "buffer_load_dwordx4 v7, s[44:47], s94 offen offset:2048 lds\n"
    "v_mfma_f32_16x16x32_bf16 a[4:7], a[60:63], a[28:31], a[4:7]\n"
    "ds_read_b64_tr_b16 a[134:135], v5 offset:41216\n"
    "v_mfma_f32_16x16x32_bf16 a[168:171], a[64:67], a[16:19], a[168:171]\n"
    "ds_read_b64_tr_b16 a[136:137], v15 offset:41984\n"
    "s_mul_i32 s97, s99, 0\n"
    "v_mfma_f32_16x16x32_bf16 a[172:175], a[68:71], a[16:19], a[172:175]\n"
    "ds_read_b64_tr_b16 a[138:139], v15 offset:42240\n"
    "s_add_u32 s97, s97, 17408\n"
    "v_mfma_f32_16x16x32_bf16 a[176:179], a[64:67], a[20:23], a[176:179]\n"
    "ds_read_b64_tr_b16 a[140:141], v5 offset:41984\n"
    "v_mfma_f32_16x16x32_bf16 a[180:183], a[68:71], a[20:23], a[180:183]\n"
    "buffer_load_dwordx4 v7, s[44:47], s95 offen offset:3072 lds\n"
    "v_mfma_f32_16x16x32_bf16 a[184:187], a[16:19], a[72:75], a[184:187]\n"
    "buffer_store_dwordx4 v[72:75], v14, s[48:51], s97 offen sc1\n"
    "v_mfma_f32_16x16x32_bf16 a[188:191], a[20:23], a[72:75], a[188:191]\n"
    "ds_read_b64_tr_b16 a[142:143], v5 offset:42240\n"
    "v_add_u32 v6, 0x80000, v6\n"
    "v_mfma_f32_16x16x32_bf16 a[192:195], a[16:19], a[76:79], a[192:195]\n"
    "ds_read_b64_tr_b16 a[156:157], v15 offset:43008\n"
    "v_mfma_f32_16x16x32_bf16 a[196:199], a[20:23], a[76:79], a[196:199]\n"
    "ds_read_b64_tr_b16 a[158:159], v15 offset:43264\n"
    "s_waitcnt lgkmcnt(15)\n"
    "v_mfma_f32_16x16x32_bf16 a[200:203], a[16:19], a[144:147], a[200:203]\n"
    "ds_read_b64_tr_b16 a[160:161], v5 offset:43008\n"
    "v_mfma_f32_16x16x32_bf16 a[204:207], a[20:23], a[144:147], a[204:207]\n"
    "ds_read_b64_tr_b16 a[162:163], v5 offset:43264\n"
    "v_add_u32 v7, 0x80000, v7\n"
    "v_mfma_f32_16x16x32_bf16 a[208:211], a[16:19], a[148:151], a[208:211]\n"
    "ds_read_b128 a[164:167], v0 offset:448\n"
    "v_mfma_f32_16x16x32_bf16 a[212:215], a[20:23], a[148:151], a[212:215]\n"
    "v_mfma_f32_16x16x32_bf16 a[8:11], a[152:155], a[16:19], a[8:11]\n"
    "v_mfma_f32_16x16x32_bf16 a[12:15], a[152:155], a[20:23], a[12:15]\n"
    "v_mfma_f32_16x16x32_bf16 v[20:23], a[96:99], a[80:83], v[20:23]\n"
    "v_mfma_f32_16x16x32_bf16 v[28:31], a[100:103], a[80:83], v[28:31]\n"
    "v_mfma_f32_16x16x32_bf16 v[36:39], a[104:107], a[80:83], v[36:39]\n"
    "s_waitcnt vmcnt(10) lgkmcnt(0)\n"
    "s_barrier\n"
    "v_mfma_f32_16x16x32_bf16 v[44:47], a[108:111], a[80:83], v[44:47]\n"
    "ds_read_b128 a[16:19], v8 offset:0\n"
    "v_mfma_f32_16x16x32_bf16 v[52:55], a[112:115], a[80:83], v[52:55]\n"
    "ds_read_b64_tr_b16 a[32:33], v12 offset:32768\n"
    "s_add_u32 m0, s68, 32768\n"
    "v_mfma_f32_16x16x32_bf16 v[60:63], a[116:119], a[80:83], v[60:63]\n"
    "ds_read_b64_tr_b16 a[34:35], v12 offset:34816\n"
    "v_mfma_f32_16x16x32_bf16 v[68:71], a[120:123], a[80:83], v[68:71]\n"
    "ds_read_b64_tr_b16 a[36:37], v13 offset:32768\n"
    "v_mfma_f32_16x16x32_bf16 v[76:79], a[124:127], a[80:83], v[76:79]\n"
    "ds_read_b64_tr_b16 a[38:39], v13 offset:34816\n"
    "v_mfma_f32_16x16x32_bf16 v[84:87], a[96:99], a[84:87], v[84:87]\n"
    "buffer_load_dwordx4 v2, s[36:39], s80 offen offset:0 lds\n"
    "v_mfma_f32_16x16x32_bf16 v[92:95], a[100:103], a[84:87], v[92:95]\n"
    "ds_read_b64_tr_b16 a[40:41], v12 offset:33024\n"
    "s_mul_i32 s97, s99, 0\n"
    "v_mfma_f32_16x16x32_bf16 v[100:103], a[104:107], a[84:87], v[100:103]\n"
    "ds_read_b64_tr_b16 a[42:43], v12 offset:35072\n"
    "s_add_u32 s97, s97, 18432\n"
    "v_mfma_f32_16x16x32_bf16 v[108:111], a[108:111], a[84:87], v[108:111]\n"
    "buffer_load_dwordx4 v3, s[36:39], s81 offen offset:1024 lds\n"
    "v_mfma_f32_16x16x32_bf16 v[116:119], a[112:115], a[84:87], v[116:119]\n"
    "buffer_store_dwordx4 v[80:83], v14, s[48:51], s97 offen sc1\n"
    "v_mfma_f32_16x16x32_bf16 v[124:127], a[116:119], a[84:87], v[124:127]\n"
    "ds_read_b64_tr_b16 a[44:45], v13 offset:33024\n"
    "v_mfma_f32_16x16x32_bf16 v[132:135], a[120:123], a[84:87], v[132:135]\n"
    "ds_read_b64_tr_b16 a[46:47], v13 offset:35072\n"
    "v_mfma_f32_16x16x32_bf16 v[140:143], a[124:127], a[84:87], v[140:143]\n"
    "ds_read_b64_tr_b16 a[48:49], v12 offset:33280\n"
    "v_mfma_f32_16x16x32_bf16 v[148:151], a[96:99], a[88:91], v[148:151]\n"
    "buffer_load_dwordx4 v2, s[36:39], s82 offen offset:2048 lds\n"
    "v_mfma_f32_16x16x32_bf16 v[156:159], a[100:103], a[88:91], v[156:159]\n"
    "ds_read_b64_tr_b16 a[50:51], v12 offset:35328\n"
    "v_mfma_f32_16x16x32_bf16 v[164:167], a[104:107], a[88:91], v[164:167]\n"
    "ds_read_b64_tr_b16 a[52:53], v13 offset:33280\n"
    "v_mfma_f32_16x16x32_bf16 v[172:175], a[108:111], a[88:91], v[172:175]\n"
    "buffer_load_dwordx4 v3, s[36:39], s83 offen offset:3072 lds\n"
    "v_mfma_f32_16x16x32_bf16 v[180:183], a[112:115], a[88:91], v[180:183]\n"
    "ds_read_b64_tr_b16 a[54:55], v13 offset:35328\n"
    "s_add_u32 m0, s69, 32768\n"
    "v_mfma_f32_16x16x32_bf16 v[188:191], a[116:119], a[88:91], v[188:191]\n"
    "ds_read_b64_tr_b16 a[56:57], v12 offset:33536\n"
    "v_mfma_f32_16x16x32_bf16 v[196:199], a[120:123], a[88:91], v[196:199]\n"
    "ds_read_b64_tr_b16 a[58:59], v12 offset:35584\n"
    "v_mfma_f32_16x16x32_bf16 v[204:207], a[124:127], a[88:91], v[204:207]\n"
    "ds_read_b64_tr_b16 a[60:61], v13 offset:33536\n"
    "v_mfma_f32_16x16x32_bf16 v[212:215], a[96:99], a[92:95], v[212:215]\n"
    "ds_read_b64_tr_b16 a[62:63], v13 offset:35584\n"
    "v_mfma_f32_16x16x32_bf16 v[220:223], a[100:103], a[92:95], v[220:223]\n"
    "buffer_load_dwordx4 v4, s[40:43], s84 offen offset:0 sc1 lds\n"
    "v_mfma_f32_16x16x32_bf16 v[228:231], a[104:107], a[92:95], v[228:231]\n"
    "ds_read_b128 a[20:23], v8 offset:2048\n"
    "v_mfma_f32_16x16x32_bf16 v[236:239], a[108:111], a[92:95], v[236:239]\n"
    "ds_read_b128 a[24:27], v9 offset:0\n"
    "v_mfma_f32_16x16x32_bf16 v[244:247], a[112:115], a[92:95], v[244:247]\n"
    "buffer_load_dwordx4 v4, s[40:43], s85 offen offset:1024 sc1 lds\n"
    "v_mfma_f32_16x16x32_bf16 v[252:255], a[116:119], a[92:95], v[252:255]\n"
    "ds_read_b128 a[28:31], v9 offset:2048\n"
    "v_mfma_f32_16x16x32_bf16 a[0:3], a[120:123], a[92:95], a[0:3]\n"
    "ds_read_b64_tr_b16 a[64:65], v15 offset:0\n"
    "v_mfma_f32_16x16x32_bf16 a[4:7], a[124:127], a[92:95], a[4:7]\n"
    "ds_read_b64_tr_b16 a[66:67], v15 offset:256\n"
    "v_mfma_f32_16x16x32_bf16 a[168:171], a[128:131], a[80:83], a[168:171]\n"
    "buffer_load_dwordx4 v4, s[40:43], s86 offen offset:2048 sc1 lds\n"
    "v_mfma_f32_16x16x32_bf16 a[172:175], a[132:135], a[80:83], a[172:175]\n"
    "ds_read_b64_tr_b16 a[68:69], v5 offset:0\n"
    "v_add_u32 v2, 128, v2\n"
    "v_mfma_f32_16x16x32_bf16 a[176:179], a[128:131], a[84:87], a[176:179]\n"
    "ds_read_b64_tr_b16 a[70:71], v5 offset:256\n"
    "s_mul_i32 s97, s99, 0\n"
    "v_mfma_f32_16x16x32_bf16 a[180:183], a[132:135], a[84:87], a[180:183]\n"
    "ds_read_b64_tr_b16 a[72:73], v15 offset:1024\n"
    "s_add_u32 s97, s97, 19456\n"
    "v_mfma_f32_16x16x32_bf16 a[184:187], a[80:83], a[136:139], a[184:187]\n"
    "buffer_store_dwordx4 v[88:91], v14, s[48:51], s97 offen sc1\n"
    "v_mfma_f32_16x16x32_bf16 a[188:191], a[84:87], a[136:139], a[188:191]\n"
    "ds_read_b64_tr_b16 a[74:75], v15 offset:1280\n"
    "v_add_u32 v3, 128, v3\n"
    "v_mfma_f32_16x16x32_bf16 a[192:195], a[80:83], a[140:143], a[192:195]\n"
    "ds_read_b64_tr_b16 a[76:77], v5 offset:1024\n"
    "v_add_u32 v4, s74, v4\n"
    "v_mfma_f32_16x16x32_bf16 a[196:199], a[84:87], a[140:143], a[196:199]\n"
    "ds_read_b64_tr_b16 a[78:79], v5 offset:1280\n"
    "v_mfma_f32_16x16x32_bf16 a[200:203], a[80:83], a[156:159], a[200:203]\n"
    "ds_read_b64_tr_b16 a[144:145], v15 offset:2048\n"
    "s_add_u32 m0, s70, 0\n"
    "v_mfma_f32_16x16x32_bf16 a[204:207], a[84:87], a[156:159], a[204:207]\n"
    "ds_read_b64_tr_b16 a[146:147], v15 offset:2304\n"
    "v_mfma_f32_16x16x32_bf16 a[208:211], a[80:83], a[160:163], a[208:211]\n"
    "ds_read_b64_tr_b16 a[148:149], v5 offset:2048\n"
    "v_mfma_f32_16x16x32_bf16 a[212:215], a[84:87], a[160:163], a[212:215]\n"
    "ds_read_b64_tr_b16 a[150:151], v5 offset:2304\n"
    "v_mfma_f32_16x16x32_bf16 a[8:11], a[164:167], a[80:83], a[8:11]\n"
    "ds_read_b128 a[152:155], v0 offset:512\n"
    "v_mfma_f32_16x16x32_bf16 a[12:15], a[164:167], a[84:87], a[12:15]\n"
    "s_waitcnt lgkmcnt(15)\n"
    "v_mfma_f32_16x16x32_bf16 v[20:23], a[32:35], a[16:19], v[20:23]\n"
    "buffer_load_dwordx4 v6, s[44:47], s88 offen offset:0 lds\n"
    "v_mfma_f32_16x16x32_bf16 v[28:31], a[36:39], a[16:19], v[28:31]\n"
    "ds_read_b128 a[80:83], v10 offset:0\n"
    "v_mfma_f32_16x16x32_bf16 v[36:39], a[40:43], a[16:19], v[36:39]\n"
    "ds_read_b64_tr_b16 a[96:97], v12 offset:49152\n"
    "v_mfma_f32_16x16x32_bf16 v[44:47], a[44:47], a[16:19], v[44:47]\n"
    "ds_read_b64_tr_b16 a[98:99], v12 offset:51200\n"
    "v_mfma_f32_16x16x32_bf16 v[52:55], a[48:51], a[16:19], v[52:55]\n"
    "buffer_load_dwordx4 v6, s[44:47], s89 offen offset:1024 lds\n"
    "v_mfma_f32_16x16x32_bf16 v[60:63], a[52:55], a[16:19], v[60:63]\n"
    "ds_read_b64_tr_b16 a[100:101], v13 offset:49152\n"
    "v_mfma_f32_16x16x32_bf16 v[68:71], a[56:59], a[16:19], v[68:71]\n"
    "ds_read_b64_tr_b16 a[102:103], v13 offset:51200\n"
    "v_mfma_f32_16x16x32_bf16 v[76:79], a[60:63], a[16:19], v[76:79]\n"
    "ds_read_b64_tr_b16 a[104:105], v12 offset:49408\n"
    "v_mfma_f32_16x16x32_bf16 v[84:87], a[32:35], a[20:23], v[84:87]\n"
    "ds_read_b64_tr_b16 a[106:107], v12 offset:51456\n"
    "v_mfma_f32_16x16x32_bf16 v[92:95], a[36:39], a[20:23], v[92:95]\n"
    "buffer_load_dwordx4 v6, s[44:47], s90 offen offset:2048 lds\n"
    "v_mfma_f32_16x16x32_bf16 v[100:103], a[40:43], a[20:23], v[100:103]\n"
    "ds_read_b64_tr_b16 a[108:109], v13 offset:49408\n"
    "s_mul_i32 s97, s99, -4096\n"
    "v_mfma_f32_16x16x32_bf16 v[108:111], a[44:47], a[20:23], v[108:111]\n"
    "ds_read_b64_tr_b16 a[110:111], v13 offset:51456\n"
    "s_add_u32 s97, s97, 24576\n"
    "v_mfma_f32_16x16x32_bf16 v[116:119], a[48:51], a[20:23], v[116:119]\n"
    "buffer_store_dwordx4 v[96:99], v14, s[48:51], s97 offen sc1\n"
    "v_mfma_f32_16x16x32_bf16 v[124:127], a[52:55], a[20:23], v[124:127]\n"
    "ds_read_b64_tr_b16 a[112:113], v12 offset:49664\n"
    "v_mfma_f32_16x16x32_bf16 v[132:135], a[56:59], a[20:23], v[132:135]\n"
    "buffer_load_dwordx4 v6, s[44:47], s91 offen offset:3072 lds\n"
    "v_mfma_f32_16x16x32_bf16 v[140:143], a[60:63], a[20:23], v[140:143]\n"
    "ds_read_b64_tr_b16 a[114:115], v12 offset:51712\n"
    "s_waitcnt lgkmcnt(15)\n"
    "v_mfma_f32_16x16x32_bf16 v[148:151], a[32:35], a[24:27], v[148:151]\n"
    "ds_read_b64_tr_b16 a[116:117], v13 offset:49664\n"
    "s_add_u32 m0, s70, 4096\n"
    "v_mfma_f32_16x16x32_bf16 v[156:159], a[36:39], a[24:27], v[156:159]\n"
    "ds_read_b64_tr_b16 a[118:119], v13 offset:51712\n"
    "v_mfma_f32_16x16x32_bf16 v[164:167], a[40:43], a[24:27], v[164:167]\n"
    "ds_read_b64_tr_b16 a[120:121], v12 offset:49920\n"
    "v_mfma_f32_16x16x32_bf16 v[172:175], a[44:47], a[24:27], v[172:175]\n"
    "ds_read_b64_tr_b16 a[122:123], v12 offset:51968\n"
    "v_mfma_f32_16x16x32_bf16 v[180:183], a[48:51], a[24:27], v[180:183]\n"
    "ds_read_b64_tr_b16 a[124:125], v13 offset:49920\n"
    "v_mfma_f32_16x16x32_bf16 v[188:191], a[52:55], a[24:27], v[188:191]\n"
    "ds_read_b64_tr_b16 a[126:127], v13 offset:51968\n"
    "v_mfma_f32_16x16x32_bf16 v[196:199], a[56:59], a[24:27], v[196:199]\n"
    "buffer_load_dwordx4 v7, s[44:47], s92 offen offset:0 lds\n"
    "v_mfma_f32_16x16x32_bf16 v[204:207], a[60:63], a[24:27], v[204:207]\n"
    "ds_read_b128 a[84:87], v10 offset:2048\n"
    "v_mfma_f32_16x16x32_bf16 v[212:215], a[32:35], a[28:31], v[212:215]\n"
    "ds_read_b128 a[88:91], v11 offset:0\n"
    "v_mfma_f32_16x16x32_bf16 v[220:223], a[36:39], a[28:31], v[220:223]\n"
    "ds_read_b128 a[92:95], v11 offset:2048\n"
    "v_mfma_f32_16x16x32_bf16 v[228:231], a[40:43], a[28:31], v[228:231]\n"
    "buffer_load_dwordx4 v7, s[44:47], s93 offen offset:1024 lds\n"
    "v_mfma_f32_16x16x32_bf16 v[236:239], a[44:47], a[28:31], v[236:239]\n"
    "ds_read_b64_tr_b16 a[128:129], v15 offset:8192\n"
    "v_mfma_f32_16x16x32_bf16 v[244:247], a[48:51], a[28:31], v[244:247]\n"
    "ds_read_b64_tr_b16 a[130:131], v15 offset:8448\n"
    "v_mfma_f32_16x16x32_bf16 v[252:255], a[52:55], a[28:31], v[252:255]\n"
    "ds_read_b64_tr_b16 a[132:133], v5 offset:8192\n"
    "v_mfma_f32_16x16x32_bf16 a[0:3], a[56:59], a[28:31], a[0:3]\n"
    "buffer_load_dwordx4 v7, s[44:47], s94 offen offset:2048 lds\n"
    "v_mfma_f32_16x16x32_bf16 a[4:7], a[60:63], a[28:31], a[4:7]\n"
    "ds_read_b64_tr_b16 a[134:135], v5 offset:8448\n"
    "v_mfma_f32_16x16x32_bf16 a[168:171], a[64:67], a[16:19], a[168:171]\n"
    "ds_read_b64_tr_b16 a[136:137], v15 offset:9216\n"
    "s_mul_i32 s97, s99, -4096\n"
    "v_mfma_f32_16x16x32_bf16 a[172:175], a[68:71], a[16:19], a[172:175]\n"
    "ds_read_b64_tr_b16 a[138:139], v15 offset:9472\n"
    "s_add_u32 s97, s97, 25600\n"
    "v_mfma_f32_16x16x32_bf16 a[176:179], a[64:67], a[20:23], a[176:179]\n"
    "ds_read_b64_tr_b16 a[140:141], v5 offset:9216\n"
    "v_mfma_f32_16x16x32_bf16 a[180:183], a[68:71], a[20:23], a[180:183]\n"
    "buffer_load_dwordx4 v7, s[44:47], s95 offen offset:3072 lds\n"
    "v_mfma_f32_16x16x32_bf16 a[184:187], a[16:19], a[72:75], a[184:187]\n"
    "buffer_store_dwordx4 v[104:107], v14, s[48:51], s97 offen sc1\n"
    "v_mfma_f32_16x16x32_bf16 a[188:191], a[20:23], a[72:75], a[188:191]\n"
    "ds_read_b64_tr_b16 a[142:143], v5 offset:9472\n"
    "v_add_u32 v6, 0x80000, v6\n"
    "v_mfma_f32_16x16x32_bf16 a[192:195], a[16:19], a[76:79], a[192:195]\n"
    "ds_read_b64_tr_b16 a[156:157], v15 offset:10240\n"
    "v_mfma_f32_16x16x32_bf16 a[196:199], a[20:23], a[76:79], a[196:199]\n"
    "ds_read_b64_tr_b16 a[158:159], v15 offset:10496\n"
    "s_waitcnt lgkmcnt(15)\n"
    "v_mfma_f32_16x16x32_bf16 a[200:203], a[16:19], a[144:147], a[200:203]\n"
    "ds_read_b64_tr_b16 a[160:161], v5 offset:10240\n"
    "v_mfma_f32_16x16x32_bf16 a[204:207], a[20:23], a[144:147], a[204:207]\n"
    "ds_read_b64_tr_b16 a[162:163], v5 offset:10496\n"
    "v_add_u32 v7, 0x80000, v7\n"
    "v_mfma_f32_16x16x32_bf16 a[208:211], a[16:19], a[148:151], a[208:211]\n"
    "ds_read_b128 a[164:167], v0 offset:576\n"
    "v_mfma_f32_16x16x32_bf16 a[212:215], a[20:23], a[148:151], a[212:215]\n"
    "v_mfma_f32_16x16x32_bf16 a[8:11], a[152:155], a[16:19], a[8:11]\n"
    "v_mfma_f32_16x16x32_bf16 a[12:15], a[152:155], a[20:23], a[12:15]\n"
    "v_mfma_f32_16x16x32_bf16 v[20:23], a[96:99], a[80:83], v[20:23]\n"
    "v_mfma_f32_16x16x32_bf16 v[28:31], a[100:103], a[80:83], v[28:31]\n"
    "v_mfma_f32_16x16x32_bf16 v[36:39], a[104:107], a[80:83], v[36:39]\n"
    "s_waitcnt vmcnt(11) lgkmcnt(0)\n"
    "s_barrier\n"
    "v_mfma_f32_16x16x32_bf16 v[44:47], a[108:111], a[80:83], v[44:47]\n"
    "ds_read_b128 a[16:19], v8 offset:32768\n"
    "v_mfma_f32_16x16x32_bf16 v[52:55], a[112:115], a[80:83], v[52:55]\n"
    "v_add_u32 v12, 0x10000, v12\n"
    "s_add_u32 m0, s68, 0\n"
    "v_mfma_f32_16x16x32_bf16 v[60:63], a[116:119], a[80:83], v[60:63]\n"
    "v_add_u32 v13, 0x10000, v13\n"
    "v_mfma_f32_16x16x32_bf16 v[68:71], a[120:123], a[80:83], v[68:71]\n"
    "ds_read_b64_tr_b16 a[32:33], v12 offset:0\n"
    "v_mfma_f32_16x16x32_bf16 v[76:79], a[124:127], a[80:83], v[76:79]\n"
    "ds_read_b64_tr_b16 a[34:35], v12 offset:2048\n"
    "v_mfma_f32_16x16x32_bf16 v[84:87], a[96:99], a[84:87], v[84:87]\n"
    "buffer_load_dwordx4 v2, s[36:39], s80 offen offset:0 lds\n"
    "v_mfma_f32_16x16x32_bf16 v[92:95], a[100:103], a[84:87], v[92:95]\n"
    "ds_read_b64_tr_b16 a[36:37], v13 offset:0\n"
    "s_mul_i32 s97, s99, -4096\n"
    "v_mfma_f32_16x16x32_bf16 v[100:103], a[104:107], a[84:87], v[100:103]\n"
    "ds_read_b64_tr_b16 a[38:39], v13 offset:2048\n"
    "s_add_u32 s97, s97, 26624\n"
    "v_mfma_f32_16x16x32_bf16 v[108:111], a[108:111], a[84:87], v[108:111]\n"
    "buffer_load_dwordx4 v3, s[36:39], s81 offen offset:1024 lds\n"
    "v_mfma_f32_16x16x32_bf16 v[116:119], a[112:115], a[84:87], v[116:119]\n"
    "buffer_store_dwordx4 v[112:115], v14, s[48:51], s97 offen sc1\n"
    "v_mfma_f32_16x16x32_bf16 v[124:127], a[116:119], a[84:87], v[124:127]\n"
    "ds_read_b64_tr_b16 a[40:41], v12 offset:256\n"
    "v_mfma_f32_16x16x32_bf16 v[132:135], a[120:123], a[84:87], v[132:135]\n"
    "ds_read_b64_tr_b16 a[42:43], v12 offset:2304\n"
    "v_mfma_f32_16x16x32_bf16 v[140:143], a[124:127], a[84:87], v[140:143]\n"
    "ds_read_b64_tr_b16 a[44:45], v13 offset:256\n"
    "v_mfma_f32_16x16x32_bf16 v[148:151], a[96:99], a[88:91], v[148:151]\n"
    "buffer_load_dwordx4 v2, s[36:39], s82 offen offset:2048 lds\n"
    "v_mfma_f32_16x16x32_bf16 v[156:159], a[100:103], a[88:91], v[156:159]\n"
    "ds_read_b64_tr_b16 a[46:47], v13 offset:2304\n"
    "v_mfma_f32_16x16x32_bf16 v[164:167], a[104:107], a[88:91], v[164:167]\n"
    "ds_read_b64_tr_b16 a[48:49], v12 offset:512\n"
    "v_mfma_f32_16x16x32_bf16 v[172:175], a[108:111], a[88:91], v[172:175]\n"
    "buffer_load_dwordx4 v3, s[36:39], s83 offen offset:3072 lds\n"
    "v_mfma_f32_16x16x32_bf16 v[180:183], a[112:115], a[88:91], v[180:183]\n"
    "ds_read_b64_tr_b16 a[50:51], v12 offset:2560\n"
    "s_add_u32 m0, s69, 0\n"
    "v_mfma_f32_16x16x32_bf16 v[188:191], a[116:119], a[88:91], v[188:191]\n"
    "ds_read_b64_tr_b16 a[52:53], v13 offset:512\n"
    "v_mfma_f32_16x16x32_bf16 v[196:199], a[120:123], a[88:91], v[196:199]\n"
    "ds_read_b64_tr_b16 a[54:55], v13 offset:2560\n"
    "v_mfma_f32_16x16x32_bf16 v[204:207], a[124:127], a[88:91], v[204:207]\n"
    "ds_read_b64_tr_b16 a[56:57], v12 offset:768\n"
    "v_mfma_f32_16x16x32_bf16 v[212:215], a[96:99], a[92:95], v[212:215]\n"
    "ds_read_b64_tr_b16 a[58:59], v12 offset:2816\n"
    "v_mfma_f32_16x16x32_bf16 v[220:223], a[100:103], a[92:95], v[220:223]\n"
    "ds_read_b64_tr_b16 a[60:61], v13 offset:768\n"
    "v_mfma_f32_16x16x32_bf16 v[228:231], a[104:107], a[92:95], v[228:231]\n"
    "buffer_load_dwordx4 v4, s[40:43], s84 offen offset:0 sc1 lds\n"
    "v_mfma_f32_16x16x32_bf16 v[236:239], a[108:111], a[92:95], v[236:239]\n"
    "ds_read_b64_tr_b16 a[62:63], v13 offset:2816\n"
    "v_mfma_f32_16x16x32_bf16 v[244:247], a[112:115], a[92:95], v[244:247]\n"
    "ds_read_b128 a[20:23], v8 offset:34816\n"
    "v_mfma_f32_16x16x32_bf16 v[252:255], a[116:119], a[92:95], v[252:255]\n"
    "buffer_load_dwordx4 v4, s[40:43], s85 offen offset:1024 sc1 lds\n"
    "v_mfma_f32_16x16x32_bf16 a[0:3], a[120:123], a[92:95], a[0:3]\n"
    "ds_read_b128 a[24:27], v9 offset:32768\n"
    "v_mfma_f32_16x16x32_bf16 a[4:7], a[124:127], a[92:95], a[4:7]\n"
    "ds_read_b128 a[28:31], v9 offset:34816\n"
    "v_mfma_f32_16x16x32_bf16 a[168:171], a[128:131], a[80:83], a[168:171]\n"
    "ds_read_b64_tr_b16 a[64:65], v15 offset:32768\n"
    "s_mul_i32 s97, s99, -4096\n"
    "v_mfma_f32_16x16x32_bf16 a[172:175], a[132:135], a[80:83], a[172:175]\n"
    "buffer_load_dwordx4 v4, s[40:43], s86 offen offset:2048 sc1 lds\n"
    "v_mfma_f32_16x16x32_bf16 a[176:179], a[128:131], a[84:87], a[176:179]\n"
    "ds_read_b64_tr_b16 a[66:67], v15 offset:33024\n"
    "v_add_u32 v2, 128, v2\n"
    "v_mfma_f32_16x16x32_bf16 a[180:183], a[132:135], a[84:87], a[180:183]\n"
    "ds_read_b64_tr_b16 a[68:69], v5 offset:32768\n"
    "s_add_u32 s97, s97, 27648\n"
    "v_mfma_f32_16x16x32_bf16 a[184:187], a[80:83], a[136:139], a[184:187]\n"
    "ds_read_b64_tr_b16 a[70:71], v5 offset:33024\n"
    "v_add_u32 v3, 128, v3\n"
    "v_mfma_f32_16x16x32_bf16 a[188:191], a[84:87], a[136:139], a[188:191]\n"
    "ds_read_b64_tr_b16 a[72:73], v15 offset:33792\n"
    "v_add_u32 v4, s74, v4\n"
    "v_mfma_f32_16x16x32_bf16 a[192:195], a[80:83], a[140:143], a[192:195]\n"
    "buffer_store_dwordx4 v[120:123], v14, s[48:51], s97 offen sc1\n"
    "v_mfma_f32_16x16x32_bf16 a[196:199], a[84:87], a[140:143], a[196:199]\n"
    "ds_read_b64_tr_b16 a[74:75], v15 offset:34048\n"
    "v_mfma_f32_16x16x32_bf16 a[200:203], a[80:83], a[156:159], a[200:203]\n"
    "ds_read_b64_tr_b16 a[76:77], v5 offset:33792\n"
    "v_mfma_f32_16x16x32_bf16 a[204:207], a[84:87], a[156:159], a[204:207]\n"
    "ds_read_b64_tr_b16 a[78:79], v5 offset:34048\n"
    "v_mfma_f32_16x16x32_bf16 a[208:211], a[80:83], a[160:163], a[208:211]\n"
    "ds_read_b64_tr_b16 a[144:145], v15 offset:34816\n"
    "v_mfma_f32_16x16x32_bf16 a[212:215], a[84:87], a[160:163], a[212:215]\n"
    "ds_read_b64_tr_b16 a[146:147], v15 offset:35072\n"
    "v_mfma_f32_16x16x32_bf16 a[8:11], a[164:167], a[80:83], a[8:11]\n"
    "ds_read_b64_tr_b16 a[148:149], v5 offset:34816\n"
    "v_mfma_f32_16x16x32_bf16 a[12:15], a[164:167], a[84:87], a[12:15]\n"
    "ds_read_b64_tr_b16 a[150:151], v5 offset:35072\n"
    "s_waitcnt lgkmcnt(15)\n"
    "v_mfma_f32_16x16x32_bf16 v[20:23], a[32:35], a[16:19], v[20:23]\n"
    "ds_read_b128 a[152:155], v0 offset:640\n"
    "v_mfma_f32_16x16x32_bf16 v[28:31], a[36:39], a[16:19], v[28:31]\n"
    "ds_read_b128 a[80:83], v10 offset:32768\n"
    "v_mfma_f32_16x16x32_bf16 v[36:39], a[40:43], a[16:19], v[36:39]\n"
    "ds_read_b64_tr_b16 a[96:97], v12 offset:16384\n"
    "v_mfma_f32_16x16x32_bf16 v[44:47], a[44:47], a[16:19], v[44:47]\n"
    "ds_read_b64_tr_b16 a[98:99], v12 offset:18432\n"
    "v_mfma_f32_16x16x32_bf16 v[52:55], a[48:51], a[16:19], v[52:55]\n"
    "ds_read_b64_tr_b16 a[100:101], v13 offset:16384\n"
    "v_mfma_f32_16x16x32_bf16 v[60:63], a[52:55], a[16:19], v[60:63]\n"
    "ds_read_b64_tr_b16 a[102:103], v13 offset:18432\n"
    "s_mul_i32 s97, s99, 0\n"
    "v_mfma_f32_16x16x32_bf16 v[68:71], a[56:59], a[16:19], v[68:71]\n"
    "ds_read_b64_tr_b16 a[104:105], v12 offset:16640\n"
    "s_add_u32 s97, s97, 32768\n"
    "v_mfma_f32_16x16x32_bf16 v[76:79], a[60:63], a[16:19], v[76:79]\n"
    "ds_read_b64_tr_b16 a[106:107], v12 offset:18688\n"
    "s_waitcnt lgkmcnt(15)\n"
    "v_mfma_f32_16x16x32_bf16 v[84:87], a[32:35], a[20:23], v[84:87]\n"
    "buffer_store_dwordx4 v[128:131], v14, s[48:51], s97 offen sc1\n"
    "v_mfma_f32_16x16x32_bf16 v[92:95], a[36:39], a[20:23], v[92:95]\n"
    "ds_read_b64_tr_b16 a[108:109], v13 offset:16640\n"
    "v_mfma_f32_16x16x32_bf16 v[100:103], a[40:43], a[20:23], v[100:103]\n"
    "ds_read_b64_tr_b16 a[110:111], v13 offset:18688\n"
    "v_mfma_f32_16x16x32_bf16 v[108:111], a[44:47], a[20:23], v[108:111]\n"
    "ds_read_b64_tr_b16 a[112:113], v12 offset:16896\n"
    "v_mfma_f32_16x16x32_bf16 v[116:119], a[48:51], a[20:23], v[116:119]\n"
    "ds_read_b64_tr_b16 a[114:115], v12 offset:18944\n"
    "v_mfma_f32_16x16x32_bf16 v[124:127], a[52:55], a[20:23], v[124:127]\n"
    "v_mfma_f32_16x16x32_bf16 v[132:135], a[56:59], a[20:23], v[132:135]\n"
    "ds_read_b64_tr_b16 a[116:117], v13 offset:16896\n"
    "v_mfma_f32_16x16x32_bf16 v[140:143], a[60:63], a[20:23], v[140:143]\n"
    "ds_read_b64_tr_b16 a[118:119], v13 offset:18944\n"
    "v_mfma_f32_16x16x32_bf16 v[148:151], a[32:35], a[24:27], v[148:151]\n"
    "ds_read_b64_tr_b16 a[120:121], v12 offset:17152\n"
    "v_mfma_f32_16x16x32_bf16 v[156:159], a[36:39], a[24:27], v[156:159]\n"
    "ds_read_b64_tr_b16 a[122:123], v12 offset:19200\n"
    "v_mfma_f32_16x16x32_bf16 v[164:167], a[40:43], a[24:27], v[164:167]\n"
    "v_mfma_f32_16x16x32_bf16 v[172:175], a[44:47], a[24:27], v[172:175]\n"
    "ds_read_b64_tr_b16 a[124:125], v13 offset:17152\n"
    "v_mfma_f32_16x16x32_bf16 v[180:183], a[48:51], a[24:27], v[180:183]\n"
    "ds_read_b64_tr_b16 a[126:127], v13 offset:19200\n"
    "v_mfma_f32_16x16x32_bf16 v[188:191], a[52:55], a[24:27], v[188:191]\n"
    "ds_read_b128 a[84:87], v10 offset:34816\n"
    "s_mul_i32 s97, s99, 0\n"
    "v_mfma_f32_16x16x32_bf16 v[196:199], a[56:59], a[24:27], v[196:199]\n"
    "ds_read_b128 a[88:91], v11 offset:32768\n"
    "s_add_u32 s97, s97, 33792\n"
    "v_mfma_f32_16x16x32_bf16 v[204:207], a[60:63], a[24:27], v[204:207]\n"
    "v_mfma_f32_16x16x32_bf16 v[212:215], a[32:35], a[28:31], v[212:215]\n"
    "buffer_store_dwordx4 v[136:139], v14, s[48:51], s97 offen sc1\n"
    "v_mfma_f32_16x16x32_bf16 v[220:223], a[36:39], a[28:31], v[220:223]\n"
    "ds_read_b128 a[92:95], v11 offset:34816\n"
    "v_mfma_f32_16x16x32_bf16 v[228:231], a[40:43], a[28:31], v[228:231]\n"
    "ds_read_b64_tr_b16 a[128:129], v15 offset:40960\n"
    "v_mfma_f32_16x16x32_bf16 v[236:239], a[44:47], a[28:31], v[236:239]\n"
    "ds_read_b64_tr_b16 a[130:131], v15 offset:41216\n"
    "v_mfma_f32_16x16x32_bf16 v[244:247], a[48:51], a[28:31], v[244:247]\n"
    "ds_read_b64_tr_b16 a[132:133], v5 offset:40960\n"
    "v_mfma_f32_16x16x32_bf16 v[252:255], a[52:55], a[28:31], v[252:255]\n"
    "ds_read_b64_tr_b16 a[134:135], v5 offset:41216\n"
    "v_mfma_f32_16x16x32_bf16 a[0:3], a[56:59], a[28:31], a[0:3]\n"
    "ds_read_b64_tr_b16 a[136:137], v15 offset:41984\n"
    "v_mfma_f32_16x16x32_bf16 a[4:7], a[60:63], a[28:31], a[4:7]\n"
    "ds_read_b64_tr_b16 a[138:139], v15 offset:42240\n"
    "v_mfma_f32_16x16x32_bf16 a[168:171], a[64:67], a[16:19], a[168:171]\n"
    "v_mfma_f32_16x16x32_bf16 a[172:175], a[68:71], a[16:19], a[172:175]\n"
    "ds_read_b64_tr_b16 a[140:141], v5 offset:41984\n"
    "v_mfma_f32_16x16x32_bf16 a[176:179], a[64:67], a[20:23], a[176:179]\n"
    "ds_read_b64_tr_b16 a[142:143], v5 offset:42240\n"
    "v_mfma_f32_16x16x32_bf16 a[180:183], a[68:71], a[20:23], a[180:183]\n"
    "ds_read_b64_tr_b16 a[156:157], v15 offset:43008\n"
    "s_waitcnt lgkmcnt(15)\n"
    "v_mfma_f32_16x16x32_bf16 a[184:187], a[16:19], a[72:75], a[184:187]\n"
    "ds_read_b64_tr_b16 a[158:159], v15 offset:43264\n"
    "s_mul_i32 s97, s99, 0\n"
    "v_mfma_f32_16x16x32_bf16 a[188:191], a[20:23], a[72:75], a[188:191]\n"
    "s_add_u32 s97, s97, 34816\n"
    "v_mfma_f32_16x16x32_bf16 a[192:195], a[16:19], a[76:79], a[192:195]\n"
    "buffer_store_dwordx4 v[144:147], v14, s[48:51], s97 offen sc1\n"
    "v_mfma_f32_16x16x32_bf16 a[196:199], a[20:23], a[76:79], a[196:199]\n"
    "ds_read_b64_tr_b16 a[160:161], v5 offset:43008\n"
    "v_mfma_f32_16x16x32_bf16 a[200:203], a[16:19], a[144:147], a[200:203]\n"
    "ds_read_b64_tr_b16 a[162:163], v5 offset:43264\n"
    "v_mfma_f32_16x16x32_bf16 a[204:207], a[20:23], a[144:147], a[204:207]\n"
    "ds_read_b128 a[164:167], v0 offset:704\n"
    "v_mfma_f32_16x16x32_bf16 a[208:211], a[16:19], a[148:151], a[208:211]\n"
    "v_mfma_f32_16x16x32_bf16 a[212:215], a[20:23], a[148:151], a[212:215]\n"
    "v_mfma_f32_16x16x32_bf16 a[8:11], a[152:155], a[16:19], a[8:11]\n"
    "v_mfma_f32_16x16x32_bf16 a[12:15], a[152:155], a[20:23], a[12:15]\n"
    "v_mfma_f32_16x16x32_bf16 v[20:23], a[96:99], a[80:83], v[20:23]\n"
    "v_mfma_f32_16x16x32_bf16 v[28:31], a[100:103], a[80:83], v[28:31]\n"
    "v_mfma_f32_16x16x32_bf16 v[36:39], a[104:107], a[80:83], v[36:39]\n"
    "s_waitcnt vmcnt(4) lgkmcnt(0)\n"
    "s_barrier\n"
    "v_mfma_f32_16x16x32_bf16 v[44:47], a[108:111], a[80:83], v[44:47]\n"
    "ds_read_b128 a[16:19], v8 offset:0\n"
    "v_mfma_f32_16x16x32_bf16 v[52:55], a[112:115], a[80:83], v[52:55]\n"
    "v_subrev_u32 v12, 0x10000, v12\n"
    "v_mfma_f32_16x16x32_bf16 v[60:63], a[116:119], a[80:83], v[60:63]\n"
    "v_subrev_u32 v13, 0x10000, v13\n"
    "s_mul_i32 s97, s99, 0\n"
    "v_mfma_f32_16x16x32_bf16 v[68:71], a[120:123], a[80:83], v[68:71]\n"
    "ds_read_b64_tr_b16 a[32:33], v12 offset:0\n"
    "s_add_u32 s97, s97, 35840\n"
    "v_mfma_f32_16x16x32_bf16 v[76:79], a[124:127], a[80:83], v[76:79]\n"
    "ds_read_b64_tr_b16 a[34:35], v12 offset:2048\n"
    "v_mfma_f32_16x16x32_bf16 v[84:87], a[96:99], a[84:87], v[84:87]\n"
    "buffer_store_dwordx4 v[152:155], v14, s[48:51], s97 offen sc1\n"
    "v_mfma_f32_16x16x32_bf16 v[92:95], a[100:103], a[84:87], v[92:95]\n"
    "ds_read_b64_tr_b16 a[36:37], v13 offset:0\n"
    "v_mfma_f32_16x16x32_bf16 v[100:103], a[104:107], a[84:87], v[100:103]\n"
    "ds_read_b64_tr_b16 a[38:39], v13 offset:2048\n"
    "v_mfma_f32_16x16x32_bf16 v[108:111], a[108:111], a[84:87], v[108:111]\n"
    "ds_read_b64_tr_b16 a[40:41], v12 offset:256\n"
    "v_mfma_f32_16x16x32_bf16 v[116:119], a[112:115], a[84:87], v[116:119]\n"
    "ds_read_b64_tr_b16 a[42:43], v12 offset:2304\n"
    "v_mfma_f32_16x16x32_bf16 v[124:127], a[116:119], a[84:87], v[124:127]\n"
    "ds_read_b64_tr_b16 a[44:45], v13 offset:256\n"
    "v_mfma_f32_16x16x32_bf16 v[132:135], a[120:123], a[84:87], v[132:135]\n"
    "ds_read_b64_tr_b16 a[46:47], v13 offset:2304\n"
    "v_mfma_f32_16x16x32_bf16 v[140:143], a[124:127], a[84:87], v[140:143]\n"
    "ds_read_b64_tr_b16 a[48:49], v12 offset:512\n"
    "v_mfma_f32_16x16x32_bf16 v[148:151], a[96:99], a[88:91], v[148:151]\n"
    "v_mfma_f32_16x16x32_bf16 v[156:159], a[100:103], a[88:91], v[156:159]\n"
    "ds_read_b64_tr_b16 a[50:51], v12 offset:2560\n"
    "v_mfma_f32_16x16x32_bf16 v[164:167], a[104:107], a[88:91], v[164:167]\n"
    "ds_read_b64_tr_b16 a[52:53], v13 offset:512\n"
    "v_mfma_f32_16x16x32_bf16 v[172:175], a[108:111], a[88:91], v[172:175]\n"
    "ds_read_b64_tr_b16 a[54:55], v13 offset:2560\n"
    "v_mfma_f32_16x16x32_bf16 v[180:183], a[112:115], a[88:91], v[180:183]\n"
    "ds_read_b64_tr_b16 a[56:57], v12 offset:768\n"
    "v_mfma_f32_16x16x32_bf16 v[188:191], a[116:119], a[88:91], v[188:191]\n"
    "ds_read_b64_tr_b16 a[58:59], v12 offset:2816\n"
    "s_mul_i32 s97, s99, -4096\n"
    "v_mfma_f32_16x16x32_bf16 v[196:199], a[120:123], a[88:91], v[196:199]\n"
    "ds_read_b64_tr_b16 a[60:61], v13 offset:768\n"
    "s_add_u32 s97, s97, 40960\n"
    "v_mfma_f32_16x16x32_bf16 v[204:207], a[124:127], a[88:91], v[204:207]\n"
    "v_mfma_f32_16x16x32_bf16 v[212:215], a[96:99], a[92:95], v[212:215]\n"
    "buffer_store_dwordx4 v[160:163], v14, s[48:51], s97 offen sc1\n"
    "v_mfma_f32_16x16x32_bf16 v[220:223], a[100:103], a[92:95], v[220:223]\n"
    "ds_read_b64_tr_b16 a[62:63], v13 offset:2816\n"
    "v_mfma_f32_16x16x32_bf16 v[228:231], a[104:107], a[92:95], v[228:231]\n"
    "ds_read_b128 a[20:23], v8 offset:2048\n"
    "v_mfma_f32_16x16x32_bf16 v[236:239], a[108:111], a[92:95], v[236:239]\n"
    "ds_read_b128 a[24:27], v9 offset:0\n"
    "v_mfma_f32_16x16x32_bf16 v[244:247], a[112:115], a[92:95], v[244:247]\n"
    "ds_read_b128 a[28:31], v9 offset:2048\n"
    "v_mfma_f32_16x16x32_bf16 v[252:255], a[116:119], a[92:95], v[252:255]\n"
    "ds_read_b64_tr_b16 a[64:65], v15 offset:0\n"
    "v_mfma_f32_16x16x32_bf16 a[0:3], a[120:123], a[92:95], a[0:3]\n"
    "ds_read_b64_tr_b16 a[66:67], v15 offset:256\n"
    "v_mfma_f32_16x16x32_bf16 a[4:7], a[124:127], a[92:95], a[4:7]\n"
    "ds_read_b64_tr_b16 a[68:69], v5 offset:0\n"
    "v_mfma_f32_16x16x32_bf16 a[168:171], a[128:131], a[80:83], a[168:171]\n"
    "ds_read_b64_tr_b16 a[70:71], v5 offset:256\n"
    "v_mfma_f32_16x16x32_bf16 a[172:175], a[132:135], a[80:83], a[172:175]\n"
    "ds_read_b64_tr_b16 a[72:73], v15 offset:1024\n"
    "v_mfma_f32_16x16x32_bf16 a[176:179], a[128:131], a[84:87], a[176:179]\n"
    "ds_read_b64_tr_b16 a[74:75], v15 offset:1280\n"
    "v_mfma_f32_16x16x32_bf16 a[180:183], a[132:135], a[84:87], a[180:183]\n"
    "ds_read_b64_tr_b16 a[76:77], v5 offset:1024\n"
    "v_mfma_f32_16x16x32_bf16 a[184:187], a[80:83], a[136:139], a[184:187]\n"
    "ds_read_b64_tr_b16 a[78:79], v5 offset:1280\n"
    "s_mul_i32 s97, s99, -4096\n"
    "v_mfma_f32_16x16x32_bf16 a[188:191], a[84:87], a[136:139], a[188:191]\n"
    "s_add_u32 s97, s97, 41984\n"
    "v_mfma_f32_16x16x32_bf16 a[192:195], a[80:83], a[140:143], a[192:195]\n"
    "buffer_store_dwordx4 v[168:171], v14, s[48:51], s97 offen sc1\n"
    "v_mfma_f32_16x16x32_bf16 a[196:199], a[84:87], a[140:143], a[196:199]\n"
    "ds_read_b64_tr_b16 a[144:145], v15 offset:2048\n"
    "v_mfma_f32_16x16x32_bf16 a[200:203], a[80:83], a[156:159], a[200:203]\n"
    "ds_read_b64_tr_b16 a[146:147], v15 offset:2304\n"
    "v_mfma_f32_16x16x32_bf16 a[204:207], a[84:87], a[156:159], a[204:207]\n"
    "ds_read_b64_tr_b16 a[148:149], v5 offset:2048\n"
    "v_mfma_f32_16x16x32_bf16 a[208:211], a[80:83], a[160:163], a[208:211]\n"
    "ds_read_b64_tr_b16 a[150:151], v5 offset:2304\n"
    "v_mfma_f32_16x16x32_bf16 a[212:215], a[84:87], a[160:163], a[212:215]\n"
    "ds_read_b128 a[152:155], v0 offset:768\n"
    "v_mfma_f32_16x16x32_bf16 a[8:11], a[164:167], a[80:83], a[8:11]\n"
    "v_mfma_f32_16x16x32_bf16 a[12:15], a[164:167], a[84:87], a[12:15]\n"
    "s_waitcnt lgkmcnt(15)\n"
    "v_mfma_f32_16x16x32_bf16 v[20:23], a[32:35], a[16:19], v[20:23]\n"
    "ds_read_b128 a[80:83], v10 offset:0\n"
    "v_mfma_f32_16x16x32_bf16 v[28:31], a[36:39], a[16:19], v[28:31]\n"
    "ds_read_b64_tr_b16 a[96:97], v12 offset:16384\n"
    "v_mfma_f32_16x16x32_bf16 v[36:39], a[40:43], a[16:19], v[36:39]\n"
    "ds_read_b64_tr_b16 a[98:99], v12 offset:18432\n"
    "v_mfma_f32_16x16x32_bf16 v[44:47], a[44:47], a[16:19], v[44:47]\n"
    "ds_read_b64_tr_b16 a[100:101], v13 offset:16384\n"
    "v_mfma_f32_16x16x32_bf16 v[52:55], a[48:51], a[16:19], v[52:55]\n"
    "v_mfma_f32_16x16x32_bf16 v[60:63], a[52:55], a[16:19], v[60:63]\n"
    "ds_read_b64_tr_b16 a[102:103], v13 offset:18432\n"
    "s_mul_i32 s97, s99, -4096\n"
    "v_mfma_f32_16x16x32_bf16 v[68:71], a[56:59], a[16:19], v[68:71]\n"
    "ds_read_b64_tr_b16 a[104:105], v12 offset:16640\n"
    "s_add_u32 s97, s97, 43008\n"
    "v_mfma_f32_16x16x32_bf16 v[76:79], a[60:63], a[16:19], v[76:79]\n"
    "ds_read_b64_tr_b16 a[106:107], v12 offset:18688\n"
    "v_mfma_f32_16x16x32_bf16 v[84:87], a[32:35], a[20:23], v[84:87]\n"
    "buffer_store_dwordx4 v[176:179], v14, s[48:51], s97 offen sc1\n"
    "v_mfma_f32_16x16x32_bf16 v[92:95], a[36:39], a[20:23], v[92:95]\n"
    "ds_read_b64_tr_b16 a[108:109], v13 offset:16640\n"
    "v_mfma_f32_16x16x32_bf16 v[100:103], a[40:43], a[20:23], v[100:103]\n"
    "ds_read_b64_tr_b16 a[110:111], v13 offset:18688\n"
    "v_mfma_f32_16x16x32_bf16 v[108:111], a[44:47], a[20:23], v[108:111]\n"
    "ds_read_b64_tr_b16 a[112:113], v12 offset:16896\n"
    "v_mfma_f32_16x16x32_bf16 v[116:119], a[48:51], a[20:23], v[116:119]\n"
    "ds_read_b64_tr_b16 a[114:115], v12 offset:18944\n"
    "v_mfma_f32_16x16x32_bf16 v[124:127], a[52:55], a[20:23], v[124:127]\n"
    "v_mfma_f32_16x16x32_bf16 v[132:135], a[56:59], a[20:23], v[132:135]\n"
    "ds_read_b64_tr_b16 a[116:117], v13 offset:16896\n"
    "v_mfma_f32_16x16x32_bf16 v[140:143], a[60:63], a[20:23], v[140:143]\n"
    "ds_read_b64_tr_b16 a[118:119], v13 offset:18944\n"
    "s_waitcnt lgkmcnt(15)\n"
    "v_mfma_f32_16x16x32_bf16 v[148:151], a[32:35], a[24:27], v[148:151]\n"
    "ds_read_b64_tr_b16 a[120:121], v12 offset:17152\n"
    "v_mfma_f32_16x16x32_bf16 v[156:159], a[36:39], a[24:27], v[156:159]\n"
    "ds_read_b64_tr_b16 a[122:123], v12 offset:19200\n"
    "v_mfma_f32_16x16x32_bf16 v[164:167], a[40:43], a[24:27], v[164:167]\n"
    "v_mfma_f32_16x16x32_bf16 v[172:175], a[44:47], a[24:27], v[172:175]\n"
    "ds_read_b64_tr_b16 a[124:125], v13 offset:17152\n"
    "v_mfma_f32_16x16x32_bf16 v[180:183], a[48:51], a[24:27], v[180:183]\n"
    "ds_read_b64_tr_b16 a[126:127], v13 offset:19200\n"
    "v_mfma_f32_16x16x32_bf16 v[188:191], a[52:55], a[24:27], v[188:191]\n"
    "ds_read_b128 a[84:87], v10 offset:2048\n"
    "s_mul_i32 s97, s99, -4096\n"
    "v_mfma_f32_16x16x32_bf16 v[196:199], a[56:59], a[24:27], v[196:199]\n"
    "ds_read_b128 a[88:91], v11 offset:0\n"
    "s_add_u32 s97, s97, 44032\n"
    "v_mfma_f32_16x16x32_bf16 v[204:207], a[60:63], a[24:27], v[204:207]\n"
    "v_mfma_f32_16x16x32_bf16 v[212:215], a[32:35], a[28:31], v[212:215]\n"
    "buffer_store_dwordx4 v[184:187], v14, s[48:51], s97 offen sc1\n"
    "v_mfma_f32_16x16x32_bf16 v[220:223], a[36:39], a[28:31], v[220:223]\n"
    "ds_read_b128 a[92:95], v11 offset:2048\n"
    "v_mfma_f32_16x16x32_bf16 v[228:231], a[40:43], a[28:31], v[228:231]\n"
    "ds_read_b64_tr_b16 a[128:129], v15 offset:8192\n"
    "v_mfma_f32_16x16x32_bf16 v[236:239], a[44:47], a[28:31], v[236:239]\n"
    "ds_read_b64_tr_b16 a[130:131], v15 offset:8448\n"
    "v_mfma_f32_16x16x32_bf16 v[244:247], a[48:51], a[28:31], v[244:247]\n"
    "ds_read_b64_tr_b16 a[132:133], v5 offset:8192\n"
    "v_mfma_f32_16x16x32_bf16 v[252:255], a[52:55], a[28:31], v[252:255]\n"
    "ds_read_b64_tr_b16 a[134:135], v5 offset:8448\n"
    "v_mfma_f32_16x16x32_bf16 a[0:3], a[56:59], a[28:31], a[0:3]\n"
    "ds_read_b64_tr_b16 a[136:137], v15 offset:9216\n"
    "v_mfma_f32_16x16x32_bf16 a[4:7], a[60:63], a[28:31], a[4:7]\n"
    "ds_read_b64_tr_b16 a[138:139], v15 offset:9472\n"
    "v_mfma_f32_16x16x32_bf16 a[168:171], a[64:67], a[16:19], a[168:171]\n"
    "v_mfma_f32_16x16x32_bf16 a[172:175], a[68:71], a[16:19], a[172:175]\n"
    "ds_read_b64_tr_b16 a[140:141], v5 offset:9216\n"
    "v_mfma_f32_16x16x32_bf16 a[176:179], a[64:67], a[20:23], a[176:179]\n"
    "ds_read_b64_tr_b16 a[142:143], v5 offset:9472\n"
    "v_mfma_f32_16x16x32_bf16 a[180:183], a[68:71], a[20:23], a[180:183]\n"
    "ds_read_b64_tr_b16 a[156:157], v15 offset:10240\n"
    "v_mfma_f32_16x16x32_bf16 a[184:187], a[16:19], a[72:75], a[184:187]\n"
    "ds_read_b64_tr_b16 a[158:159], v15 offset:10496\n"
    "s_mul_i32 s97, s99, 0\n"
    "v_mfma_f32_16x16x32_bf16 a[188:191], a[20:23], a[72:75], a[188:191]\n"
    "s_add_u32 s97, s97, 49152\n"
    "v_mfma_f32_16x16x32_bf16 a[192:195], a[16:19], a[76:79], a[192:195]\n"
    "buffer_store_dwordx4 v[192:195], v14, s[48:51], s97 offen sc1\n"
    "v_mfma_f32_16x16x32_bf16 a[196:199], a[20:23], a[76:79], a[196:199]\n"
    "ds_read_b64_tr_b16 a[160:161], v5 offset:10240\n"
    "v_mfma_f32_16x16x32_bf16 a[200:203], a[16:19], a[144:147], a[200:203]\n"
    "ds_read_b64_tr_b16 a[162:163], v5 offset:10496\n"
    "v_mfma_f32_16x16x32_bf16 a[204:207], a[20:23], a[144:147], a[204:207]\n"
    "ds_read_b128 a[164:167], v0 offset:832\n"
    "s_waitcnt lgkmcnt(15)\n"
    "v_mfma_f32_16x16x32_bf16 a[208:211], a[16:19], a[148:151], a[208:211]\n"
    "v_mfma_f32_16x16x32_bf16 a[212:215], a[20:23], a[148:151], a[212:215]\n"
    "v_mfma_f32_16x16x32_bf16 a[8:11], a[152:155], a[16:19], a[8:11]\n"
    "v_mfma_f32_16x16x32_bf16 a[12:15], a[152:155], a[20:23], a[12:15]\n"
    "v_mfma_f32_16x16x32_bf16 v[20:23], a[96:99], a[80:83], v[20:23]\n"
    "v_mfma_f32_16x16x32_bf16 v[28:31], a[100:103], a[80:83], v[28:31]\n"
    "v_mfma_f32_16x16x32_bf16 v[36:39], a[104:107], a[80:83], v[36:39]\n"
    "s_waitcnt lgkmcnt(0)\n"
    "s_barrier\n"
    "v_mfma_f32_16x16x32_bf16 v[44:47], a[108:111], a[80:83], v[44:47]\n"
    "v_mfma_f32_16x16x32_bf16 v[52:55], a[112:115], a[80:83], v[52:55]\n"
    "v_accvgpr_read_b32 v2, a220\n"
    "v_accvgpr_read_b32 v0, a220\n"
    "v_mfma_f32_16x16x32_bf16 v[60:63], a[116:119], a[80:83], v[60:63]\n"
    "v_mov_b32 v6, s16\n"
    "s_mul_i32 s97, s99, 0\n"
    "v_mfma_f32_16x16x32_bf16 v[68:71], a[120:123], a[80:83], v[68:71]\n"
    "s_add_u32 s97, s97, 50176\n"
    "v_mov_b32 v7, s17\n"
    "v_mfma_f32_16x16x32_bf16 v[76:79], a[124:127], a[80:83], v[76:79]\n"
    "v_readlane_b32 s52, v2, 0\n"
    "v_mfma_f32_16x16x32_bf16 v[84:87], a[96:99], a[84:87], v[84:87]\n"
    "buffer_store_dwordx4 v[200:203], v14, s[48:51], s97 offen sc1\n"
    "v_mfma_f32_16x16x32_bf16 v[92:95], a[100:103], a[84:87], v[92:95]\n"
    "v_readlane_b32 s53, v2, 1\n"
    "v_readlane_b32 s54, v2, 2\n"
    "v_mfma_f32_16x16x32_bf16 v[100:103], a[104:107], a[84:87], v[100:103]\n"
    "v_readlane_b32 s55, v2, 3\n"
    "v_readlane_b32 s56, v2, 4\n"
    "v_mfma_f32_16x16x32_bf16 v[108:111], a[108:111], a[84:87], v[108:111]\n"
    "v_readlane_b32 s57, v2, 5\n"
    "v_readlane_b32 s58, v2, 6\n"
    "v_mfma_f32_16x16x32_bf16 v[116:119], a[112:115], a[84:87], v[116:119]\n"
    "v_readlane_b32 s59, v2, 7\n"
    "v_readlane_b32 s60, v2, 8\n"
    "v_mfma_f32_16x16x32_bf16 v[124:127], a[116:119], a[84:87], v[124:127]\n"
    "v_readlane_b32 s61, v2, 9\n"
    "v_readlane_b32 s62, v2, 10\n"
    "v_mfma_f32_16x16x32_bf16 v[132:135], a[120:123], a[84:87], v[132:135]\n"
    "v_readlane_b32 s63, v2, 11\n"
    "v_readlane_b32 s64, v2, 12\n"
    "v_mfma_f32_16x16x32_bf16 v[140:143], a[124:127], a[84:87], v[140:143]\n"
    "v_readlane_b32 s65, v2, 13\n"
    "v_mfma_f32_16x16x32_bf16 v[148:151], a[96:99], a[88:91], v[148:151]\n"
    "v_readlane_b32 s66, v2, 14\n"
    "v_readlane_b32 s67, v2, 15\n"
    "v_mfma_f32_16x16x32_bf16 v[156:159], a[100:103], a[88:91], v[156:159]\n"
    "v_readlane_b32 s68, v2, 16\n"
    "v_mfma_f32_16x16x32_bf16 v[164:167], a[104:107], a[88:91], v[164:167]\n"
    "v_readlane_b32 s69, v2, 17\n"
    "v_readlane_b32 s70, v2, 18\n"
    "v_mfma_f32_16x16x32_bf16 v[172:175], a[108:111], a[88:91], v[172:175]\n"
    "v_readlane_b32 s71, v2, 19\n"
    "v_mfma_f32_16x16x32_bf16 v[180:183], a[112:115], a[88:91], v[180:183]\n"
    "v_readlane_b32 s72, v2, 20\n"
    "v_readlane_b32 s73, v2, 21\n"
    "v_mfma_f32_16x16x32_bf16 v[188:191], a[116:119], a[88:91], v[188:191]\n"
    "v_readlane_b32 s74, v2, 22\n"
    "s_mul_i32 s97, s99, 0\n"
    "v_mfma_f32_16x16x32_bf16 v[196:199], a[120:123], a[88:91], v[196:199]\n"
    "s_add_u32 s97, s97, 51200\n"
    "v_readlane_b32 s75, v2, 23\n"
    "v_mfma_f32_16x16x32_bf16 v[204:207], a[124:127], a[88:91], v[204:207]\n"
    "v_readlane_b32 s76, v2, 24\n"
    "v_mfma_f32_16x16x32_bf16 v[212:215], a[96:99], a[92:95], v[212:215]\n"
    "buffer_store_dwordx4 v[208:211], v14, s[48:51], s97 offen sc1\n"
    "v_mfma_f32_16x16x32_bf16 v[220:223], a[100:103], a[92:95], v[220:223]\n"
    "v_readlane_b32 s77, v2, 25\n"
    "v_readlane_b32 s78, v2, 26\n"
    "v_mfma_f32_16x16x32_bf16 v[228:231], a[104:107], a[92:95], v[228:231]\n"
    "v_readlane_b32 s79, v2, 27\n"
    "v_or_b32 v4, s62, v1\n"
    "v_mfma_f32_16x16x32_bf16 v[236:239], a[108:111], a[92:95], v[236:239]\n"
    "v_cmp_ne_u32 vcc, 0, v4\n"
    "v_mov_b32 v3, 1073741824\n"
    "v_mfma_f32_16x16x32_bf16 v[244:247], a[112:115], a[92:95], v[244:247]\n"
    "v_accvgpr_read_b32 v8, a248\n"
    "v_cndmask_b32 v12, 0, v3, vcc\n"
    "v_mfma_f32_16x16x32_bf16 v[252:255], a[116:119], a[92:95], v[252:255]\n"
    "v_accvgpr_read_b32 v9, a249\n"
    "v_accvgpr_read_b32 v10, a252\n"
    "v_mfma_f32_16x16x32_bf16 a[0:3], a[120:123], a[92:95], a[0:3]\n"
    "v_accvgpr_read_b32 v11, a253\n"
    "s_lshl_b32 s96, s62, 13\n"
    "v_mfma_f32_16x16x32_bf16 a[4:7], a[124:127], a[92:95], a[4:7]\n"
    "s_sub_u32 s95, s61, s96\n"
    "v_mfma_f32_16x16x32_bf16 a[168:171], a[128:131], a[80:83], a[168:171]\n"
    "v_add_u32 v2, s95, v1\n"
    "v_add_u32 v3, 0x10000, v2\n"
    "v_mfma_f32_16x16x32_bf16 a[172:175], a[132:135], a[80:83], a[172:175]\n"
    "v_add_u32 v4, s96, v1\n"
    "v_mfma_f32_16x16x32_bf16 a[176:179], a[128:131], a[84:87], a[176:179]\n"
    "s_lshl_b32 s96, s62, 10\n"
    "v_add_u32 v5, s96, v1\n"
    "v_mfma_f32_16x16x32_bf16 a[180:183], a[132:135], a[84:87], a[180:183]\n"
    "s_mov_b32 s80, s53\n"
    "v_mfma_f32_16x16x32_bf16 a[184:187], a[80:83], a[136:139], a[184:187]\n"
    "s_mov_b32 s82, -1\n"
    "s_mov_b32 s88, 0\n"
    "v_mfma_f32_16x16x32_bf16 a[188:191], a[84:87], a[136:139], a[188:191]\n"
    "s_mul_i32 s97, s99, 0\n"
    "s_add_u32 s97, s97, 52224\n"
    "v_mfma_f32_16x16x32_bf16 a[192:195], a[80:83], a[140:143], a[192:195]\n"
    "s_mov_b32 s41, 0\n"
    "s_mov_b32 s84, 1073741824\n"
    "v_mfma_f32_16x16x32_bf16 a[196:199], a[84:87], a[140:143], a[196:199]\n"
    "s_add_u32 s87, s67, 256\n"
    "v_mfma_f32_16x16x32_bf16 a[200:203], a[80:83], a[156:159], a[200:203]\n"
    "buffer_store_dwordx4 v[216:219], v14, s[48:51], s97 offen sc1\n"
    "v_mfma_f32_16x16x32_bf16 a[204:207], a[84:87], a[156:159], a[204:207]\n"
    "s_sub_u32 s87, s87, s96\n"
    "v_mfma_f32_16x16x32_bf16 a[208:211], a[80:83], a[160:163], a[208:211]\n"
    "s_add_u32 s36, s48, s87\n"
    "s_addc_u32 s37, s49, 0\n"
    "v_mfma_f32_16x16x32_bf16 a[212:215], a[84:87], a[160:163], a[212:215]\n"
    "s_add_u32 s38, s96, 256\n"
    "s_mov_b32 s39, 0x20000\n"
    "v_mfma_f32_16x16x32_bf16 a[8:11], a[164:167], a[80:83], a[8:11]\n"
    "s_mov_b32 s42, 0x3db8aa3b\n"
    "s_mov_b32 s43, 0x3db8aa3b\n"
    "v_mfma_f32_16x16x32_bf16 a[12:15], a[164:167], a[84:87], a[12:15]\n"
    "s_mov_b32 s98, 8192\n"
    "s_nop 7\n"
    "s_mul_i32 s97, s99, -4096\n"
    "s_add_u32 s97, s97, 57344\n"
    "buffer_store_dwordx4 v[224:227], v14, s[48:51], s97 offen sc1\n"
    "v_pk_mul_f32 v[20:21], v[20:21], s[42:43]\n"
    "v_pk_mul_f32 v[22:23], v[22:23], s[42:43]\n"
    "v_pk_mul_f32 v[28:29], v[28:29], s[42:43]\n"
    "v_pk_mul_f32 v[30:31], v[30:31], s[42:43]\n"
    "v_cvt_pk_bf16_f32 v16, v20, v21\n"
    "v_cvt_pk_bf16_f32 v17, v22, v23\n"
    "v_cvt_pk_bf16_f32 v18, v28, v29\n"
    "v_cvt_pk_bf16_f32 v19, v30, v31\n"
    "ds_write_b128 v10, v[16:19] offset:0\n"
    "v_pk_mul_f32 v[36:37], v[36:37], s[42:43]\n"
    "v_pk_mul_f32 v[38:39], v[38:39], s[42:43]\n"
    "v_pk_mul_f32 v[44:45], v[44:45], s[42:43]\n"
    "v_pk_mul_f32 v[46:47], v[46:47], s[42:43]\n"
    "v_cvt_pk_bf16_f32 v24, v36, v37\n"
    "v_cvt_pk_bf16_f32 v25, v38, v39\n"
    "v_cvt_pk_bf16_f32 v26, v44, v45\n"
    "v_cvt_pk_bf16_f32 v27, v46, v47\n"
    "ds_write_b128 v10, v[24:27] offset:1024\n"
    "v_pk_mul_f32 v[52:53], v[52:53], s[42:43]\n"
    "v_pk_mul_f32 v[54:55], v[54:55], s[42:43]\n"
    "v_pk_mul_f32 v[60:61], v[60:61], s[42:43]\n"
    "v_pk_mul_f32 v[62:63], v[62:63], s[42:43]\n"
    "v_cvt_pk_bf16_f32 v32, v52, v53\n"
    "v_cvt_pk_bf16_f32 v33, v54, v55\n"
    "v_cvt_pk_bf16_f32 v34, v60, v61\n"
    "v_cvt_pk_bf16_f32 v35, v62, v63\n"
    "ds_write_b128 v10, v[32:35] offset:2048\n"
    "v_pk_mul_f32 v[68:69], v[68:69], s[42:43]\n"
    "v_pk_mul_f32 v[70:71], v[70:71], s[42:43]\n"
    "v_pk_mul_f32 v[76:77], v[76:77], s[42:43]\n"
    "v_pk_mul_f32 v[78:79], v[78:79], s[42:43]\n"
    "v_cvt_pk_bf16_f32 v40, v68, v69\n"
    "v_cvt_pk_bf16_f32 v41, v70, v71\n"
    "v_cvt_pk_bf16_f32 v42, v76, v77\n"
    "v_cvt_pk_bf16_f32 v43, v78, v79\n"
    "ds_write_b128 v10, v[40:43] offset:3072\n"
    "v_pk_mul_f32 v[84:85], v[84:85], s[42:43]\n"
    "v_pk_mul_f32 v[86:87], v[86:87], s[42:43]\n"
    "s_mul_i32 s97, s99, -4096\n"
    "s_add_u32 s97, s97, 58368\n"
    "buffer_store_dwordx4 v[232:235], v14, s[48:51], s97 offen sc1\n"
    "v_pk_mul_f32 v[92:93], v[92:93], s[42:43]\n"
    "v_pk_mul_f32 v[94:95], v[94:95], s[42:43]\n"
    "v_cvt_pk_bf16_f32 v48, v84, v85\n"
    "v_cvt_pk_bf16_f32 v49, v86, v87\n"
    "v_cvt_pk_bf16_f32 v50, v92, v93\n"
    "v_cvt_pk_bf16_f32 v51, v94, v95\n"
    "ds_write_b128 v10, v[48:51] offset:32768\n"
    "v_pk_mul_f32 v[100:101], v[100:101], s[42:43]\n"
    "v_pk_mul_f32 v[102:103], v[102:103], s[42:43]\n"
    "v_pk_mul_f32 v[108:109], v[108:109], s[42:43]\n"
    "v_pk_mul_f32 v[110:111], v[110:111], s[42:43]\n"
    "v_cvt_pk_bf16_f32 v56, v100, v101\n"
    "v_cvt_pk_bf16_f32 v57, v102, v103\n"
    "v_cvt_pk_bf16_f32 v58, v108, v109\n"
    "v_cvt_pk_bf16_f32 v59, v110, v111\n"
    "ds_write_b128 v10, v[56:59] offset:33792\n"
    "v_pk_mul_f32 v[116:117], v[116:117], s[42:43]\n"
    "v_pk_mul_f32 v[118:119], v[118:119], s[42:43]\n"
    "v_pk_mul_f32 v[124:125], v[124:125], s[42:43]\n"
    "v_pk_mul_f32 v[126:127], v[126:127], s[42:43]\n"
    "v_cvt_pk_bf16_f32 v64, v116, v117\n"
    "v_cvt_pk_bf16_f32 v65, v118, v119\n"
    "v_cvt_pk_bf16_f32 v66, v124, v125\n"
    "v_cvt_pk_bf16_f32 v67, v126, v127\n"
    "ds_write_b128 v10, v[64:67] offset:34816\n"
    "v_pk_mul_f32 v[132:133], v[132:133], s[42:43]\n"
    "v_pk_mul_f32 v[134:135], v[134:135], s[42:43]\n"
    "v_pk_mul_f32 v[140:141], v[140:141], s[42:43]\n"
    "v_pk_mul_f32 v[142:143], v[142:143], s[42:43]\n"
    "v_cvt_pk_bf16_f32 v72, v132, v133\n"
    "v_cvt_pk_bf16_f32 v73, v134, v135\n"
    "v_cvt_pk_bf16_f32 v74, v140, v141\n"
    "v_cvt_pk_bf16_f32 v75, v142, v143\n"
    "ds_write_b128 v10, v[72:75] offset:35840\n"
    "v_pk_mul_f32 v[148:149], v[148:149], s[42:43]\n"
    "v_pk_mul_f32 v[150:151], v[150:151], s[42:43]\n"
    "v_pk_mul_f32 v[156:157], v[156:157], s[42:43]\n"
    "v_pk_mul_f32 v[158:159], v[158:159], s[42:43]\n"
    "s_mul_i32 s97, s99, -4096\n"
    "s_add_u32 s97, s97, 59392\n"
    "buffer_store_dwordx4 v[240:243], v14, s[48:51], s97 offen sc1\n"
    "v_cvt_pk_bf16_f32 v80, v148, v149\n"
    "v_cvt_pk_bf16_f32 v81, v150, v151\n"
    "v_cvt_pk_bf16_f32 v82, v156, v157\n"
    "v_cvt_pk_bf16_f32 v83, v158, v159\n"
    "ds_write_b128 v11, v[80:83] offset:0\n"
    "v_pk_mul_f32 v[164:165], v[164:165], s[42:43]\n"
    "v_pk_mul_f32 v[166:167], v[166:167], s[42:43]\n"
    "v_pk_mul_f32 v[172:173], v[172:173], s[42:43]\n"
    "v_pk_mul_f32 v[174:175], v[174:175], s[42:43]\n"
    "v_cvt_pk_bf16_f32 v88, v164, v165\n"
    "v_cvt_pk_bf16_f32 v89, v166, v167\n"
    "v_cvt_pk_bf16_f32 v90, v172, v173\n"
    "v_cvt_pk_bf16_f32 v91, v174, v175\n"
    "ds_write_b128 v11, v[88:91] offset:1024\n"
    "v_pk_mul_f32 v[180:181], v[180:181], s[42:43]\n"
    "v_pk_mul_f32 v[182:183], v[182:183], s[42:43]\n"
    "v_pk_mul_f32 v[188:189], v[188:189], s[42:43]\n"
    "v_pk_mul_f32 v[190:191], v[190:191], s[42:43]\n"
    "v_cvt_pk_bf16_f32 v96, v180, v181\n"
    "v_cvt_pk_bf16_f32 v97, v182, v183\n"
    "v_cvt_pk_bf16_f32 v98, v188, v189\n"
    "v_cvt_pk_bf16_f32 v99, v190, v191\n"
    "ds_write_b128 v11, v[96:99] offset:2048\n"
    "v_pk_mul_f32 v[196:197], v[196:197], s[42:43]\n"
    "v_pk_mul_f32 v[198:199], v[198:199], s[42:43]\n"
    "v_pk_mul_f32 v[204:205], v[204:205], s[42:43]\n"
    "v_pk_mul_f32 v[206:207], v[206:207], s[42:43]\n"
    "v_cvt_pk_bf16_f32 v104, v196, v197\n"
    "v_cvt_pk_bf16_f32 v105, v198, v199\n"
    "v_cvt_pk_bf16_f32 v106, v204, v205\n"
    "v_cvt_pk_bf16_f32 v107, v206, v207\n"
    "ds_write_b128 v11, v[104:107] offset:3072\n"
    "v_pk_mul_f32 v[212:213], v[212:213], s[42:43]\n"
    "v_pk_mul_f32 v[214:215], v[214:215], s[42:43]\n"
    "v_pk_mul_f32 v[220:221], v[220:221], s[42:43]\n"
    "v_pk_mul_f32 v[222:223], v[222:223], s[42:43]\n"
    "v_cvt_pk_bf16_f32 v112, v212, v213\n"
    "v_cvt_pk_bf16_f32 v113, v214, v215\n"
    "s_mul_i32 s97, s99, -4096\n"
    "s_add_u32 s97, s97, 60416\n"
    "buffer_store_dwordx4 v[248:251], v14, s[48:51], s97 offen sc1\n"
    "v_cvt_pk_bf16_f32 v114, v220, v221\n"
    "v_cvt_pk_bf16_f32 v115, v222, v223\n"
    "ds_write_b128 v11, v[112:115] offset:32768\n"
    "v_pk_mul_f32 v[228:229], v[228:229], s[42:43]\n"
    "v_pk_mul_f32 v[230:231], v[230:231], s[42:43]\n"
    "v_pk_mul_f32 v[236:237], v[236:237], s[42:43]\n"
    "v_pk_mul_f32 v[238:239], v[238:239], s[42:43]\n"
    "v_cvt_pk_bf16_f32 v120, v228, v229\n"
    "v_cvt_pk_bf16_f32 v121, v230, v231\n"
    "v_cvt_pk_bf16_f32 v122, v236, v237\n"
    "v_cvt_pk_bf16_f32 v123, v238, v239\n"
    "ds_write_b128 v11, v[120:123] offset:33792\n"
    "v_pk_mul_f32 v[244:245], v[244:245], s[42:43]\n"
    "v_pk_mul_f32 v[246:247], v[246:247], s[42:43]\n"
    "v_pk_mul_f32 v[252:253], v[252:253], s[42:43]\n"
    "v_pk_mul_f32 v[254:255], v[254:255], s[42:43]\n"
    "v_cvt_pk_bf16_f32 v128, v244, v245\n"
    "v_cvt_pk_bf16_f32 v129, v246, v247\n"
    "v_cvt_pk_bf16_f32 v130, v252, v253\n"
    "v_cvt_pk_bf16_f32 v131, v254, v255\n"
    "ds_write_b128 v11, v[128:131] offset:34816\n"
    "v_accvgpr_read_b32 v20, a0\n"
    "v_accvgpr_read_b32 v21, a1\n"
    "v_accvgpr_read_b32 v22, a2\n"
    "v_accvgpr_read_b32 v23, a3\n"
    "v_accvgpr_read_b32 v28, a4\n"
    "v_accvgpr_read_b32 v29, a5\n"
    "v_accvgpr_read_b32 v30, a6\n"
    "v_accvgpr_read_b32 v31, a7\n"
    "v_pk_mul_f32 v[20:21], v[20:21], s[42:43]\n"
    "v_pk_mul_f32 v[22:23], v[22:23], s[42:43]\n"
    "v_pk_mul_f32 v[28:29], v[28:29], s[42:43]\n"
    "v_pk_mul_f32 v[30:31], v[30:31], s[42:43]\n"
    "v_cvt_pk_bf16_f32 v136, v20, v21\n"
    "v_cvt_pk_bf16_f32 v137, v22, v23\n"
    "v_cvt_pk_bf16_f32 v138, v28, v29\n"
    "v_cvt_pk_bf16_f32 v139, v30, v31\n"
    "ds_write_b128 v11, v[136:139] offset:35840\n"
    "s_lshl_b32 s96, s62, 10\n"
    "v_add_u32 v239, s96, v3\n"
    "s_lshl_b32 s96, s62, 13\n"
    "s_add_u32 s96, s96, 32768\n"
    "v_add_u32 v238, s96, v3\n"
    "s_waitcnt vmcnt(0) lgkmcnt(0)\n"
    "s_barrier\n"
    "buffer_store_dwordx2 v[6:7], v12, s[48:51], s66 offen sc1\n"
    "s_bitcmp1_b32 s55, 2\n"
    "s_cbranch_scc0 .Lf_q0\n"
    "buffer_store_dwordx4 v[16:19], v8, s[48:51], 0 offen offset:0 sc1\n"
    "buffer_store_dwordx4 v[24:27], v8, s[48:51], 0 offen offset:1024 sc1\n"
    "buffer_store_dwordx4 v[32:35], v8, s[48:51], 0 offen offset:2048 sc1\n"
    "buffer_store_dwordx4 v[40:43], v8, s[48:51], 0 offen offset:3072 sc1\n"
    "buffer_store_dwordx4 v[48:51], v8, s[48:51], s98 offen offset:0 sc1\n"
    "buffer_store_dwordx4 v[56:59], v8, s[48:51], s98 offen offset:1024 sc1\n"
    "buffer_store_dwordx4 v[64:67], v8, s[48:51], s98 offen offset:2048 sc1\n"
    "buffer_store_dwordx4 v[72:75], v8, s[48:51], s98 offen offset:3072 sc1\n"
    "buffer_store_dwordx4 v[80:83], v9, s[48:51], 0 offen offset:0 sc1\n"
    "buffer_store_dwordx4 v[88:91], v9, s[48:51], 0 offen offset:1024 sc1\n"
    "buffer_store_dwordx4 v[96:99], v9, s[48:51], 0 offen offset:2048 sc1\n"
    "buffer_store_dwordx4 v[104:107], v9, s[48:51], 0 offen offset:3072 sc1\n"
    "buffer_store_dwordx4 v[112:115], v9, s[48:51], s98 offen offset:0 sc1\n"
    "buffer_store_dwordx4 v[120:123], v9, s[48:51], s98 offen offset:1024 sc1\n"
    "buffer_store_dwordx4 v[128:131], v9, s[48:51], s98 offen offset:2048 sc1\n"
    "buffer_store_dwordx4 v[136:139], v9, s[48:51], s98 offen offset:3072 sc1\n"
    ".Lf_q0:\n"
    "s_cmp_lg_u32 s72, 0\n"
    "s_cbranch_scc0 .Lf_w0\n"
    "v_lshrrev_b32 v144, 6, v1\n"
    "v_mul_u32_u24 v144, 896, v144\n"
    "v_and_b32 v145, 48, v1\n"
    "v_add_u32 v144, v144, v145\n"
    "v_add_u32 v145, 14336, v144\n"
    "v_add_u32 v146, s61, v1\n"
    "s_add_u32 s44, s69, 0\n"
    "s_min_u32 s45, s44, 125\n"
    "s_mul_i32 s96, s45, 57\n"
    "s_lshr_b32 s96, s96, 9\n"
    "s_mul_i32 s97, s96, 9\n"
    "s_sub_u32 s97, s45, s97\n"
    "s_mul_i32 s81, s96, 18\n"
    "s_add_u32 s81, s81, s97\n"
    "s_lshl_b32 s81, s81, 10\n"
    "s_add_u32 s81, s81, s63\n"
    "s_cmp_lt_u32 s44, 126\n"
    "s_cselect_b32 s81, s81, 1073741824\n"
    "s_mul_i32 s44, s97, 28672\n"
    "s_cmp_lt_u32 s97, 8\n"
    "s_cselect_b32 s44, s44, 0\n"
    "s_cselect_b64 s[42:43], s[76:77], s[78:79]\n"
    "s_lshl_b32 s96, s96, 6\n"
    "s_add_u32 s44, s44, s96\n"
    "s_add_u32 s42, s42, s44\n"
    "s_addc_u32 s43, s43, 0\n"
    "s_add_u32 m0, s61, 0\n"
    "s_nop 0\n"
    "global_load_lds_dwordx4 v144, s[42:43]\n"
    "s_add_u32 m0, s61, 1024\n"
    "s_nop 0\n"
    "global_load_lds_dwordx4 v145, s[42:43]\n"
    "s_add_u32 s44, s69, 1\n"
    "s_min_u32 s45, s44, 125\n"
    "s_mul_i32 s96, s45, 57\n"
    "s_lshr_b32 s96, s96, 9\n"
    "s_mul_i32 s97, s96, 9\n"
    "s_sub_u32 s97, s45, s97\n"
    "s_mul_i32 s83, s96, 18\n"
    "s_add_u32 s83, s83, s97\n"
    "s_lshl_b32 s83, s83, 10\n"
    "s_add_u32 s83, s83, s63\n"
    "s_cmp_lt_u32 s44, 126\n"
    "s_cselect_b32 s83, s83, 1073741824\n"
    "s_mul_i32 s44, s97, 28672\n"
    "s_cmp_lt_u32 s97, 8\n"
    "s_cselect_b32 s44, s44, 0\n"
    "s_cselect_b64 s[42:43], s[76:77], s[78:79]\n"
    "s_lshl_b32 s96, s96, 6\n"
    "s_add_u32 s44, s44, s96\n"
    "s_add_u32 s42, s42, s44\n"
    "s_addc_u32 s43, s43, 0\n"
    "s_add_u32 m0, s61, 2048\n"
    "s_nop 0\n"
    "global_load_lds_dwordx4 v144, s[42:43]\n"
    "s_add_u32 m0, s61, 3072\n"
    "s_nop 0\n"
    "global_load_lds_dwordx4 v145, s[42:43]\n"
    "s_add_u32 s44, s69, 2\n"
    "s_min_u32 s45, s44, 125\n"
    "s_mul_i32 s96, s45, 57\n"
    "s_lshr_b32 s96, s96, 9\n"
    "s_mul_i32 s97, s96, 9\n"
    "s_sub_u32 s97, s45, s97\n"
    "s_mul_i32 s86, s96, 18\n"
    "s_add_u32 s86, s86, s97\n"
    "s_lshl_b32 s86, s86, 10\n"
    "s_add_u32 s86, s86, s63\n"
    "s_cmp_lt_u32 s44, 126\n"
    "s_cselect_b32 s86, s86, 1073741824\n"
    "s_mul_i32 s44, s97, 28672\n"
    "s_cmp_lt_u32 s97, 8\n"
    "s_cselect_b32 s44, s44, 0\n"
    "s_cselect_b64 s[42:43], s[76:77], s[78:79]\n"
    "s_lshl_b32 s96, s96, 6\n"
    "s_add_u32 s44, s44, s96\n"
    "s_add_u32 s42, s42, s44\n"
    "s_addc_u32 s43, s43, 0\n"
    "s_add_u32 m0, s61, 4096\n"
    "s_nop 0\n"
    "global_load_lds_dwordx4 v144, s[42:43]\n"
    "s_add_u32 m0, s61, 5120\n"
    "s_nop 0\n"
    "global_load_lds_dwordx4 v145, s[42:43]\n"
    "s_add_u32 s44, s69, 3\n"
    "s_min_u32 s45, s44, 125\n"
    "s_mul_i32 s96, s45, 57\n"
    "s_lshr_b32 s96, s96, 9\n"
    "s_mul_i32 s97, s96, 9\n"
    "s_sub_u32 s97, s45, s97\n"
    "s_mul_i32 s89, s96, 18\n"
    "s_add_u32 s89, s89, s97\n"
    "s_lshl_b32 s89, s89, 10\n"
    "s_add_u32 s89, s89, s63\n"
    "s_cmp_lt_u32 s44, 126\n"
    "s_cselect_b32 s89, s89, 1073741824\n"
    "s_mul_i32 s44, s97, 28672\n"
    "s_cmp_lt_u32 s97, 8\n"
    "s_cselect_b32 s44, s44, 0\n"
    "s_cselect_b64 s[42:43], s[76:77], s[78:79]\n"
    "s_lshl_b32 s96, s96, 6\n"
    "s_add_u32 s44, s44, s96\n"
    "s_add_u32 s42, s42, s44\n"
    "s_addc_u32 s43, s43, 0\n"
    "s_add_u32 m0, s61, 6144\n"
    "s_nop 0\n"
    "global_load_lds_dwordx4 v144, s[42:43]\n"
    "s_add_u32 m0, s61, 7168\n"
    "s_nop 0\n"
    "global_load_lds_dwordx4 v145, s[42:43]\n"
    ".Lf_w0:\n"
    "v_accvgpr_read_b32 v36, a184\n"
    "v_accvgpr_read_b32 v37, a185\n"
    "v_accvgpr_read_b32 v38, a186\n"
    "v_accvgpr_read_b32 v39, a187\n"
    "v_accvgpr_read_b32 v44, a188\n"
    "v_accvgpr_read_b32 v45, a189\n"
    "v_accvgpr_read_b32 v46, a190\n"
    "v_accvgpr_read_b32 v47, a191\n"
    "v_accvgpr_read_b32 v52, a192\n"
    "v_accvgpr_read_b32 v53, a193\n"
    "v_accvgpr_read_b32 v54, a194\n"
    "v_accvgpr_read_b32 v55, a195\n"
    "v_accvgpr_read_b32 v60, a196\n"
    "v_accvgpr_read_b32 v61, a197\n"
    "v_accvgpr_read_b32 v62, a198\n"
    "v_accvgpr_read_b32 v63, a199\n"
    "v_accvgpr_read_b32 v68, a200\n"
    "v_accvgpr_read_b32 v69, a201\n"
    "v_accvgpr_read_b32 v70, a202\n"
    "v_accvgpr_read_b32 v71, a203\n"
    "v_accvgpr_read_b32 v76, a204\n"
    "v_accvgpr_read_b32 v77, a205\n"
    "v_accvgpr_read_b32 v78, a206\n"
    "v_accvgpr_read_b32 v79, a207\n"
    "v_accvgpr_read_b32 v84, a208\n"
    "v_accvgpr_read_b32 v85, a209\n"
    "v_accvgpr_read_b32 v86, a210\n"
    "v_accvgpr_read_b32 v87, a211\n"
    "v_accvgpr_read_b32 v92, a212\n"
    "v_accvgpr_read_b32 v93, a213\n"
    "v_accvgpr_read_b32 v94, a214\n"
    "v_accvgpr_read_b32 v95, a215\n"
    "v_accvgpr_read_b32 v100, a168\n"
    "v_accvgpr_read_b32 v101, a169\n"
    "v_accvgpr_read_b32 v102, a170\n"
    "v_accvgpr_read_b32 v103, a171\n"
    "v_accvgpr_read_b32 v108, a172\n"
    "v_accvgpr_read_b32 v109, a173\n"
    "v_accvgpr_read_b32 v110, a174\n"
    "v_accvgpr_read_b32 v111, a175\n"
    "v_accvgpr_read_b32 v116, a176\n"
    "v_accvgpr_read_b32 v117, a177\n"
    "v_accvgpr_read_b32 v118, a178\n"
    "v_accvgpr_read_b32 v119, a179\n"
    "v_accvgpr_read_b32 v124, a180\n"
    "v_accvgpr_read_b32 v125, a181\n"
    "v_accvgpr_read_b32 v126, a182\n"
    "v_accvgpr_read_b32 v127, a183\n"
    "s_bitcmp1_b32 s55, 1\n"
    "s_cbranch_scc1 .Lf_qp\n"
    "ds_read_b128 a[192:195], v238 offset:0\n"
    "ds_read_b128 a[196:199], v238 offset:1024\n"
    "ds_read_b128 a[200:203], v238 offset:2048\n"
    "ds_read_b128 a[204:207], v238 offset:3072\n"
    "ds_read_b128 a[208:211], v238 offset:4096\n"
    "ds_read_b128 a[212:215], v238 offset:5120\n"
    "ds_read_b128 a[216:219], v238 offset:6144\n"
    "ds_read_b128 a[220:223], v238 offset:7168\n"
    "ds_read_b128 a[224:227], v238 offset:32768\n"
    "ds_read_b128 a[228:231], v238 offset:33792\n"
    "ds_read_b128 a[232:235], v238 offset:34816\n"
    "ds_read_b128 a[236:239], v238 offset:35840\n"
    "ds_read_b128 a[240:243], v238 offset:36864\n"
    "ds_read_b128 a[244:247], v238 offset:37888\n"
    "ds_read_b128 a[248:251], v238 offset:38912\n"
    "ds_read_b128 a[252:255], v238 offset:39936\n"
    "s_branch .Lf_qd\n"
    ".Lf_qp:\n"
    "ds_read_b128 a[32:35], v238 offset:0\n"
    "ds_read_b128 a[36:39], v238 offset:1024\n"
    "ds_read_b128 a[40:43], v238 offset:2048\n"
    "ds_read_b128 a[44:47], v238 offset:3072\n"
    "ds_read_b128 a[48:51], v238 offset:4096\n"
    "ds_read_b128 a[52:55], v238 offset:5120\n"
    "ds_read_b128 a[56:59], v238 offset:6144\n"
    "ds_read_b128 a[60:63], v238 offset:7168\n"
    ".Lf_qd:\n"
    "v_mul_f32 v132, 0x3fb8aa3b, v36\n"
    "v_mul_f32 v133, 0x3fb8aa3b, v37\n"
    "v_mul_f32 v134, 0x3fb8aa3b, v38\n"
    "v_mul_f32 v135, 0x3fb8aa3b, v39\n"
    "v_mul_f32 v140, 0x3fb8aa3b, v44\n"
    "v_mul_f32 v141, 0x3fb8aa3b, v45\n"
    "v_mul_f32 v142, 0x3fb8aa3b, v46\n"
    "v_mul_f32 v143, 0x3fb8aa3b, v47\n"
    "v_mul_f32 v148, 0x3fb8aa3b, v52\n"
    "v_mul_f32 v149, 0x3fb8aa3b, v53\n"
    "v_mul_f32 v150, 0x3fb8aa3b, v54\n"
    "v_mul_f32 v151, 0x3fb8aa3b, v55\n"
    "v_mul_f32 v156, 0x3fb8aa3b, v60\n"
    "v_mul_f32 v157, 0x3fb8aa3b, v61\n"
    "v_mul_f32 v158, 0x3fb8aa3b, v62\n"
    "v_mul_f32 v159, 0x3fb8aa3b, v63\n"
    "v_exp_f32 v132, v132 clamp\n"
    "v_max_f32 v36, 0x358637bd, v36\n"
    "v_exp_f32 v133, v133 clamp\n"
    "v_max_f32 v37, 0x358637bd, v37\n"
    "v_exp_f32 v134, v134 clamp\n"
    "v_max_f32 v38, 0x358637bd, v38\n"
    "v_exp_f32 v135, v135 clamp\n"
    "v_max_f32 v39, 0x358637bd, v39\n"
    "v_exp_f32 v140, v140 clamp\n"
    "v_max_f32 v44, 0x358637bd, v44\n"
    "v_exp_f32 v141, v141 clamp\n"
    "v_max_f32 v45, 0x358637bd, v45\n"
    "v_exp_f32 v142, v142 clamp\n"
    "v_max_f32 v46, 0x358637bd, v46\n"
    "v_exp_f32 v143, v143 clamp\n"
    "v_max_f32 v47, 0x358637bd, v47\n"
    "v_exp_f32 v148, v148 clamp\n"
    "v_max_f32 v52, 0x358637bd, v52\n"
    "v_exp_f32 v149, v149 clamp\n"
    "v_max_f32 v53, 0x358637bd, v53\n"
    "v_exp_f32 v150, v150 clamp\n"
    "v_max_f32 v54, 0x358637bd, v54\n"
    "v_exp_f32 v151, v151 clamp\n"
    "v_max_f32 v55, 0x358637bd, v55\n"
    "v_exp_f32 v156, v156 clamp\n"
    "v_max_f32 v60, 0x358637bd, v60\n"
    "v_exp_f32 v157, v157 clamp\n"
    "v_max_f32 v61, 0x358637bd, v61\n"
    "v_exp_f32 v158, v158 clamp\n"
    "v_max_f32 v62, 0x358637bd, v62\n"
    "v_exp_f32 v159, v159 clamp\n"
    "v_max_f32 v63, 0x358637bd, v63\n"
    "v_add_f32 v36, v36, v132\n"
    "v_add_f32 v37, v37, v133\n"
    "v_add_f32 v38, v38, v134\n"
    "v_add_f32 v39, v39, v135\n"
    "v_add_f32 v44, v44, v140\n"
    "v_add_f32 v45, v45, v141\n"
    "v_add_f32 v46, v46, v142\n"
    "v_add_f32 v47, v47, v143\n"
    "v_add_f32 v52, v52, v148\n"
    "v_add_f32 v53, v53, v149\n"
    "v_add_f32 v54, v54, v150\n"
    "v_add_f32 v55, v55, v151\n"
    "v_add_f32 v60, v60, v156\n"
    "v_add_f32 v61, v61, v157\n"
    "v_add_f32 v62, v62, v158\n"
    "v_add_f32 v63, v63, v159\n"
    "s_cmp_eq_u32 s60, 0\n"
    "s_cselect_b32 s92, 0, 0x00010001\n"
    "s_cselect_b32 s93, 0, 0x00010001\n"
    "s_cselect_b32 s94, 0x10001000, 0\n"
    "s_cselect_b32 s95, 0x10001000, 0\n"
    "s_cselect_b32 s46, 0, 0xffff\n"
    "s_mov_b32 s47, 0\n"
    "s_mov_b32 s90, 0\n"
    "s_cselect_b32 s91, 0xffff0000, 0\n"
    "v_mov_b32 v132, 0x3f803f80\n"
    "v_cvt_pk_bf16_f32 v164, v36, v37\n"
    "v_cvt_pk_bf16_f32 v165, v38, v39\n"
    "v_cvt_pk_bf16_f32 v166, v44, v45\n"
    "v_cvt_pk_bf16_f32 v167, v46, v47\n"
    "v_cvt_pk_bf16_f32 v172, v52, v53\n"
    "v_cvt_pk_bf16_f32 v173, v54, v55\n"
    "v_cvt_pk_bf16_f32 v174, v60, v61\n"
    "v_cvt_pk_bf16_f32 v175, v62, v63\n"
    "v_cvt_pk_bf16_f32 v180, v68, v69\n"
    "v_cvt_pk_bf16_f32 v181, v70, v71\n"
    "v_cvt_pk_bf16_f32 v182, v76, v77\n"
    "v_cvt_pk_bf16_f32 v183, v78, v79\n"
    "v_cvt_pk_bf16_f32 v188, v84, v85\n"
    "v_cvt_pk_bf16_f32 v189, v86, v87\n"
    "v_cvt_pk_bf16_f32 v190, v92, v93\n"
    "v_cvt_pk_bf16_f32 v191, v94, v95\n"
    "v_cndmask_b32 v180, v180, v132, s[92:93]\n"
    "v_cndmask_b32 v181, v181, v132, s[92:93]\n"
    "v_cndmask_b32 v182, v182, v132, s[92:93]\n"
    "v_cndmask_b32 v183, v183, v132, s[92:93]\n"
    "v_cndmask_b32 v188, v188, v132, s[94:95]\n"
    "v_cndmask_b32 v189, v189, v132, s[94:95]\n"
    "v_cndmask_b32 v190, v190, v132, s[94:95]\n"
    "v_cndmask_b32 v191, v191, v132, s[94:95]\n"
    "s_lshl_b32 s96, s62, 12\n"
    "v_add_u32 v13, s96, v3\n"
    "v_mfma_f32_16x16x32_bf16 v[196:199], v[164:167], v[180:183], 0\n"
    "v_mfma_f32_16x16x32_bf16 v[204:207], v[172:175], v[180:183], 0\n"
    "v_mfma_f32_16x16x32_bf16 v[212:215], v[164:167], v[188:191], 0\n"
    "v_mfma_f32_16x16x32_bf16 v[220:223], v[172:175], v[188:191], 0\n"
    "s_nop 7\n"
    "ds_write_b128 v13, v[196:199] offset:0\n"
    "ds_write_b128 v13, v[204:207] offset:1024\n"
    "ds_write_b128 v13, v[212:215] offset:2048\n"
    "ds_write_b128 v13, v[220:223] offset:3072\n"
    "v_lshrrev_b32 v13, 1, v1\n"
    "v_and_b32 v13, 0xf8, v13\n"
    "s_cmp_lg_u32 s72, 0\n"
    "s_cbranch_scc0 .Lf_w1\n"
    "s_waitcnt vmcnt(0)\n"
    "ds_read_b64_tr_b16 v[152:153], v146 offset:0\n"
    "ds_read_b64_tr_b16 v[154:155], v146 offset:1024\n"
    "ds_read_b64_tr_b16 v[160:161], v146 offset:8\n"
    "ds_read_b64_tr_b16 v[162:163], v146 offset:1032\n"
    "ds_read_b64_tr_b16 v[168:169], v146 offset:2048\n"
    "ds_read_b64_tr_b16 v[170:171], v146 offset:3072\n"
    "ds_read_b64_tr_b16 v[176:177], v146 offset:2056\n"
    "ds_read_b64_tr_b16 v[178:179], v146 offset:3080\n"
    "ds_read_b64_tr_b16 v[184:185], v146 offset:4096\n"
    "ds_read_b64_tr_b16 v[186:187], v146 offset:5120\n"
    "ds_read_b64_tr_b16 v[192:193], v146 offset:4104\n"
    "ds_read_b64_tr_b16 v[194:195], v146 offset:5128\n"
    "ds_read_b64_tr_b16 v[200:201], v146 offset:6144\n"
    "ds_read_b64_tr_b16 v[202:203], v146 offset:7168\n"
    "ds_read_b64_tr_b16 v[208:209], v146 offset:6152\n"
    "ds_read_b64_tr_b16 v[210:211], v146 offset:7176\n"
    ".Lf_w1:\n"
    "s_cmp_lg_u32 s62, 0\n"
    "s_cbranch_scc1 .Lf_got\n"
    "s_mov_b32 s45, 0xaaaa\n"
    "s_mov_b32 s85, 0\n"
    ".Lf_spin:\n"
    "buffer_load_dwordx2 v[14:15], v13, s[48:51], s67 offen sc1\n"
    "s_waitcnt vmcnt(0)\n"
    "v_cmp_eq_u64 vcc, s[16:17], v[14:15]\n"
    "s_nop 1\n"
    "s_and_b32 s87, vcc_lo, s45\n"
    "s_cmp_eq_u32 s87, s45\n"
    "s_cbranch_scc1 .Lf_got\n"
    "s_add_u32 s85, s85, 1\n"
    "s_cmp_gt_u32 s85, 0x40000\n"
    "s_cbranch_scc1 .Lf_got\n"
    "s_sleep 8\n"
    "s_branch .Lf_spin\n"
    ".Lf_got:\n"
    "s_cmp_eq_u32 s72, 0\n"
    "s_cselect_b32 s87, s75, 1073741824\n"
    "s_waitcnt vmcnt(0) lgkmcnt(0)\n"
    "s_barrier\n"
    "buffer_store_dwordx2 v[6:7], v12, s[48:51], s87 offen sc1\n"
    "s_and_b32 s44, s54, 1\n"
    "s_lshl_b32 s44, s44, 15\n"
    "s_sub_u32 s43, s61, s44\n"
    "s_add_u32 s44, s61, s44\n"
    "ds_read_b128 v[196:199], v239 offset:0\n"
    "ds_read_b128 v[204:207], v239 offset:4096\n"
    "ds_read_b128 v[212:215], v239 offset:8192\n"
    "ds_read_b128 v[220:223], v239 offset:12288\n"
    "s_cmp_eq_u32 s80, s82\n"
    "s_cselect_b32 s80, s58, s80\n"
    "v_mul_f32 v132, 0x3fb8aa3b, v100\n"
    "v_mul_f32 v133, 0x3fb8aa3b, v101\n"
    "s_add_u32 m0, s44, 0\n"
    "s_add_u32 s85, s80, 4096\n"
    "v_mul_f32 v134, 0x3fb8aa3b, v102\n"
    "v_mul_f32 v135, 0x3fb8aa3b, v103\n"
    "v_mul_f32 v140, 0x3fb8aa3b, v108\n"
    "buffer_load_dwordx4 v4, s[48:51], s80 offen offset:0 sc1 lds\n"
    "v_mul_f32 v141, 0x3fb8aa3b, v109\n"
    "v_mul_f32 v142, 0x3fb8aa3b, v110\n"
    "v_mul_f32 v143, 0x3fb8aa3b, v111\n"
    "buffer_load_dwordx4 v4, s[48:51], s80 offen offset:1024 sc1 lds\n"
    "v_mul_f32 v148, 0x3fb8aa3b, v116\n"
    "v_mul_f32 v149, 0x3fb8aa3b, v117\n"
    "v_mul_f32 v150, 0x3fb8aa3b, v118\n"
    "buffer_load_dwordx4 v4, s[48:51], s80 offen offset:2048 sc1 lds\n"
    "v_mul_f32 v151, 0x3fb8aa3b, v119\n"
    "v_mul_f32 v156, 0x3fb8aa3b, v124\n"
    "v_mul_f32 v157, 0x3fb8aa3b, v125\n"
    "buffer_load_dwordx4 v4, s[48:51], s80 offen offset:3072 sc1 lds\n"
    "v_mul_f32 v158, 0x3fb8aa3b, v126\n"
    "v_mul_f32 v159, 0x3fb8aa3b, v127\n"
    "v_exp_f32 v132, v132 clamp\n"
    "s_add_u32 m0, s44, 4096\n"
    "s_add_u32 s80, s80, 32768\n"
    "v_max_f32 v100, 0x358637bd, v100\n"
    "v_exp_f32 v133, v133 clamp\n"
    "v_max_f32 v101, 0x358637bd, v101\n"
    "buffer_load_dwordx4 v4, s[48:51], s85 offen offset:0 sc1 lds\n"
    "v_exp_f32 v134, v134 clamp\n"
    "v_max_f32 v102, 0x358637bd, v102\n"
    "v_exp_f32 v135, v135 clamp\n"
    "buffer_load_dwordx4 v4, s[48:51], s85 offen offset:1024 sc1 lds\n"
    "v_max_f32 v103, 0x358637bd, v103\n"
    "v_exp_f32 v140, v140 clamp\n"
    "v_max_f32 v108, 0x358637bd, v108\n"
    "buffer_load_dwordx4 v4, s[48:51], s85 offen offset:2048 sc1 lds\n"
    "v_exp_f32 v141, v141 clamp\n"
    "v_max_f32 v109, 0x358637bd, v109\n"
    "v_exp_f32 v142, v142 clamp\n"
    "buffer_load_dwordx4 v4, s[48:51], s85 offen offset:3072 sc1 lds\n"
    "v_max_f32 v110, 0x358637bd, v110\n"
    "v_exp_f32 v143, v143 clamp\n"
    "v_max_f32 v111, 0x358637bd, v111\n"
    "s_cmp_eq_u32 s80, s82\n"
    "s_cselect_b32 s80, s58, s80\n"
    "v_exp_f32 v148, v148 clamp\n"
    "v_max_f32 v116, 0x358637bd, v116\n"
    "s_add_u32 m0, s43, 32768\n"
    "s_add_u32 s85, s80, 4096\n"
    "v_exp_f32 v149, v149 clamp\n"
    "v_max_f32 v117, 0x358637bd, v117\n"
    "v_exp_f32 v150, v150 clamp\n"
    "buffer_load_dwordx4 v4, s[48:51], s80 offen offset:0 sc1 lds\n"
    "v_max_f32 v118, 0x358637bd, v118\n"
    "v_exp_f32 v151, v151 clamp\n"
    "v_max_f32 v119, 0x358637bd, v119\n"
    "buffer_load_dwordx4 v4, s[48:51], s80 offen offset:1024 sc1 lds\n"
    "v_exp_f32 v156, v156 clamp\n"
    "v_max_f32 v124, 0x358637bd, v124\n"
    "v_exp_f32 v157, v157 clamp\n"
    "buffer_load_dwordx4 v4, s[48:51], s80 offen offset:2048 sc1 lds\n"
    "v_max_f32 v125, 0x358637bd, v125\n"
    "v_exp_f32 v158, v158 clamp\n"
    "v_max_f32 v126, 0x358637bd, v126\n"
    "buffer_load_dwordx4 v4, s[48:51], s80 offen offset:3072 sc1 lds\n"
    "v_exp_f32 v159, v159 clamp\n"
    "v_max_f32 v127, 0x358637bd, v127\n"
    "v_add_f32 v100, v100, v132\n"
    "s_add_u32 m0, s43, 36864\n"
    "s_add_u32 s80, s80, 32768\n"
    "v_add_f32 v101, v101, v133\n"
    "v_add_f32 v102, v102, v134\n"
    "v_add_f32 v103, v103, v135\n"
    "buffer_load_dwordx4 v4, s[48:51], s85 offen offset:0 sc1 lds\n"
    "v_add_f32 v108, v108, v140\n"
    "v_add_f32 v109, v109, v141\n"
    "v_add_f32 v110, v110, v142\n"
    "buffer_load_dwordx4 v4, s[48:51], s85 offen offset:1024 sc1 lds\n"
    "v_add_f32 v111, v111, v143\n"
    "v_add_f32 v116, v116, v148\n"
    "v_add_f32 v117, v117, v149\n"
    "buffer_load_dwordx4 v4, s[48:51], s85 offen offset:2048 sc1 lds\n"
    "v_add_f32 v118, v118, v150\n"
    "v_add_f32 v119, v119, v151\n"
    "v_add_f32 v124, v124, v156\n"
    "buffer_load_dwordx4 v4, s[48:51], s85 offen offset:3072 sc1 lds\n"
    "v_add_f32 v125, v125, v157\n"
    "v_add_f32 v126, v126, v158\n"
    "v_add_f32 v127, v127, v159\n"
    "s_waitcnt lgkmcnt(2)\n"
    "v_pk_add_f32 v[196:197], v[196:197], v[204:205]\n"
    "v_pk_add_f32 v[198:199], v[198:199], v[206:207]\n"
    "s_waitcnt lgkmcnt(1)\n"
    "v_pk_add_f32 v[196:197], v[196:197], v[212:213]\n"
    "v_pk_add_f32 v[198:199], v[198:199], v[214:215]\n"
    "s_waitcnt lgkmcnt(0)\n"
    "v_pk_add_f32 v[196:197], v[196:197], v[220:221]\n"
    "v_pk_add_f32 v[198:199], v[198:199], v[222:223]\n"
    "buffer_store_dwordx4 v[196:199], v1, s[48:51], s71 offen sc1\n"
    "s_lshl_b32 s96, s62, 14\n"
    "v_add_u32 v230, s96, v1\n"
    "s_cmp_lg_u32 s72, 0\n"
    "s_cbranch_scc1 .Lf_help\n"
    "v_cvt_pk_bf16_f32 v164, v100, v101\n"
    "v_cvt_pk_bf16_f32 v165, v102, v103\n"
    "v_cvt_pk_bf16_f32 v166, v108, v109\n"
    "v_cvt_pk_bf16_f32 v167, v110, v111\n"
    "v_cvt_pk_bf16_f32 v172, v116, v117\n"
    "v_cvt_pk_bf16_f32 v173, v118, v119\n"
    "v_cvt_pk_bf16_f32 v174, v124, v125\n"
    "v_cvt_pk_bf16_f32 v175, v126, v127\n"
    "v_cndmask_b32 v164, v164, 0, s[46:47]\n"
    "v_cndmask_b32 v165, v165, 0, s[46:47]\n"
    "v_cndmask_b32 v166, v166, 0, s[90:91]\n"
    "v_cndmask_b32 v167, v167, 0, s[90:91]\n"
    "v_cndmask_b32 v172, v172, 0, s[46:47]\n"
    "v_cndmask_b32 v173, v173, 0, s[46:47]\n"
    "v_cndmask_b32 v174, v174, 0, s[90:91]\n"
    "v_cndmask_b32 v175, v175, 0, s[90:91]\n"
    "v_accvgpr_write_b32 a16, v164\n"
    "v_accvgpr_write_b32 a17, v165\n"
    "v_accvgpr_write_b32 a18, v166\n"
    "v_accvgpr_write_b32 a19, v167\n"
    "v_accvgpr_write_b32 a20, v172\n"
    "v_accvgpr_write_b32 a21, v173\n"
    "v_accvgpr_write_b32 a22, v174\n"
    "v_accvgpr_write_b32 a23, v175\n"
    "v_accvgpr_read_b32 v228, a8\n"
    "v_accvgpr_read_b32 v229, a12\n"
    "v_mul_f32 v228, 0xbfb8aa3b, v228\n"
    "v_mul_f32 v229, 0xbfb8aa3b, v229\n"
    "v_exp_f32 v228, v228\n"
    "v_exp_f32 v229, v229\n"
    "v_add_f32 v228, 1.0, v228\n"
    "v_add_f32 v229, 1.0, v229\n"
    "v_rcp_f32 v228, v228\n"
    "v_rcp_f32 v229, v229\n"
    "s_mul_i32 s96, s62, 28672\n"
    "v_add_u32 v231, s96, v1\n"
    "v_bfe_u32 v236, v1, 4, 4\n"
    "v_lshrrev_b32 v237, 6, v1\n"
    "v_and_b32 v237, 12, v237\n"
    "v_sub_u32 v236, v236, v237\n"
    "s_lshl_b32 s96, s62, 4\n"
    "v_add_u32 v236, s96, v236\n"
    "v_accvgpr_write_b32 a12, v228\n"
    "v_accvgpr_write_b32 a13, v229\n"
    "v_accvgpr_write_b32 a8, v230\n"
    "v_accvgpr_write_b32 a9, v231\n"
    "v_accvgpr_write_b32 a10, v236\n"
    "v_accvgpr_write_b32 a11, v12\n"
    "v_accvgpr_write_b32 a15, v0\n"
    "s_mov_b32 s89, 1157627904\n"
    "s_lshl_b32 s87, s54, 15\n"
    "s_add_u32 s82, s53, s87\n"
    "s_add_u32 s82, s82, s57\n"
    "s_add_u32 s81, s53, 0x1000000\n"
    "s_mov_b32 s83, s54\n"
    "s_and_b32 s42, s55, 1\n"
    "s_mov_b32 s45, s42\n"
    "s_mul_i32 s42, s42, 4\n"
    "s_cmp_lg_u32 s57, 0\n"
    "s_cselect_b32 s43, 24, 0\n"
    "s_or_b32 s43, s43, s42\n"
    "s_cmp_lg_u32 s41, 0\n"
    "s_cselect_b32 s40, s43, -1\n"
    "s_lshl_b32 s44, s72, 1\n"
    "s_mov_b32 s84, s68\n"
    "v_mov_b64 v[56:57], 0\n"
    "v_mov_b64 v[58:59], 0\n"
    "v_mov_b64 v[128:129], 0\n"
    "v_mov_b64 v[130:131], 0\n"
    "v_mov_b64 v[132:133], 0\n"
    "v_mov_b64 v[134:135], 0\n"
    "v_mov_b64 v[136:137], 0\n"
    "v_mov_b64 v[138:139], 0\n"
    "v_mov_b64 v[140:141], 0\n"
    "v_mov_b64 v[142:143], 0\n"
    "v_mov_b64 v[144:145], 0\n"
    "v_mov_b64 v[146:147], 0\n"
    "v_mov_b64 v[148:149], 0\n"
    "v_mov_b64 v[150:151], 0\n"
    "v_mov_b64 v[152:153], 0\n"
    "v_mov_b64 v[154:155], 0\n"
    "v_mov_b64 v[156:157], 0\n"
    "v_mov_b64 v[158:159], 0\n"
    "v_mov_b64 v[160:161], 0\n"
    "v_mov_b64 v[162:163], 0\n"
    "v_mov_b64 v[164:165], 0\n"
    "v_mov_b64 v[166:167], 0\n"
    "v_mov_b64 v[168:169], 0\n"
    "v_mov_b64 v[170:171], 0\n"
    "v_mov_b64 v[172:173], 0\n"
    "v_mov_b64 v[174:175], 0\n"
    "v_mov_b64 v[176:177], 0\n"
    "v_mov_b64 v[178:179], 0\n"
    "v_mov_b64 v[180:181], 0\n"
    "v_mov_b64 v[182:183], 0\n"
    "v_mov_b64 v[184:185], 0\n"
    "v_mov_b64 v[186:187], 0\n"
    "v_mov_b64 v[188:189], 0\n"
    "v_mov_b64 v[190:191], 0\n"
    "v_mov_b64 v[192:193], 0\n"
    "v_mov_b64 v[194:195], 0\n"
    "v_mov_b64 v[196:197], 0\n"
    "v_mov_b64 v[198:199], 0\n"
    "v_mov_b64 v[200:201], 0\n"
    "v_mov_b64 v[202:203], 0\n"
    "v_mov_b64 v[204:205], 0\n"
    "v_mov_b64 v[206:207], 0\n"
    "v_mov_b64 v[208:209], 0\n"
    "v_mov_b64 v[210:211], 0\n"
    "v_mov_b64 v[212:213], 0\n"
    "v_mov_b64 v[214:215], 0\n"
    "v_mov_b64 v[216:217], 0\n"
    "v_mov_b64 v[218:219], 0\n"
    "v_mov_b64 v[220:221], 0\n"
    "v_mov_b64 v[222:223], 0\n"
    "v_mov_b64 v[224:225], 0\n"
    "v_mov_b64 v[226:227], 0\n"
    "v_mov_b64 v[228:229], 0\n"
    "v_mov_b64 v[230:231], 0\n"
    "v_mov_b64 v[232:233], 0\n"
    "v_mov_b64 v[234:235], 0\n"
    "v_mov_b64 v[236:237], 0\n"
    "v_mov_b64 v[238:239], 0\n"
    "v_mov_b64 v[240:241], 0\n"
    "v_mov_b64 v[242:243], 0\n"
    "v_mov_b64 v[244:245], 0\n"
    "v_mov_b64 v[246:247], 0\n"
    "v_mov_b64 v[248:249], 0\n"
    "v_mov_b64 v[250:251], 0\n"
    "v_mov_b64 v[252:253], 0\n"
    "v_mov_b64 v[254:255], 0\n"
    "s_waitcnt vmcnt(9)\n"
    "s_barrier\n"
    "v_mov_b64 v[12:13], 0\n"
    "v_mov_b64 v[14:15], 0\n"
    "s_bitcmp1_b32 s54, 0\n"
    "s_cbranch_scc1 .Lk2_seg1o\n"
    "s_branch .Lk2_seg1\n"
    ".Lf_help:\n"
    "v_cvt_pk_bf16_f32 v164, v100, v101\n"
    "v_cvt_pk_bf16_f32 v165, v102, v103\n"
    "v_cvt_pk_bf16_f32 v166, v108, v109\n"
    "v_cvt_pk_bf16_f32 v167, v110, v111\n"
    "v_cvt_pk_bf16_f32 v172, v116, v117\n"
    "v_cvt_pk_bf16_f32 v173, v118, v119\n"
    "v_cvt_pk_bf16_f32 v174, v124, v125\n"
    "v_cvt_pk_bf16_f32 v175, v126, v127\n"
    "v_cndmask_b32 v164, v164, 0, s[46:47]\n"
    "v_cndmask_b32 v165, v165, 0, s[46:47]\n"
    "v_cndmask_b32 v166, v166, 0, s[90:91]\n"
    "v_cndmask_b32 v167, v167, 0, s[90:91]\n"
    "v_cndmask_b32 v172, v172, 0, s[46:47]\n"
    "v_cndmask_b32 v173, v173, 0, s[46:47]\n"
    "v_cndmask_b32 v174, v174, 0, s[90:91]\n"
    "v_cndmask_b32 v175, v175, 0, s[90:91]\n"
    "v_accvgpr_write_b32 a16, v164\n"
    "v_accvgpr_write_b32 a17, v165\n"
    "v_accvgpr_write_b32 a18, v166\n"
    "v_accvgpr_write_b32 a19, v167\n"
    "v_accvgpr_write_b32 a20, v172\n"
    "v_accvgpr_write_b32 a21, v173\n"
    "v_accvgpr_write_b32 a22, v174\n"
    "v_accvgpr_write_b32 a23, v175\n"
    "s_bitcmp1_b32 s55, 1\n"
    "s_cbranch_scc1 .Lf_hq\n"
    "s_waitcnt vmcnt(9)\n"
    "s_barrier\n"
    "s_branch .Lf_hr\n"
    ".Lf_hq:\n"
    "s_cmp_lg_u32 s62, 0\n"
    "s_cbranch_scc1 .Lh_got\n"
    "s_mov_b32 s45, 1\n"
    "s_mov_b32 s85, 0\n"
    ".Lh_spin:\n"
    "buffer_load_dwordx2 v[14:15], off, s[48:51], s75 sc1\n"
    "s_waitcnt vmcnt(0)\n"
    "v_cmp_eq_u64 vcc, s[16:17], v[14:15]\n"
    "s_nop 1\n"
    "s_and_b32 s87, vcc_lo, s45\n"
    "s_cmp_eq_u32 s87, s45\n"
    "s_cbranch_scc1 .Lh_got\n"
    "s_add_u32 s85, s85, 1\n"
    "s_cmp_gt_u32 s85, 0x40000\n"
    "s_cbranch_scc1 .Lh_got\n"
    "s_sleep 8\n"
    "s_branch .Lh_spin\n"
    ".Lh_got:\n"
    "s_waitcnt vmcnt(9)\n"
    "s_barrier\n"
    "s_add_u32 s87, s52, 0\n"
    "buffer_load_dwordx4 a[192:195], v230, s[48:51], s87 offen offset:0 sc1\n"
    "buffer_load_dwordx4 a[196:199], v230, s[48:51], s87 offen offset:1024 sc1\n"
    "buffer_load_dwordx4 a[200:203], v230, s[48:51], s87 offen offset:2048 sc1\n"
    "buffer_load_dwordx4 a[204:207], v230, s[48:51], s87 offen offset:3072 sc1\n"
    "s_add_u32 s87, s52, 4096\n"
    "buffer_load_dwordx4 a[208:211], v230, s[48:51], s87 offen offset:0 sc1\n"
    "buffer_load_dwordx4 a[212:215], v230, s[48:51], s87 offen offset:1024 sc1\n"
    "buffer_load_dwordx4 a[216:219], v230, s[48:51], s87 offen offset:2048 sc1\n"
    "buffer_load_dwordx4 a[220:223], v230, s[48:51], s87 offen offset:3072 sc1\n"
    "s_add_u32 s87, s52, 8192\n"
    "buffer_load_dwordx4 a[224:227], v230, s[48:51], s87 offen offset:0 sc1\n"
    "buffer_load_dwordx4 a[228:231], v230, s[48:51], s87 offen offset:1024 sc1\n"
    "buffer_load_dwordx4 a[232:235], v230, s[48:51], s87 offen offset:2048 sc1\n"
    "buffer_load_dwordx4 a[236:239], v230, s[48:51], s87 offen offset:3072 sc1\n"
    "s_add_u32 s87, s52, 12288\n"
    "buffer_load_dwordx4 a[240:243], v230, s[48:51], s87 offen offset:0 sc1\n"
    "buffer_load_dwordx4 a[244:247], v230, s[48:51], s87 offen offset:1024 sc1\n"
    "buffer_load_dwordx4 a[248:251], v230, s[48:51], s87 offen offset:2048 sc1\n"
    "buffer_load_dwordx4 a[252:255], v230, s[48:51], s87 offen offset:3072 sc1\n"
    ".Lf_hr:\n"
    "v_add_u32 v144, 9216, v1\n"
    "s_waitcnt lgkmcnt(0)\n"
    "buffer_store_dwordx4 v[152:155], v1, s[48:51], s81 offen sc1\n"
    "buffer_store_dwordx4 v[160:163], v144, s[48:51], s81 offen sc1\n"
    "buffer_store_dwordx4 v[168:171], v1, s[48:51], s83 offen sc1\n"
    "buffer_store_dwordx4 v[176:179], v144, s[48:51], s83 offen sc1\n"
    "buffer_store_dwordx4 v[184:187], v1, s[48:51], s86 offen sc1\n"
    "buffer_store_dwordx4 v[192:195], v144, s[48:51], s86 offen sc1\n"
    "buffer_store_dwordx4 v[200:203], v1, s[48:51], s89 offen sc1\n"
    "buffer_store_dwordx4 v[208:211], v144, s[48:51], s89 offen sc1\n"
    "v_accvgpr_read_b32 v228, a8\n"
    "v_accvgpr_read_b32 v229, a12\n"
    "v_mul_f32 v228, 0xbfb8aa3b, v228\n"
    "v_mul_f32 v229, 0xbfb8aa3b, v229\n"
    "v_exp_f32 v228, v228\n"
    "v_exp_f32 v229, v229\n"
    "v_add_f32 v228, 1.0, v228\n"
    "v_add_f32 v229, 1.0, v229\n"
    "v_rcp_f32 v228, v228\n"
    "v_rcp_f32 v229, v229\n"
    "s_mul_i32 s96, s62, 28672\n"
    "v_add_u32 v231, s96, v1\n"
    "v_bfe_u32 v236, v1, 4, 4\n"
    "v_lshrrev_b32 v237, 6, v1\n"
    "v_and_b32 v237, 12, v237\n"
    "v_sub_u32 v236, v236, v237\n"
    "s_lshl_b32 s96, s62, 4\n"
    "v_add_u32 v236, s96, v236\n"
    "v_accvgpr_write_b32 a12, v228\n"
    "v_accvgpr_write_b32 a13, v229\n"
    "v_accvgpr_write_b32 a8, v230\n"
    "v_accvgpr_write_b32 a9, v231\n"
    "v_accvgpr_write_b32 a10, v236\n"
    "v_accvgpr_write_b32 a11, v12\n"
    "v_accvgpr_write_b32 a15, v0\n"
    "s_mov_b32 s89, 1157627904\n"
    "s_lshl_b32 s87, s54, 15\n"
    "s_add_u32 s82, s53, s87\n"
    "s_add_u32 s82, s82, s57\n"
    "s_add_u32 s81, s53, 0x1000000\n"
    "s_mov_b32 s83, s54\n"
    "s_and_b32 s42, s55, 1\n"
    "s_mov_b32 s45, s42\n"
    "s_mul_i32 s42, s42, 4\n"
    "s_cmp_lg_u32 s57, 0\n"
    "s_cselect_b32 s43, 24, 0\n"
    "s_or_b32 s43, s43, s42\n"
    "s_cmp_lg_u32 s41, 0\n"
    "s_cselect_b32 s40, s43, -1\n"
    "s_lshl_b32 s44, s72, 1\n"
    "s_mov_b32 s84, s68\n"
    "v_mov_b64 v[56:57], 0\n"
    "v_mov_b64 v[58:59], 0\n"
    "v_mov_b64 v[12:13], 0\n"
    "v_mov_b64 v[14:15], 0\n"
    "v_mov_b64 v[128:129], 0\n"
    "v_mov_b64 v[130:131], 0\n"
    "v_mov_b64 v[132:133], 0\n"
    "v_mov_b64 v[134:135], 0\n"
    "v_mov_b64 v[136:137], 0\n"
    "v_mov_b64 v[138:139], 0\n"
    "v_mov_b64 v[140:141], 0\n"
    "v_mov_b64 v[142:143], 0\n"
    "v_mov_b64 v[144:145], 0\n"
    "v_mov_b64 v[146:147], 0\n"
    "v_mov_b64 v[148:149], 0\n"
    "v_mov_b64 v[150:151], 0\n"
    "v_mov_b64 v[152:153], 0\n"
    "v_mov_b64 v[154:155], 0\n"
    "v_mov_b64 v[156:157], 0\n"
    "v_mov_b64 v[158:159], 0\n"
    "v_mov_b64 v[160:161], 0\n"
    "v_mov_b64 v[162:163], 0\n"
    "v_mov_b64 v[164:165], 0\n"
    "v_mov_b64 v[166:167], 0\n"
    "v_mov_b64 v[168:169], 0\n"
    "v_mov_b64 v[170:171], 0\n"
    "v_mov_b64 v[172:173], 0\n"
    "v_mov_b64 v[174:175], 0\n"
    "v_mov_b64 v[176:177], 0\n"
    "v_mov_b64 v[178:179], 0\n"
    "v_mov_b64 v[180:181], 0\n"
    "v_mov_b64 v[182:183], 0\n"
    "v_mov_b64 v[184:185], 0\n"
    "v_mov_b64 v[186:187], 0\n"
    "v_mov_b64 v[188:189], 0\n"
    "v_mov_b64 v[190:191], 0\n"
    "v_mov_b64 v[192:193], 0\n"
    "v_mov_b64 v[194:195], 0\n"
    "v_mov_b64 v[196:197], 0\n"
    "v_mov_b64 v[198:199], 0\n"
    "v_mov_b64 v[200:201], 0\n"
    "v_mov_b64 v[202:203], 0\n"
    "v_mov_b64 v[204:205], 0\n"
    "v_mov_b64 v[206:207], 0\n"
    "v_mov_b64 v[208:209], 0\n"
    "v_mov_b64 v[210:211], 0\n"
    "v_mov_b64 v[212:213], 0\n"
    "v_mov_b64 v[214:215], 0\n"
    "v_mov_b64 v[216:217], 0\n"
    "v_mov_b64 v[218:219], 0\n"
    "v_mov_b64 v[220:221], 0\n"
    "v_mov_b64 v[222:223], 0\n"
    "v_mov_b64 v[224:225], 0\n"
    "v_mov_b64 v[226:227], 0\n"
    "v_mov_b64 v[228:229], 0\n"
    "v_mov_b64 v[230:231], 0\n"
    "v_mov_b64 v[232:233], 0\n"
    "v_mov_b64 v[234:235], 0\n"
    "v_mov_b64 v[236:237], 0\n"
    "v_mov_b64 v[238:239], 0\n"
    "v_mov_b64 v[240:241], 0\n"
    "v_mov_b64 v[242:243], 0\n"
    "v_mov_b64 v[244:245], 0\n"
    "v_mov_b64 v[246:247], 0\n"
    "v_mov_b64 v[248:249], 0\n"
    "v_mov_b64 v[250:251], 0\n"
    "v_mov_b64 v[252:253], 0\n"
    "v_mov_b64 v[254:255], 0\n"
    "s_waitcnt vmcnt(8)\n"
    "s_bitcmp1_b32 s54, 0\n"
    "s_cbranch_scc1 .Lk2_seg1o\n"
    "s_branch .Lk2_seg1\n"
    ".Lk2_seg1o:\n"
    "ds_read_b128 a[64:67], v2 offset:32768\n"
    "ds_read_b128 a[68:71], v2 offset:33792\n"
    "ds_read_b128 a[72:75], v2 offset:34816\n"
    "ds_read_b128 a[76:79], v2 offset:35840\n"
    "ds_read_b128 a[80:83], v2 offset:36864\n"
    "ds_read_b128 a[84:87], v2 offset:37888\n"
    "ds_read_b128 a[88:91], v2 offset:38912\n"
    "ds_read_b128 a[92:95], v2 offset:39936\n"
    "ds_read_b128 a[96:99], v2 offset:40960\n"
    "ds_read_b128 a[100:103], v2 offset:41984\n"
    "ds_read_b128 a[104:107], v2 offset:43008\n"
    "ds_read_b128 a[108:111], v2 offset:44032\n"
    "s_waitcnt lgkmcnt(8)\n"
    "v_mfma_f32_16x16x32_bf16 v[96:99], a[64:67], a[192:195], 0\n"
    "ds_read_b128 a[112:115], v2 offset:45056\n"
    "s_add_u32 m0, s61, 98304\n"
    "s_add_u32 s86, s81, 4096\n"
    "v_mfma_f32_16x16x32_bf16 v[96:99], a[68:71], a[196:199], v[96:99]\n"
    "ds_read_b128 a[116:119], v2 offset:46080\n"
    "buffer_load_dwordx4 v4, s[48:51], s81 offen offset:0 sc1 lds\n"
    "v_mov_b32 v48, s16\n"
    "v_mfma_f32_16x16x32_bf16 v[96:99], a[72:75], a[200:203], v[96:99]\n"
    "ds_read_b128 a[120:123], v2 offset:47104\n"
    "buffer_load_dwordx4 v4, s[48:51], s81 offen offset:1024 sc1 lds\n"
    "v_mfma_f32_16x16x32_bf16 v[96:99], a[76:79], a[204:207], v[96:99]\n"
    "ds_read_b128 a[124:127], v2 offset:48128\n"
    "buffer_load_dwordx4 v4, s[48:51], s81 offen offset:2048 sc1 lds\n"
    "s_waitcnt lgkmcnt(8)\n"
    "v_mfma_f32_16x16x32_bf16 v[96:99], a[80:83], a[208:211], v[96:99]\n"
    "ds_read_b128 a[128:131], v2 offset:49152\n"
    "v_mfma_f32_16x16x32_bf16 v[96:99], a[84:87], a[212:215], v[96:99]\n"
    "ds_read_b128 a[132:135], v2 offset:50176\n"
    "v_mfma_f32_16x16x32_bf16 v[96:99], a[88:91], a[216:219], v[96:99]\n"
    "ds_read_b128 a[136:139], v2 offset:51200\n"
    "buffer_load_dwordx4 v4, s[48:51], s81 offen offset:3072 sc1 lds\n"
    "v_mov_b32 v49, s17\n"
    "v_mfma_f32_16x16x32_bf16 v[96:99], a[92:95], a[220:223], v[96:99]\n"
    "ds_read_b128 a[140:143], v2 offset:52224\n"
    "v_mfma_f32_16x16x32_bf16 v[100:103], a[64:67], a[224:227], 0\n"
    "ds_read_b128 a[144:147], v2 offset:53248\n"
    "s_add_u32 m0, s61, 102400\n"
    "s_add_u32 s81, s81, 32768\n"
    "v_mfma_f32_16x16x32_bf16 v[100:103], a[68:71], a[228:231], v[100:103]\n"
    "ds_read_b128 a[148:151], v2 offset:54272\n"
    "v_mfma_f32_16x16x32_bf16 v[100:103], a[72:75], a[232:235], v[100:103]\n"
    "ds_read_b128 a[152:155], v2 offset:55296\n"
    "buffer_load_dwordx4 v4, s[48:51], s86 offen offset:0 sc1 lds\n"
    "v_mfma_f32_16x16x32_bf16 v[100:103], a[76:79], a[236:239], v[100:103]\n"
    "ds_read_b128 a[156:159], v2 offset:56320\n"
    "v_accvgpr_read_b32 v50, a11\n"
    "v_mfma_f32_16x16x32_bf16 v[100:103], a[80:83], a[240:243], v[100:103]\n"
    "ds_read_b128 a[160:163], v2 offset:57344\n"
    "v_mfma_f32_16x16x32_bf16 v[100:103], a[84:87], a[244:247], v[100:103]\n"
    "ds_read_b128 a[164:167], v2 offset:58368\n"
    "v_max3_f32 v60, v96, v97, v98\n"
    "v_max_f32 v60, v60, v99\n"
    "v_mfma_f32_16x16x32_bf16 v[100:103], a[88:91], a[248:251], v[100:103]\n"
    "ds_read_b128 a[168:171], v2 offset:59392\n"
    "buffer_load_dwordx4 v4, s[48:51], s86 offen offset:1024 sc1 lds\n"
    "v_mfma_f32_16x16x32_bf16 v[100:103], a[92:95], a[252:255], v[100:103]\n"
    "ds_read_b128 a[172:175], v2 offset:60416\n"
    "buffer_load_dwordx4 v4, s[48:51], s86 offen offset:2048 sc1 lds\n"
    "s_waitcnt lgkmcnt(15)\n"
    "v_mfma_f32_16x16x32_bf16 v[104:107], a[96:99], a[192:195], 0\n"
    "ds_read_b128 a[176:179], v2 offset:61440\n"
    "v_mfma_f32_16x16x32_bf16 v[104:107], a[100:103], a[196:199], v[104:107]\n"
    "ds_read_b128 a[180:183], v2 offset:62464\n"
    "buffer_load_dwordx4 v4, s[48:51], s86 offen offset:3072 sc1 lds\n"
    "v_mfma_f32_16x16x32_bf16 v[104:107], a[104:107], a[200:203], v[104:107]\n"
    "ds_read_b128 a[184:187], v2 offset:63488\n"
    "v_mfma_f32_16x16x32_bf16 v[104:107], a[108:111], a[204:207], v[104:107]\n"
    "ds_read_b128 a[188:191], v2 offset:64512\n"
    "v_mfma_f32_16x16x32_bf16 v[104:107], a[112:115], a[208:211], v[104:107]\n"
    "s_waitcnt lgkmcnt(15)\n"
    "v_mfma_f32_16x16x32_bf16 v[104:107], a[116:119], a[212:215], v[104:107]\n"
    "v_max3_f32 v61, v100, v101, v102\n"
    "v_max_f32 v61, v61, v103\n"
    "v_mov_b32 v16, v60\n"
    "v_mov_b32 v17, v61\n"
    "v_mfma_f32_16x16x32_bf16 v[104:107], a[120:123], a[216:219], v[104:107]\n"
    "s_nop 0\n"
    "v_permlane32_swap_b32 v16, v60\n"
    "v_permlane32_swap_b32 v17, v61\n"
    "v_max_f32 v60, v60, v16\n"
    "v_mfma_f32_16x16x32_bf16 v[104:107], a[124:127], a[220:223], v[104:107]\n"
    "v_max_f32 v61, v61, v17\n"
    "v_mov_b32 v16, v60\n"
    "v_mov_b32 v17, v61\n"
    "v_mfma_f32_16x16x32_bf16 v[108:111], a[96:99], a[224:227], 0\n"
    "s_waitcnt vmcnt(8) lgkmcnt(0)\n"
    "s_barrier\n"
    "s_nop 0\n"
    "v_permlane16_swap_b32 v16, v60\n"
    "v_mfma_f32_16x16x32_bf16 v[108:111], a[100:103], a[228:231], v[108:111]\n"
    "buffer_store_dwordx2 v[48:49], v50, s[48:51], s84 offen sc1\n"
    "s_bitcmp1_b32 s40, s83\n"
    "s_cbranch_scc1 .Lk2_hook0o\n"
    ".Lk2_hook0o_ret:\n"
    "v_mfma_f32_16x16x32_bf16 v[108:111], a[104:107], a[232:235], v[108:111]\n"
    "s_cmp_eq_u32 s80, s82\n"
    "s_cselect_b32 s80, s58, s80\n"
    "s_mov_b32 s84, 1073741824\n"
    "v_permlane16_swap_b32 v17, v61\n"
    "v_mfma_f32_16x16x32_bf16 v[108:111], a[108:111], a[236:239], v[108:111]\n"
    "s_add_u32 m0, s61, 32768\n"
    "s_add_u32 s85, s80, 4096\n"
    "v_max_f32 v60, v60, v16\n"
    "v_max_f32 v61, v61, v17\n"
    "v_mfma_f32_16x16x32_bf16 v[108:111], a[112:115], a[240:243], v[108:111]\n"
    "buffer_load_dwordx4 v4, s[48:51], s80 offen offset:0 sc1 lds\n"
    "v_sub_f32 v24, 0, v60\n"
    "v_sub_f32 v28, 0, v61\n"
    "v_mfma_f32_16x16x32_bf16 v[108:111], a[116:119], a[244:247], v[108:111]\n"
    "v_sub_f32 v25, 0, v60\n"
    "v_sub_f32 v29, 0, v61\n"
    "v_sub_f32 v26, 0, v60\n"
    "v_mfma_f32_16x16x32_bf16 v[108:111], a[120:123], a[248:251], v[108:111]\n"
    "buffer_load_dwordx4 v4, s[48:51], s80 offen offset:1024 sc1 lds\n"
    "v_sub_f32 v30, 0, v61\n"
    "v_sub_f32 v27, 0, v60\n"
    "v_mfma_f32_16x16x32_bf16 v[108:111], a[124:127], a[252:255], v[108:111]\n"
    "v_sub_f32 v31, 0, v61\n"
    "v_mov_b32 v6, v60\n"
    "v_mfma_f32_16x16x32_bf16 v[112:115], a[128:131], a[192:195], 0\n"
    "buffer_load_dwordx4 v4, s[48:51], s80 offen offset:2048 sc1 lds\n"
    "v_mov_b32 v7, v61\n"
    "v_mov_b64 v[8:9], 0\n"
    "v_mfma_f32_16x16x32_bf16 v[112:115], a[132:135], a[196:199], v[112:115]\n"
    "v_pk_add_f32 v[96:97], v[96:97], v[24:25]\n"
    "v_mfma_f32_16x16x32_bf16 v[112:115], a[136:139], a[200:203], v[112:115]\n"
    "buffer_load_dwordx4 v4, s[48:51], s80 offen offset:3072 sc1 lds\n"
    "v_pk_add_f32 v[98:99], v[98:99], v[24:25]\n"
    "v_pk_add_f32 v[100:101], v[100:101], v[28:29]\n"
    "v_mfma_f32_16x16x32_bf16 v[112:115], a[140:143], a[204:207], v[112:115]\n"
    "v_pk_add_f32 v[102:103], v[102:103], v[28:29]\n"
    "v_pk_add_f32 v[104:105], v[104:105], v[24:25]\n"
    "v_mfma_f32_16x16x32_bf16 v[112:115], a[144:147], a[208:211], v[112:115]\n"
    "s_add_u32 m0, s61, 36864\n"
    "s_add_u32 s80, s80, 32768\n"
    "v_pk_add_f32 v[106:107], v[106:107], v[24:25]\n"
    "v_mfma_f32_16x16x32_bf16 v[112:115], a[148:151], a[212:215], v[112:115]\n"
    "v_pk_add_f32 v[108:109], v[108:109], v[28:29]\n"
    "v_pk_add_f32 v[110:111], v[110:111], v[28:29]\n"
    "v_exp_f32 v16, v96\n"
    "v_mfma_f32_16x16x32_bf16 v[112:115], a[152:155], a[216:219], v[112:115]\n"
    "v_exp_f32 v17, v97\n"
    "v_exp_f32 v18, v98\n"
    "v_mfma_f32_16x16x32_bf16 v[112:115], a[156:159], a[220:223], v[112:115]\n"
    "buffer_load_dwordx4 v4, s[48:51], s85 offen offset:0 sc1 lds\n"
    "v_exp_f32 v19, v99\n"
    "v_mfma_f32_16x16x32_bf16 v[116:119], a[128:131], a[224:227], 0\n"
    "ds_read_b128 a[64:67], v2 offset:0\n"
    "buffer_load_dwordx4 v4, s[48:51], s85 offen offset:1024 sc1 lds\n"
    "v_add_f32 v10, v16, v17\n"
    "v_mfma_f32_16x16x32_bf16 v[116:119], a[132:135], a[228:231], v[116:119]\n"
    "ds_read_b128 a[68:71], v2 offset:1024\n"
    "v_cvt_pk_bf16_f32 v40, v16, v17\n"
    "v_exp_f32 v20, v104\n"
    "v_mfma_f32_16x16x32_bf16 v[116:119], a[136:139], a[232:235], v[116:119]\n"
    "ds_read_b128 a[72:75], v2 offset:2048\n"
    "v_exp_f32 v21, v105\n"
    "v_mfma_f32_16x16x32_bf16 v[116:119], a[140:143], a[236:239], v[116:119]\n"
    "ds_read_b128 a[76:79], v2 offset:3072\n"
    "buffer_load_dwordx4 v4, s[48:51], s85 offen offset:2048 sc1 lds\n"
    "v_add_f32 v10, v10, v18\n"
    "v_mfma_f32_16x16x32_bf16 v[116:119], a[144:147], a[240:243], v[116:119]\n"
    "ds_read_b128 a[80:83], v2 offset:4096\n"
    "v_add_f32 v10, v10, v19\n"
    "v_cvt_pk_bf16_f32 v41, v18, v19\n"
    "v_mfma_f32_16x16x32_bf16 v[116:119], a[148:151], a[244:247], v[116:119]\n"
    "v_pk_add_f32 v[112:113], v[112:113], v[24:25]\n"
    "v_pk_add_f32 v[114:115], v[114:115], v[24:25]\n"
    "v_exp_f32 v22, v106\n"
    "v_mfma_f32_16x16x32_bf16 v[116:119], a[152:155], a[248:251], v[116:119]\n"
    "buffer_load_dwordx4 v4, s[48:51], s85 offen offset:3072 sc1 lds\n"
    "v_exp_f32 v23, v107\n"
    "v_mfma_f32_16x16x32_bf16 v[116:119], a[156:159], a[252:255], v[116:119]\n"
    "ds_read_b128 a[84:87], v2 offset:5120\n"
    "v_add_f32 v10, v10, v20\n"
    "v_add_f32 v10, v10, v21\n"
    "v_add_f32 v56, v56, v12\n"
    "v_mfma_f32_16x16x32_bf16 v[120:123], a[160:163], a[192:195], 0\n"
    "ds_read_b128 a[88:91], v2 offset:6144\n"
    "v_cvt_pk_bf16_f32 v42, v20, v21\n"
    "v_mfma_f32_16x16x32_bf16 v[120:123], a[164:167], a[196:199], v[120:123]\n"
    "v_exp_f32 v16, v100\n"
    "v_exp_f32 v17, v101\n"
    "v_mfma_f32_16x16x32_bf16 v[120:123], a[168:171], a[200:203], v[120:123]\n"
    "ds_read_b128 a[92:95], v2 offset:7168\n"
    "v_add_f32 v57, v57, v13\n"
    "v_add_f32 v10, v10, v22\n"
    "v_mfma_f32_16x16x32_bf16 v[120:123], a[172:175], a[204:207], v[120:123]\n"
    "v_add_f32 v10, v10, v23\n"
    "v_add_f32 v58, v58, v14\n"
    "v_mfma_f32_16x16x32_bf16 v[120:123], a[176:179], a[208:211], v[120:123]\n"
    "ds_read_b128 a[96:99], v2 offset:8192\n"
    "v_cvt_pk_bf16_f32 v43, v22, v23\n"
    "v_exp_f32 v18, v102\n"
    "v_mfma_f32_16x16x32_bf16 v[120:123], a[180:183], a[212:215], v[120:123]\n"
    "v_pk_add_f32 v[116:117], v[116:117], v[28:29]\n"
    "v_pk_add_f32 v[118:119], v[118:119], v[28:29]\n"
    "v_exp_f32 v19, v103\n"
    "v_mfma_f32_16x16x32_bf16 v[120:123], a[184:187], a[216:219], v[120:123]\n"
    "ds_read_b128 a[100:103], v2 offset:9216\n"
    "v_add_f32 v59, v59, v15\n"
    "v_add_f32 v11, v16, v17\n"
    "v_mfma_f32_16x16x32_bf16 v[120:123], a[188:191], a[220:223], v[120:123]\n"
    "buffer_load_dwordx4 v[12:15], v5, s[36:39], s88 offen sc1\n"
    "v_cvt_pk_bf16_f32 v44, v16, v17\n"
    "s_nop 0\n"
    "v_mfma_f32_16x16x32_bf16 v[124:127], a[160:163], a[224:227], 0\n"
    "ds_read_b128 a[104:107], v2 offset:10240\n"
    "v_add_f32 v11, v11, v18\n"
    "v_mfma_f32_16x16x32_bf16 v[124:127], a[164:167], a[228:231], v[124:127]\n"
    "v_add_f32 v11, v11, v19\n"
    "s_add_u32 s88, s88, s41\n"
    "v_mfma_f32_16x16x32_bf16 v[124:127], a[168:171], a[232:235], v[124:127]\n"
    "ds_read_b128 a[108:111], v2 offset:11264\n"
    "v_cvt_pk_bf16_f32 v45, v18, v19\n"
    "v_mfma_f32_16x16x32_bf16 v[124:127], a[172:175], a[236:239], v[124:127]\n"
    "v_mfma_f32_16x16x32_bf16 v[124:127], a[176:179], a[240:243], v[124:127]\n"
    "v_mfma_f32_16x16x32_bf16 v[124:127], a[180:183], a[244:247], v[124:127]\n"
    "v_pk_add_f32 v[120:121], v[120:121], v[24:25]\n"
    "v_pk_add_f32 v[122:123], v[122:123], v[24:25]\n"
    "v_mfma_f32_16x16x32_bf16 v[124:127], a[184:187], a[248:251], v[124:127]\n"
    "v_mfma_f32_16x16x32_bf16 v[124:127], a[188:191], a[252:255], v[124:127]\n"
    "s_nop 1\n"
    "s_sub_u32 s83, s83, 1\n"
    "s_nop 4\n"
    "v_pk_add_f32 v[124:125], v[124:125], v[28:29]\n"
    "v_pk_add_f32 v[126:127], v[126:127], v[28:29]\n"
    "s_waitcnt lgkmcnt(0)\n"
    "s_branch .Lk2_loop2\n"
    ".Lk2_mask0:\n"
    "s_cmp_eq_u32 s83, 4\n"
    "s_cselect_b32 s87, s57, 0\n"
    "s_add_u32 s80, s80, s87\n"
    "s_cmp_lg_u32 s41, 0\n"
    "s_cbranch_scc1 .Lk2_mask0_m\n"
    "s_add_u32 s44, s44, 1\n"
    "s_mov_b32 s46, 0\n"
    ".Lk2_mask0_p:\n"
    "v_cmp_eq_u64 vcc, s[16:17], v[12:13]\n"
    "s_nop 1\n"
    "s_and_b32 s87, vcc_lo, 0xffff\n"
    "s_cmp_eq_u32 s87, 0xffff\n"
    "s_cbranch_scc1 .Lk2_mask0_s\n"
    "s_cmp_ge_u32 s44, 7\n"
    "s_cbranch_scc0 .Lk2_mask0_m\n"
    "s_add_u32 s46, s46, 1\n"
    "s_cmp_gt_u32 s46, 0x40000\n"
    "s_cbranch_scc1 .Lk2_mask0_s\n"
    "s_sleep 2\n"
    "buffer_load_dwordx4 v[12:15], v5, s[36:39], s88 offen sc1\n"
    "s_waitcnt vmcnt(0)\n"
    "s_branch .Lk2_mask0_p\n"
    ".Lk2_mask0_s:\n"
    "s_add_u32 s36, s48, s70\n"
    "s_addc_u32 s37, s49, 0\n"
    "s_mov_b32 s38, 65536\n"
    "s_mov_b32 s41, 4096\n"
    "s_mov_b32 s40, s43\n"
    "v_mov_b64 v[12:13], 0\n"
    "v_mov_b64 v[14:15], 0\n"
    "v_mov_b64 v[56:57], 0\n"
    "v_mov_b64 v[58:59], 0\n"
    ".Lk2_mask0_m:\n"
    "s_bitcmp1_b32 s42, s83\n"
    "s_cbranch_scc0 .Lk2_mask0_ret\n"
    "s_getpc_b64 s[90:91]\n"
    ".Lk2_mask0_pc:\n"
    "s_add_u32 s90, s90, .Lk2_late-.Lk2_mask0_pc\n"
    "s_addc_u32 s91, s91, 0\n"
    "v_mul_u32_u24 v60, 5, v5\n"
    "global_load_dword a14, v60, s[90:91]\n"
    "s_nop 5\n"
    "v_accvgpr_read_b32 v60, a10\n"
    "v_mov_b32 v61, 0xff800000\n"
    "v_cmp_gt_i32 s[90:91], 0, v60\n"
    "v_cmp_gt_i32 s[92:93], 1, v60\n"
    "v_cmp_gt_i32 s[94:95], 2, v60\n"
    "v_cmp_gt_i32 s[96:97], 3, v60\n"
    "v_cndmask_b32 v64, v64, v61, s[90:91]\n"
    "v_cndmask_b32 v65, v65, v61, s[92:93]\n"
    "v_cndmask_b32 v66, v66, v61, s[94:95]\n"
    "v_cndmask_b32 v67, v67, v61, s[96:97]\n"
    "v_cmp_gt_i32 s[90:91], 16, v60\n"
    "v_cmp_gt_i32 s[92:93], 17, v60\n"
    "v_cmp_gt_i32 s[94:95], 18, v60\n"
    "v_cmp_gt_i32 s[96:97], 19, v60\n"
    "v_cndmask_b32 v72, v72, v61, s[90:91]\n"
    "v_cndmask_b32 v73, v73, v61, s[92:93]\n"
    "v_cndmask_b32 v74, v74, v61, s[94:95]\n"
    "v_cndmask_b32 v75, v75, v61, s[96:97]\n"
    "v_cmp_gt_i32 s[90:91], 32, v60\n"
    "v_cmp_gt_i32 s[92:93], 33, v60\n"
    "v_cmp_gt_i32 s[94:95], 34, v60\n"
    "v_cmp_gt_i32 s[96:97], 35, v60\n"
    "v_cndmask_b32 v80, v80, v61, s[90:91]\n"
    "v_cndmask_b32 v81, v81, v61, s[92:93]\n"
    "v_cndmask_b32 v82, v82, v61, s[94:95]\n"
    "v_cndmask_b32 v83, v83, v61, s[96:97]\n"
    "v_cmp_gt_i32 s[90:91], 48, v60\n"
    "v_cmp_gt_i32 s[92:93], 49, v60\n"
    "v_cmp_gt_i32 s[94:95], 50, v60\n"
    "v_cmp_gt_i32 s[96:97], 51, v60\n"
    "v_cndmask_b32 v88, v88, v61, s[90:91]\n"
    "v_cndmask_b32 v89, v89, v61, s[92:93]\n"
    "v_cndmask_b32 v90, v90, v61, s[94:95]\n"
    "v_cndmask_b32 v91, v91, v61, s[96:97]\n"
    "s_branch .Lk2_mask0_ret\n"
    ".Lk2_mask1:\n"
    "s_cmp_eq_u32 s83, 3\n"
    "s_cselect_b32 s87, s57, 0\n"
    "s_add_u32 s81, s81, s87\n"
    "s_cmp_lg_u32 s41, 0\n"
    "s_cbranch_scc1 .Lk2_mask1_m\n"
    "s_add_u32 s44, s44, 1\n"
    "s_mov_b32 s46, 0\n"
    ".Lk2_mask1_p:\n"
    "v_cmp_eq_u64 vcc, s[16:17], v[12:13]\n"
    "s_nop 1\n"
    "s_and_b32 s87, vcc_lo, 0xffff\n"
    "s_cmp_eq_u32 s87, 0xffff\n"
    "s_cbranch_scc1 .Lk2_mask1_s\n"
    "s_cmp_ge_u32 s44, 7\n"
    "s_cbranch_scc0 .Lk2_mask1_m\n"
    "s_add_u32 s46, s46, 1\n"
    "s_cmp_gt_u32 s46, 0x40000\n"
    "s_cbranch_scc1 .Lk2_mask1_s\n"
    "s_sleep 2\n"
    "buffer_load_dwordx4 v[12:15], v5, s[36:39], s88 offen sc1\n"
    "s_waitcnt vmcnt(0)\n"
    "s_branch .Lk2_mask1_p\n"
    ".Lk2_mask1_s:\n"
    "s_add_u32 s36, s48, s70\n"
    "s_addc_u32 s37, s49, 0\n"
    "s_mov_b32 s38, 65536\n"
    "s_mov_b32 s41, 4096\n"
    "s_mov_b32 s40, s43\n"
    "v_mov_b64 v[12:13], 0\n"
    "v_mov_b64 v[14:15], 0\n"
    "v_mov_b64 v[56:57], 0\n"
    "v_mov_b64 v[58:59], 0\n"
    ".Lk2_mask1_m:\n"
    "s_branch .Lk2_mask1_ret\n"
    ".Lk2_hook0:\n"
    "s_cmp_eq_u32 s83, 4\n"
    "s_cselect_b32 s87, s57, 0\n"
    "s_add_u32 s80, s80, s87\n"
    "s_cmp_lg_u32 s41, 0\n"
    "s_cbranch_scc1 .Lk2_hook0_m\n"
    "s_add_u32 s44, s44, 1\n"
    "s_mov_b32 s46, 0\n"
    ".Lk2_hook0_p:\n"
    "v_cmp_eq_u64 vcc, s[16:17], v[12:13]\n"
    "s_nop 1\n"
    "s_and_b32 s87, vcc_lo, 0xffff\n"
    "s_cmp_eq_u32 s87, 0xffff\n"
    "s_cbranch_scc1 .Lk2_hook0_s\n"
    "s_cmp_ge_u32 s44, 7\n"
    "s_cbranch_scc0 .Lk2_hook0_m\n"
    "s_add_u32 s46, s46, 1\n"
    "s_cmp_gt_u32 s46, 0x40000\n"
    "s_cbranch_scc1 .Lk2_hook0_s\n"
    "s_sleep 2\n"
    "buffer_load_dwordx4 v[12:15], v5, s[36:39], s88 offen sc1\n"
    "s_waitcnt vmcnt(0)\n"
    "s_branch .Lk2_hook0_p\n"
    ".Lk2_hook0_s:\n"
    "s_add_u32 s36, s48, s70\n"
    "s_addc_u32 s37, s49, 0\n"
    "s_mov_b32 s38, 65536\n"
    "s_mov_b32 s41, 4096\n"
    "s_mov_b32 s40, s43\n"
    "v_mov_b64 v[12:13], 0\n"
    "v_mov_b64 v[14:15], 0\n"
    "v_mov_b64 v[56:57], 0\n"
    "v_mov_b64 v[58:59], 0\n"
    ".Lk2_hook0_m:\n"
    "s_branch .Lk2_hook0_ret\n"
    ".Lk2_mask0f:\n"
    "s_getpc_b64 s[90:91]\n"
    ".Lk2_hook0_pc:\n"
    "s_add_u32 s90, s90, .Lk2_late-.Lk2_hook0_pc\n"
    "s_addc_u32 s91, s91, 0\n"
    "v_mul_u32_u24 v60, 5, v5\n"
    "global_load_dword a14, v60, s[90:91]\n"
    "s_nop 5\n"
    "v_accvgpr_read_b32 v60, a10\n"
    "v_mov_b32 v61, 0xff800000\n"
    "v_cmp_gt_i32 s[90:91], 0, v60\n"
    "v_cmp_gt_i32 s[92:93], 1, v60\n"
    "v_cmp_gt_i32 s[94:95], 2, v60\n"
    "v_cmp_gt_i32 s[96:97], 3, v60\n"
    "v_cndmask_b32 v64, v64, v61, s[90:91]\n"
    "v_cndmask_b32 v65, v65, v61, s[92:93]\n"
    "v_cndmask_b32 v66, v66, v61, s[94:95]\n"
    "v_cndmask_b32 v67, v67, v61, s[96:97]\n"
    "v_cmp_gt_i32 s[90:91], 16, v60\n"
    "v_cmp_gt_i32 s[92:93], 17, v60\n"
    "v_cmp_gt_i32 s[94:95], 18, v60\n"
    "v_cmp_gt_i32 s[96:97], 19, v60\n"
    "v_cndmask_b32 v72, v72, v61, s[90:91]\n"
    "v_cndmask_b32 v73, v73, v61, s[92:93]\n"
    "v_cndmask_b32 v74, v74, v61, s[94:95]\n"
    "v_cndmask_b32 v75, v75, v61, s[96:97]\n"
    "v_cmp_gt_i32 s[90:91], 32, v60\n"
    "v_cmp_gt_i32 s[92:93], 33, v60\n"
    "v_cmp_gt_i32 s[94:95], 34, v60\n"
    "v_cmp_gt_i32 s[96:97], 35, v60\n"
    "v_cndmask_b32 v80, v80, v61, s[90:91]\n"
    "v_cndmask_b32 v81, v81, v61, s[92:93]\n"
    "v_cndmask_b32 v82, v82, v61, s[94:95]\n"
    "v_cndmask_b32 v83, v83, v61, s[96:97]\n"
    "v_cmp_gt_i32 s[90:91], 48, v60\n"
    "v_cmp_gt_i32 s[92:93], 49, v60\n"
    "v_cmp_gt_i32 s[94:95], 50, v60\n"
    "v_cmp_gt_i32 s[96:97], 51, v60\n"
    "v_cndmask_b32 v88, v88, v61, s[90:91]\n"
    "v_cndmask_b32 v89, v89, v61, s[92:93]\n"
    "v_cndmask_b32 v90, v90, v61, s[94:95]\n"
    "v_cndmask_b32 v91, v91, v61, s[96:97]\n"
    "s_branch .Lk2_mask0f_ret\n"
    ".Lk2_hook0o:\n"
    "s_cmp_eq_u32 s83, 3\n"
    "s_cselect_b32 s87, s57, 0\n"
    "s_add_u32 s81, s81, s87\n"
    "s_cmp_lg_u32 s41, 0\n"
    "s_cbranch_scc1 .Lk2_hook0o_m\n"
    "s_add_u32 s44, s44, 1\n"
    "s_mov_b32 s46, 0\n"
    ".Lk2_hook0o_p:\n"
    "v_cmp_eq_u64 vcc, s[16:17], v[12:13]\n"
    "s_nop 1\n"
    "s_and_b32 s87, vcc_lo, 0xffff\n"
    "s_cmp_eq_u32 s87, 0xffff\n"
    "s_cbranch_scc1 .Lk2_hook0o_s\n"
    "s_cmp_ge_u32 s44, 7\n"
    "s_cbranch_scc0 .Lk2_hook0o_m\n"
    "s_add_u32 s46, s46, 1\n"
    "s_cmp_gt_u32 s46, 0x40000\n"
    "s_cbranch_scc1 .Lk2_hook0o_s\n"
    "s_sleep 2\n"
    "buffer_load_dwordx4 v[12:15], v5, s[36:39], s88 offen sc1\n"
    "s_waitcnt vmcnt(0)\n"
    "s_branch .Lk2_hook0o_p\n"
    ".Lk2_hook0o_s:\n"
    "s_add_u32 s36, s48, s70\n"
    "s_addc_u32 s37, s49, 0\n"
    "s_mov_b32 s38, 65536\n"
    "s_mov_b32 s41, 4096\n"
    "s_mov_b32 s40, s43\n"
    "v_mov_b64 v[12:13], 0\n"
    "v_mov_b64 v[14:15], 0\n"
    "v_mov_b64 v[56:57], 0\n"
    "v_mov_b64 v[58:59], 0\n"
    ".Lk2_hook0o_m:\n"
    "s_branch .Lk2_hook0o_ret\n"
    ".Lk2_drain:\n"
    "v_accvgpr_read_b32 v34, a8\n"
    "s_waitcnt vmcnt(0) lgkmcnt(0)\n"
    "s_barrier\n"
    "ds_read_b128 a[64:67], v3 offset:32768\n"
    "v_exp_f32 v20, v108\n"
    "v_exp_f32 v21, v109\n"
    "v_exp_f32 v22, v110\n"
    "ds_read_b128 a[68:71], v3 offset:49152\n"
    "v_exp_f32 v23, v111\n"
    "v_add_f32 v11, v11, v20\n"
    "v_add_f32 v11, v11, v21\n"
    "ds_read_b128 a[72:75], v3 offset:33792\n"
    "v_cvt_pk_bf16_f32 v46, v20, v21\n"
    "v_exp_f32 v16, v112\n"
    "v_exp_f32 v17, v113\n"
    "ds_read_b128 a[76:79], v3 offset:50176\n"
    "v_add_f32 v11, v11, v22\n"
    "v_add_f32 v11, v11, v23\n"
    "v_cvt_pk_bf16_f32 v47, v22, v23\n"
    "ds_read_b128 a[80:83], v3 offset:34816\n"
    "v_exp_f32 v18, v114\n"
    "v_exp_f32 v19, v115\n"
    "v_add_f32 v10, v10, v16\n"
    "ds_read_b128 a[84:87], v3 offset:51200\n"
    "v_add_f32 v10, v10, v17\n"
    "v_cvt_pk_bf16_f32 v48, v16, v17\n"
    "v_exp_f32 v20, v120\n"
    "ds_read_b128 a[88:91], v3 offset:35840\n"
    "v_exp_f32 v21, v121\n"
    "v_add_f32 v10, v10, v18\n"
    "v_add_f32 v10, v10, v19\n"
    "ds_read_b128 a[92:95], v3 offset:52224\n"
    "v_cvt_pk_bf16_f32 v49, v18, v19\n"
    "v_exp_f32 v22, v122\n"
    "v_exp_f32 v23, v123\n"
    "ds_read_b128 a[96:99], v3 offset:36864\n"
    "v_add_f32 v10, v10, v20\n"
    "v_add_f32 v10, v10, v21\n"
    "v_cvt_pk_bf16_f32 v50, v20, v21\n"
    "ds_read_b128 a[100:103], v3 offset:53248\n"
    "v_exp_f32 v16, v116\n"
    "v_exp_f32 v17, v117\n"
    "v_add_f32 v10, v10, v22\n"
    "ds_read_b128 a[104:107], v3 offset:37888\n"
    "v_add_f32 v10, v10, v23\n"
    "v_cvt_pk_bf16_f32 v51, v22, v23\n"
    "v_exp_f32 v18, v118\n"
    "ds_read_b128 a[108:111], v3 offset:54272\n"
    "v_exp_f32 v19, v119\n"
    "v_add_f32 v11, v11, v16\n"
    "v_add_f32 v11, v11, v17\n"
    "v_cvt_pk_bf16_f32 v52, v16, v17\n"
    "v_exp_f32 v20, v124\n"
    "v_exp_f32 v21, v125\n"
    "v_add_f32 v11, v11, v18\n"
    "v_add_f32 v11, v11, v19\n"
    "v_cvt_pk_bf16_f32 v53, v18, v19\n"
    "v_exp_f32 v22, v126\n"
    "v_exp_f32 v23, v127\n"
    "v_add_f32 v11, v11, v20\n"
    "v_add_f32 v11, v11, v21\n"
    "v_cvt_pk_bf16_f32 v54, v20, v21\n"
    "s_nop 0\n"
    "v_add_f32 v11, v11, v22\n"
    "v_add_f32 v11, v11, v23\n"
    "v_cvt_pk_bf16_f32 v55, v22, v23\n"
    "v_max_f32 v60, v10, v11\n"
    "s_nop 1\n"
    "v_cmp_nge_f32 vcc, s89, v60\n"
    "s_nop 1\n"
    "s_cbranch_vccnz .Lk2_rare1p\n"
    ".Lk2_rare1p_ret:\n"
    "s_nop 1\n"
    "v_add_f32 v8, v8, v10\n"
    "s_nop 1\n"
    "v_add_f32 v9, v9, v11\n"
    "s_nop 1\n"
    "s_waitcnt lgkmcnt(10)\n"
    "v_mfma_f32_16x16x32_bf16 v[128:131], a[64:67], v[40:43], v[128:131]\n"
    "ds_read_b128 a[112:115], v3 offset:38912\n"
    "v_mov_b32 v60, v8\n"
    "v_mfma_f32_16x16x32_bf16 v[128:131], a[68:71], v[48:51], v[128:131]\n"
    "v_add_f32 v56, v56, v12\n"
    "v_mov_b32 v61, v9\n"
    "v_mfma_f32_16x16x32_bf16 v[192:195], a[68:71], v[52:55], v[192:195]\n"
    "ds_read_b128 a[116:119], v3 offset:55296\n"
    "v_add_f32 v57, v57, v13\n"
    "v_mfma_f32_16x16x32_bf16 v[192:195], a[64:67], v[44:47], v[192:195]\n"
    "s_nop 0\n"
    "s_waitcnt lgkmcnt(10)\n"
    "v_mfma_f32_16x16x32_bf16 v[196:199], a[72:75], v[44:47], v[196:199]\n"
    "ds_read_b128 a[120:123], v3 offset:39936\n"
    "v_add_f32 v58, v58, v14\n"
    "v_mfma_f32_16x16x32_bf16 v[196:199], a[76:79], v[52:55], v[196:199]\n"
    "v_permlane32_swap_b32 v60, v8\n"
    "v_add_f32 v59, v59, v15\n"
    "v_mfma_f32_16x16x32_bf16 v[132:135], a[76:79], v[48:51], v[132:135]\n"
    "ds_read_b128 a[124:127], v3 offset:56320\n"
    "v_permlane32_swap_b32 v61, v9\n"
    "v_mfma_f32_16x16x32_bf16 v[132:135], a[72:75], v[40:43], v[132:135]\n"
    "buffer_load_dwordx4 v[12:15], v5, s[36:39], s88 offen sc1\n"
    "s_waitcnt lgkmcnt(10)\n"
    "v_mfma_f32_16x16x32_bf16 v[136:139], a[80:83], v[40:43], v[136:139]\n"
    "ds_read_b128 a[128:131], v3 offset:40960\n"
    "v_add_f32 v8, v8, v60\n"
    "v_mfma_f32_16x16x32_bf16 v[136:139], a[84:87], v[48:51], v[136:139]\n"
    "s_add_u32 s88, s88, s41\n"
    "v_add_f32 v9, v9, v61\n"
    "v_mfma_f32_16x16x32_bf16 v[200:203], a[84:87], v[52:55], v[200:203]\n"
    "ds_read_b128 a[132:135], v3 offset:57344\n"
    "v_mov_b32 v60, v8\n"
    "v_mfma_f32_16x16x32_bf16 v[200:203], a[80:83], v[44:47], v[200:203]\n"
    "v_mov_b32 v61, v9\n"
    "s_waitcnt lgkmcnt(10)\n"
    "v_mfma_f32_16x16x32_bf16 v[204:207], a[88:91], v[44:47], v[204:207]\n"
    "ds_read_b128 a[136:139], v3 offset:41984\n"
    "s_nop 0\n"
    "v_mfma_f32_16x16x32_bf16 v[204:207], a[92:95], v[52:55], v[204:207]\n"
    "v_mfma_f32_16x16x32_bf16 v[140:143], a[92:95], v[48:51], v[140:143]\n"
    "ds_read_b128 a[140:143], v3 offset:58368\n"
    "v_permlane16_swap_b32 v60, v8\n"
    "v_mfma_f32_16x16x32_bf16 v[140:143], a[88:91], v[40:43], v[140:143]\n"
    "v_permlane16_swap_b32 v61, v9\n"
    "s_waitcnt lgkmcnt(10)\n"
    "v_mfma_f32_16x16x32_bf16 v[144:147], a[96:99], v[40:43], v[144:147]\n"
    "ds_read_b128 a[144:147], v3 offset:43008\n"
    "v_mfma_f32_16x16x32_bf16 v[144:147], a[100:103], v[48:51], v[144:147]\n"
    "v_add_f32 v8, v8, v60\n"
    "v_mfma_f32_16x16x32_bf16 v[208:211], a[100:103], v[52:55], v[208:211]\n"
    "ds_read_b128 a[148:151], v3 offset:59392\n"
    "v_add_f32 v9, v9, v61\n"
    "v_mfma_f32_16x16x32_bf16 v[208:211], a[96:99], v[44:47], v[208:211]\n"
    "s_waitcnt lgkmcnt(10)\n"
    "v_mfma_f32_16x16x32_bf16 v[212:215], a[104:107], v[44:47], v[212:215]\n"
    "ds_read_b128 a[152:155], v3 offset:44032\n"
    "v_mfma_f32_16x16x32_bf16 v[212:215], a[108:111], v[52:55], v[212:215]\n"
    "v_rcp_f32 v60, v8\n"
    "v_mfma_f32_16x16x32_bf16 v[148:151], a[108:111], v[48:51], v[148:151]\n"
    "v_rcp_f32 v62, v9\n"
    "v_mfma_f32_16x16x32_bf16 v[148:151], a[104:107], v[40:43], v[148:151]\n"
    "ds_read_b128 a[156:159], v3 offset:60416\n"
    "v_mov_b32 v16, v6\n"
    "s_waitcnt lgkmcnt(10)\n"
    "v_mfma_f32_16x16x32_bf16 v[152:155], a[112:115], v[40:43], v[152:155]\n"
    "ds_read_b128 a[160:163], v3 offset:45056\n"
    "v_mov_b32 v17, v8\n"
    "v_mfma_f32_16x16x32_bf16 v[152:155], a[116:119], v[48:51], v[152:155]\n"
    "v_mov_b32 v18, v7\n"
    "v_mfma_f32_16x16x32_bf16 v[216:219], a[116:119], v[52:55], v[216:219]\n"
    "ds_read_b128 a[164:167], v3 offset:61440\n"
    "v_mfma_f32_16x16x32_bf16 v[216:219], a[112:115], v[44:47], v[216:219]\n"
    "v_mov_b32 v19, v9\n"
    "s_waitcnt lgkmcnt(10)\n"
    "v_mfma_f32_16x16x32_bf16 v[220:223], a[120:123], v[44:47], v[220:223]\n"
    "ds_read_b128 a[168:171], v3 offset:46080\n"
    "v_mfma_f32_16x16x32_bf16 v[220:223], a[124:127], v[52:55], v[220:223]\n"
    "buffer_store_dwordx4 v[16:19], v1, s[48:51], s65 offen sc1\n"
    "v_mfma_f32_16x16x32_bf16 v[156:159], a[124:127], v[48:51], v[156:159]\n"
    "s_lshl_b32 s87, s62, 13\n"
    "s_add_u32 s87, s87, 65536\n"
    "v_mfma_f32_16x16x32_bf16 v[156:159], a[120:123], v[40:43], v[156:159]\n"
    "v_add_u32 v38, s87, v3\n"
    "v_accvgpr_mov_b32 a192, a32\n"
    "s_waitcnt lgkmcnt(9)\n"
    "v_mfma_f32_16x16x32_bf16 v[160:163], a[128:131], v[40:43], v[160:163]\n"
    "v_accvgpr_mov_b32 a193, a33\n"
    "v_accvgpr_mov_b32 a194, a34\n"
    "v_mfma_f32_16x16x32_bf16 v[160:163], a[132:135], v[48:51], v[160:163]\n"
    "ds_read_b128 a[172:175], v3 offset:62464\n"
    "v_accvgpr_mov_b32 a195, a35\n"
    "v_mfma_f32_16x16x32_bf16 v[224:227], a[132:135], v[52:55], v[224:227]\n"
    "v_accvgpr_mov_b32 a196, a36\n"
    "v_accvgpr_mov_b32 a197, a37\n"
    "v_mfma_f32_16x16x32_bf16 v[224:227], a[128:131], v[44:47], v[224:227]\n"
    "ds_read_b128 a[176:179], v3 offset:47104\n"
    "v_accvgpr_mov_b32 a198, a38\n"
    "s_waitcnt lgkmcnt(9)\n"
    "v_mfma_f32_16x16x32_bf16 v[228:231], a[136:139], v[44:47], v[228:231]\n"
    "v_accvgpr_mov_b32 a199, a39\n"
    "v_accvgpr_mov_b32 a200, a40\n"
    "v_mfma_f32_16x16x32_bf16 v[228:231], a[140:143], v[52:55], v[228:231]\n"
    "ds_read_b128 a[180:183], v3 offset:63488\n"
    "v_accvgpr_mov_b32 a201, a41\n"
    "v_mfma_f32_16x16x32_bf16 v[164:167], a[140:143], v[48:51], v[164:167]\n"
    "ds_read_b128 a[184:187], v3 offset:48128\n"
    "v_accvgpr_mov_b32 a202, a42\n"
    "v_mfma_f32_16x16x32_bf16 v[164:167], a[136:139], v[40:43], v[164:167]\n"
    "v_accvgpr_mov_b32 a203, a43\n"
    "v_accvgpr_mov_b32 a204, a44\n"
    "s_waitcnt lgkmcnt(9)\n"
    "v_mfma_f32_16x16x32_bf16 v[168:171], a[144:147], v[40:43], v[168:171]\n"
    "ds_read_b128 a[188:191], v3 offset:64512\n"
    "v_accvgpr_mov_b32 a205, a45\n"
    "v_mfma_f32_16x16x32_bf16 v[168:171], a[148:151], v[48:51], v[168:171]\n"
    "v_accvgpr_mov_b32 a206, a46\n"
    "v_accvgpr_mov_b32 a207, a47\n"
    "v_mfma_f32_16x16x32_bf16 v[232:235], a[148:151], v[52:55], v[232:235]\n"
    "v_accvgpr_mov_b32 a208, a48\n"
    "v_accvgpr_mov_b32 a209, a49\n"
    "v_mfma_f32_16x16x32_bf16 v[232:235], a[144:147], v[44:47], v[232:235]\n"
    "v_accvgpr_mov_b32 a210, a50\n"
    "v_accvgpr_mov_b32 a211, a51\n"
    "s_waitcnt lgkmcnt(8)\n"
    "v_mfma_f32_16x16x32_bf16 v[236:239], a[152:155], v[44:47], v[236:239]\n"
    "v_accvgpr_mov_b32 a212, a52\n"
    "v_mfma_f32_16x16x32_bf16 v[236:239], a[156:159], v[52:55], v[236:239]\n"
    "v_accvgpr_mov_b32 a213, a53\n"
    "v_mfma_f32_16x16x32_bf16 v[172:175], a[156:159], v[48:51], v[172:175]\n"
    "v_accvgpr_mov_b32 a214, a54\n"
    "v_accvgpr_mov_b32 a215, a55\n"
    "v_mfma_f32_16x16x32_bf16 v[172:175], a[152:155], v[40:43], v[172:175]\n"
    "v_accvgpr_mov_b32 a216, a56\n"
    "s_waitcnt lgkmcnt(6)\n"
    "v_mfma_f32_16x16x32_bf16 v[176:179], a[160:163], v[40:43], v[176:179]\n"
    "v_accvgpr_mov_b32 a217, a57\n"
    "v_mfma_f32_16x16x32_bf16 v[176:179], a[164:167], v[48:51], v[176:179]\n"
    "v_accvgpr_mov_b32 a218, a58\n"
    "v_mfma_f32_16x16x32_bf16 v[240:243], a[164:167], v[52:55], v[240:243]\n"
    "v_accvgpr_mov_b32 a219, a59\n"
    "v_mfma_f32_16x16x32_bf16 v[240:243], a[160:163], v[44:47], v[240:243]\n"
    "v_accvgpr_mov_b32 a220, a60\n"
    "s_waitcnt lgkmcnt(4)\n"
    "v_mfma_f32_16x16x32_bf16 v[244:247], a[168:171], v[44:47], v[244:247]\n"
    "v_accvgpr_mov_b32 a221, a61\n"
    "v_mfma_f32_16x16x32_bf16 v[244:247], a[172:175], v[52:55], v[244:247]\n"
    "v_accvgpr_mov_b32 a222, a62\n"
    "v_mfma_f32_16x16x32_bf16 v[180:183], a[172:175], v[48:51], v[180:183]\n"
    "v_accvgpr_mov_b32 a223, a63\n"
    "v_mfma_f32_16x16x32_bf16 v[180:183], a[168:171], v[40:43], v[180:183]\n"
    "ds_read_b128 a[224:227], v38 offset:0\n"
    "s_waitcnt lgkmcnt(3)\n"
    "v_mfma_f32_16x16x32_bf16 v[184:187], a[176:179], v[40:43], v[184:187]\n"
    "ds_read_b128 a[228:231], v38 offset:1024\n"
    "v_mfma_f32_16x16x32_bf16 v[184:187], a[180:183], v[48:51], v[184:187]\n"
    "ds_read_b128 a[232:235], v38 offset:2048\n"
    "v_mfma_f32_16x16x32_bf16 v[248:251], a[180:183], v[52:55], v[248:251]\n"
    "ds_read_b128 a[236:239], v38 offset:3072\n"
    "v_mfma_f32_16x16x32_bf16 v[248:251], a[176:179], v[44:47], v[248:251]\n"
    "ds_read_b128 a[240:243], v38 offset:4096\n"
    "s_waitcnt lgkmcnt(5)\n"
    "v_mfma_f32_16x16x32_bf16 v[252:255], a[184:187], v[44:47], v[252:255]\n"
    "ds_read_b128 a[244:247], v38 offset:5120\n"
    "v_mfma_f32_16x16x32_bf16 v[252:255], a[188:191], v[52:55], v[252:255]\n"
    "ds_read_b128 a[248:251], v38 offset:6144\n"
    "v_mfma_f32_16x16x32_bf16 v[188:191], a[188:191], v[48:51], v[188:191]\n"
    "ds_read_b128 a[252:255], v38 offset:7168\n"
    "v_mfma_f32_16x16x32_bf16 v[188:191], a[184:187], v[40:43], v[188:191]\n"
    "s_waitcnt lgkmcnt(0)\n"
    "s_nop 7\n"
    "v_pk_mul_f32 v[128:129], v[128:129], v[60:61] op_sel_hi:[1,0]\n"
    "v_pk_mul_f32 v[130:131], v[130:131], v[60:61] op_sel_hi:[1,0]\n"
    "v_pk_mul_f32 v[132:133], v[132:133], v[60:61] op_sel_hi:[1,0]\n"
    "v_pk_mul_f32 v[134:135], v[134:135], v[60:61] op_sel_hi:[1,0]\n"
    "v_cvt_pk_bf16_f32 v128, v128, v129\n"
    "v_cvt_pk_bf16_f32 v129, v130, v131\n"
    "v_cvt_pk_bf16_f32 v130, v132, v133\n"
    "v_cvt_pk_bf16_f32 v131, v134, v135\n"
    "s_add_u32 s87, s64, 0\n"
    "buffer_store_dwordx4 v[128:131], v34, s[48:51], s87 offen offset:0 sc1\n"
    "v_pk_mul_f32 v[136:137], v[136:137], v[60:61] op_sel_hi:[1,0]\n"
    "v_pk_mul_f32 v[138:139], v[138:139], v[60:61] op_sel_hi:[1,0]\n"
    "v_pk_mul_f32 v[140:141], v[140:141], v[60:61] op_sel_hi:[1,0]\n"
    "v_pk_mul_f32 v[142:143], v[142:143], v[60:61] op_sel_hi:[1,0]\n"
    "v_cvt_pk_bf16_f32 v136, v136, v137\n"
    "v_cvt_pk_bf16_f32 v137, v138, v139\n"
    "v_cvt_pk_bf16_f32 v138, v140, v141\n"
    "v_cvt_pk_bf16_f32 v139, v142, v143\n"
    "buffer_store_dwordx4 v[136:139], v34, s[48:51], s87 offen offset:1024 sc1\n"
    "v_pk_mul_f32 v[144:145], v[144:145], v[60:61] op_sel_hi:[1,0]\n"
    "v_pk_mul_f32 v[146:147], v[146:147], v[60:61] op_sel_hi:[1,0]\n"
    "v_pk_mul_f32 v[148:149], v[148:149], v[60:61] op_sel_hi:[1,0]\n"
    "v_pk_mul_f32 v[150:151], v[150:151], v[60:61] op_sel_hi:[1,0]\n"
    "v_cvt_pk_bf16_f32 v144, v144, v145\n"
    "v_cvt_pk_bf16_f32 v145, v146, v147\n"
    "v_cvt_pk_bf16_f32 v146, v148, v149\n"
    "v_cvt_pk_bf16_f32 v147, v150, v151\n"
    "buffer_store_dwordx4 v[144:147], v34, s[48:51], s87 offen offset:2048 sc1\n"
    "v_pk_mul_f32 v[152:153], v[152:153], v[60:61] op_sel_hi:[1,0]\n"
    "v_pk_mul_f32 v[154:155], v[154:155], v[60:61] op_sel_hi:[1,0]\n"
    "v_pk_mul_f32 v[156:157], v[156:157], v[60:61] op_sel_hi:[1,0]\n"
    "v_pk_mul_f32 v[158:159], v[158:159], v[60:61] op_sel_hi:[1,0]\n"
    "v_cvt_pk_bf16_f32 v152, v152, v153\n"
    "v_cvt_pk_bf16_f32 v153, v154, v155\n"
    "v_cvt_pk_bf16_f32 v154, v156, v157\n"
    "v_cvt_pk_bf16_f32 v155, v158, v159\n"
    "buffer_store_dwordx4 v[152:155], v34, s[48:51], s87 offen offset:3072 sc1\n"
    "v_pk_mul_f32 v[160:161], v[160:161], v[60:61] op_sel_hi:[1,0]\n"
    "v_pk_mul_f32 v[162:163], v[162:163], v[60:61] op_sel_hi:[1,0]\n"
    "v_pk_mul_f32 v[164:165], v[164:165], v[60:61] op_sel_hi:[1,0]\n"
    "v_pk_mul_f32 v[166:167], v[166:167], v[60:61] op_sel_hi:[1,0]\n"
    "v_cvt_pk_bf16_f32 v160, v160, v161\n"
    "v_cvt_pk_bf16_f32 v161, v162, v163\n"
    "v_cvt_pk_bf16_f32 v162, v164, v165\n"
    "v_cvt_pk_bf16_f32 v163, v166, v167\n"
    "s_add_u32 s87, s64, 4096\n"
    "buffer_store_dwordx4 v[160:163], v34, s[48:51], s87 offen offset:0 sc1\n"
    "v_pk_mul_f32 v[168:169], v[168:169], v[60:61] op_sel_hi:[1,0]\n"
    "v_pk_mul_f32 v[170:171], v[170:171], v[60:61] op_sel_hi:[1,0]\n"
    "v_pk_mul_f32 v[172:173], v[172:173], v[60:61] op_sel_hi:[1,0]\n"
    "v_pk_mul_f32 v[174:175], v[174:175], v[60:61] op_sel_hi:[1,0]\n"
    "v_cvt_pk_bf16_f32 v168, v168, v169\n"
    "v_cvt_pk_bf16_f32 v169, v170, v171\n"
    "v_cvt_pk_bf16_f32 v170, v172, v173\n"
    "v_cvt_pk_bf16_f32 v171, v174, v175\n"
    "buffer_store_dwordx4 v[168:171], v34, s[48:51], s87 offen offset:1024 sc1\n"
    "v_pk_mul_f32 v[176:177], v[176:177], v[60:61] op_sel_hi:[1,0]\n"
    "v_pk_mul_f32 v[178:179], v[178:179], v[60:61] op_sel_hi:[1,0]\n"
    "v_pk_mul_f32 v[180:181], v[180:181], v[60:61] op_sel_hi:[1,0]\n"
    "v_pk_mul_f32 v[182:183], v[182:183], v[60:61] op_sel_hi:[1,0]\n"
    "v_cvt_pk_bf16_f32 v176, v176, v177\n"
    "v_cvt_pk_bf16_f32 v177, v178, v179\n"
    "v_cvt_pk_bf16_f32 v178, v180, v181\n"
    "v_cvt_pk_bf16_f32 v179, v182, v183\n"
    "buffer_store_dwordx4 v[176:179], v34, s[48:51], s87 offen offset:2048 sc1\n"
    "v_pk_mul_f32 v[184:185], v[184:185], v[60:61] op_sel_hi:[1,0]\n"
    "v_pk_mul_f32 v[186:187], v[186:187], v[60:61] op_sel_hi:[1,0]\n"
    "v_pk_mul_f32 v[188:189], v[188:189], v[60:61] op_sel_hi:[1,0]\n"
    "v_pk_mul_f32 v[190:191], v[190:191], v[60:61] op_sel_hi:[1,0]\n"
    "v_cvt_pk_bf16_f32 v184, v184, v185\n"
    "v_cvt_pk_bf16_f32 v185, v186, v187\n"
    "v_cvt_pk_bf16_f32 v186, v188, v189\n"
    "v_cvt_pk_bf16_f32 v187, v190, v191\n"
    "buffer_store_dwordx4 v[184:187], v34, s[48:51], s87 offen offset:3072 sc1\n"
    "v_pk_mul_f32 v[192:193], v[192:193], v[62:63] op_sel_hi:[1,0]\n"
    "v_pk_mul_f32 v[194:195], v[194:195], v[62:63] op_sel_hi:[1,0]\n"
    "v_pk_mul_f32 v[196:197], v[196:197], v[62:63] op_sel_hi:[1,0]\n"
    "v_pk_mul_f32 v[198:199], v[198:199], v[62:63] op_sel_hi:[1,0]\n"
    "v_cvt_pk_bf16_f32 v192, v192, v193\n"
    "v_cvt_pk_bf16_f32 v193, v194, v195\n"
    "v_cvt_pk_bf16_f32 v194, v196, v197\n"
    "v_cvt_pk_bf16_f32 v195, v198, v199\n"
    "s_add_u32 s87, s64, 8192\n"
    "buffer_store_dwordx4 v[192:195], v34, s[48:51], s87 offen offset:0 sc1\n"
    "v_pk_mul_f32 v[200:201], v[200:201], v[62:63] op_sel_hi:[1,0]\n"
    "v_pk_mul_f32 v[202:203], v[202:203], v[62:63] op_sel_hi:[1,0]\n"
    "v_pk_mul_f32 v[204:205], v[204:205], v[62:63] op_sel_hi:[1,0]\n"
    "v_pk_mul_f32 v[206:207], v[206:207], v[62:63] op_sel_hi:[1,0]\n"
    "v_cvt_pk_bf16_f32 v200, v200, v201\n"
    "v_cvt_pk_bf16_f32 v201, v202, v203\n"
    "v_cvt_pk_bf16_f32 v202, v204, v205\n"
    "v_cvt_pk_bf16_f32 v203, v206, v207\n"
    "buffer_store_dwordx4 v[200:203], v34, s[48:51], s87 offen offset:1024 sc1\n"
    "v_pk_mul_f32 v[208:209], v[208:209], v[62:63] op_sel_hi:[1,0]\n"
    "v_pk_mul_f32 v[210:211], v[210:211], v[62:63] op_sel_hi:[1,0]\n"
    "v_pk_mul_f32 v[212:213], v[212:213], v[62:63] op_sel_hi:[1,0]\n"
    "v_pk_mul_f32 v[214:215], v[214:215], v[62:63] op_sel_hi:[1,0]\n"
    "v_cvt_pk_bf16_f32 v208, v208, v209\n"
    "v_cvt_pk_bf16_f32 v209, v210, v211\n"
    "v_cvt_pk_bf16_f32 v210, v212, v213\n"
    "v_cvt_pk_bf16_f32 v211, v214, v215\n"
    "buffer_store_dwordx4 v[208:211], v34, s[48:51], s87 offen offset:2048 sc1\n"
    "v_pk_mul_f32 v[216:217], v[216:217], v[62:63] op_sel_hi:[1,0]\n"
    "v_pk_mul_f32 v[218:219], v[218:219], v[62:63] op_sel_hi:[1,0]\n"
    "v_pk_mul_f32 v[220:221], v[220:221], v[62:63] op_sel_hi:[1,0]\n"
    "v_pk_mul_f32 v[222:223], v[222:223], v[62:63] op_sel_hi:[1,0]\n"
    "v_cvt_pk_bf16_f32 v216, v216, v217\n"
    "v_cvt_pk_bf16_f32 v217, v218, v219\n"
    "v_cvt_pk_bf16_f32 v218, v220, v221\n"
    "v_cvt_pk_bf16_f32 v219, v222, v223\n"
    "buffer_store_dwordx4 v[216:219], v34, s[48:51], s87 offen offset:3072 sc1\n"
    "v_pk_mul_f32 v[224:225], v[224:225], v[62:63] op_sel_hi:[1,0]\n"
    "v_pk_mul_f32 v[226:227], v[226:227], v[62:63] op_sel_hi:[1,0]\n"
    "v_pk_mul_f32 v[228:229], v[228:229], v[62:63] op_sel_hi:[1,0]\n"
    "v_pk_mul_f32 v[230:231], v[230:231], v[62:63] op_sel_hi:[1,0]\n"
    "v_cvt_pk_bf16_f32 v224, v224, v225\n"
    "v_cvt_pk_bf16_f32 v225, v226, v227\n"
    "v_cvt_pk_bf16_f32 v226, v228, v229\n"
    "v_cvt_pk_bf16_f32 v227, v230, v231\n"
    "s_add_u32 s87, s64, 12288\n"
    "buffer_store_dwordx4 v[224:227], v34, s[48:51], s87 offen offset:0 sc1\n"
    "v_pk_mul_f32 v[232:233], v[232:233], v[62:63] op_sel_hi:[1,0]\n"
    "v_pk_mul_f32 v[234:235], v[234:235], v[62:63] op_sel_hi:[1,0]\n"
    "v_pk_mul_f32 v[236:237], v[236:237], v[62:63] op_sel_hi:[1,0]\n"
    "v_pk_mul_f32 v[238:239], v[238:239], v[62:63] op_sel_hi:[1,0]\n"
    "v_cvt_pk_bf16_f32 v232, v232, v233\n"
    "v_cvt_pk_bf16_f32 v233, v234, v235\n"
    "v_cvt_pk_bf16_f32 v234, v236, v237\n"
    "v_cvt_pk_bf16_f32 v235, v238, v239\n"
    "buffer_store_dwordx4 v[232:235], v34, s[48:51], s87 offen offset:1024 sc1\n"
    "v_pk_mul_f32 v[240:241], v[240:241], v[62:63] op_sel_hi:[1,0]\n"
    "v_pk_mul_f32 v[242:243], v[242:243], v[62:63] op_sel_hi:[1,0]\n"
    "v_pk_mul_f32 v[244:245], v[244:245], v[62:63] op_sel_hi:[1,0]\n"
    "v_pk_mul_f32 v[246:247], v[246:247], v[62:63] op_sel_hi:[1,0]\n"
    "v_cvt_pk_bf16_f32 v240, v240, v241\n"
    "v_cvt_pk_bf16_f32 v241, v242, v243\n"
    "v_cvt_pk_bf16_f32 v242, v244, v245\n"
    "v_cvt_pk_bf16_f32 v243, v246, v247\n"
    "buffer_store_dwordx4 v[240:243], v34, s[48:51], s87 offen offset:2048 sc1\n"
    "v_pk_mul_f32 v[248:249], v[248:249], v[62:63] op_sel_hi:[1,0]\n"
    "v_pk_mul_f32 v[250:251], v[250:251], v[62:63] op_sel_hi:[1,0]\n"
    "v_pk_mul_f32 v[252:253], v[252:253], v[62:63] op_sel_hi:[1,0]\n"
    "v_pk_mul_f32 v[254:255], v[254:255], v[62:63] op_sel_hi:[1,0]\n"
    "v_cvt_pk_bf16_f32 v248, v248, v249\n"
    "v_cvt_pk_bf16_f32 v249, v250, v251\n"
    "v_cvt_pk_bf16_f32 v250, v252, v253\n"
    "v_cvt_pk_bf16_f32 v251, v254, v255\n"
    "buffer_store_dwordx4 v[248:251], v34, s[48:51], s87 offen offset:3072 sc1\n"
    "s_mov_b32 s84, s73\n"
    "s_mov_b32 s53, s58\n"
    "s_mov_b32 s54, s59\n"
    "s_mov_b32 s55, 1\n"
    "s_mov_b32 s59, 0\n"
    "s_mov_b32 s58, s63\n"
    "s_branch .Lk2_seg\n"
    ".Lk2_seg:\n"
    "v_mov_b64 v[128:129], 0\n"
    "v_mov_b64 v[130:131], 0\n"
    "v_mov_b64 v[132:133], 0\n"
    "v_mov_b64 v[134:135], 0\n"
    "v_mov_b64 v[136:137], 0\n"
    "v_mov_b64 v[138:139], 0\n"
    "v_mov_b64 v[140:141], 0\n"
    "v_mov_b64 v[142:143], 0\n"
    "v_mov_b64 v[144:145], 0\n"
    "v_mov_b64 v[146:147], 0\n"
    "v_mov_b64 v[148:149], 0\n"
    "v_mov_b64 v[150:151], 0\n"
    "v_mov_b64 v[152:153], 0\n"
    "v_mov_b64 v[154:155], 0\n"
    "v_mov_b64 v[156:157], 0\n"
    "v_mov_b64 v[158:159], 0\n"
    "v_mov_b64 v[160:161], 0\n"
    "v_mov_b64 v[162:163], 0\n"
    "v_mov_b64 v[164:165], 0\n"
    "v_mov_b64 v[166:167], 0\n"
    "v_mov_b64 v[168:169], 0\n"
    "v_mov_b64 v[170:171], 0\n"
    "v_mov_b64 v[172:173], 0\n"
    "v_mov_b64 v[174:175], 0\n"
    "v_mov_b64 v[176:177], 0\n"
    "v_mov_b64 v[178:179], 0\n"
    "v_mov_b64 v[180:181], 0\n"
    "v_mov_b64 v[182:183], 0\n"
    "v_mov_b64 v[184:185], 0\n"
    "v_mov_b64 v[186:187], 0\n"
    "v_mov_b64 v[188:189], 0\n"
    "v_mov_b64 v[190:191], 0\n"
    "v_mov_b64 v[192:193], 0\n"
    "v_mov_b64 v[194:195], 0\n"
    "v_mov_b64 v[196:197], 0\n"
    "v_mov_b64 v[198:199], 0\n"
    "v_mov_b64 v[200:201], 0\n"
    "v_mov_b64 v[202:203], 0\n"
    "v_mov_b64 v[204:205], 0\n"
    "v_mov_b64 v[206:207], 0\n"
    "v_mov_b64 v[208:209], 0\n"
    "v_mov_b64 v[210:211], 0\n"
    "v_mov_b64 v[212:213], 0\n"
    "v_mov_b64 v[214:215], 0\n"
    "v_mov_b64 v[216:217], 0\n"
    "v_mov_b64 v[218:219], 0\n"
    "v_mov_b64 v[220:221], 0\n"
    "v_mov_b64 v[222:223], 0\n"
    "v_mov_b64 v[224:225], 0\n"
    "v_mov_b64 v[226:227], 0\n"
    "v_mov_b64 v[228:229], 0\n"
    "v_mov_b64 v[230:231], 0\n"
    "v_mov_b64 v[232:233], 0\n"
    "v_mov_b64 v[234:235], 0\n"
    "v_mov_b64 v[236:237], 0\n"
    "v_mov_b64 v[238:239], 0\n"
    "v_mov_b64 v[240:241], 0\n"
    "v_mov_b64 v[242:243], 0\n"
    "v_mov_b64 v[244:245], 0\n"
    "v_mov_b64 v[246:247], 0\n"
    "v_mov_b64 v[248:249], 0\n"
    "v_mov_b64 v[250:251], 0\n"
    "v_mov_b64 v[252:253], 0\n"
    "v_mov_b64 v[254:255], 0\n"
    "s_waitcnt vmcnt(16)\n"
    "v_add_f32 v56, v56, v12\n"
    "v_add_f32 v57, v57, v13\n"
    "v_add_f32 v58, v58, v14\n"
    "v_add_f32 v59, v59, v15\n"
    "buffer_load_dwordx4 v[12:15], v5, s[36:39], s88 offen sc1\n"
    "s_add_u32 s88, s88, s41\n"
    "s_lshl_b32 s87, s54, 15\n"
    "s_add_u32 s82, s53, s87\n"
    "s_add_u32 s82, s82, s57\n"
    "s_add_u32 s81, s53, 0x1000000\n"
    "s_mov_b32 s83, s54\n"
    "s_and_b32 s42, s55, 1\n"
    "s_mov_b32 s45, s42\n"
    "s_mul_i32 s42, s42, 4\n"
    "s_cmp_lg_u32 s57, 0\n"
    "s_cselect_b32 s43, 24, 0\n"
    "s_or_b32 s43, s43, s42\n"
    "s_cmp_lg_u32 s41, 0\n"
    "s_cselect_b32 s40, s43, -1\n"
    "s_barrier\n"
    ".Lk2_seg1:\n"
    "ds_read_b128 a[64:67], v2 offset:0\n"
    "ds_read_b128 a[68:71], v2 offset:1024\n"
    "ds_read_b128 a[72:75], v2 offset:2048\n"
    "ds_read_b128 a[76:79], v2 offset:3072\n"
    "ds_read_b128 a[80:83], v2 offset:4096\n"
    "ds_read_b128 a[84:87], v2 offset:5120\n"
    "ds_read_b128 a[88:91], v2 offset:6144\n"
    "ds_read_b128 a[92:95], v2 offset:7168\n"
    "ds_read_b128 a[96:99], v2 offset:8192\n"
    "ds_read_b128 a[100:103], v2 offset:9216\n"
    "ds_read_b128 a[104:107], v2 offset:10240\n"
    "ds_read_b128 a[108:111], v2 offset:11264\n"
    "s_waitcnt lgkmcnt(8)\n"
    "v_mfma_f32_16x16x32_bf16 v[64:67], a[64:67], a[192:195], 0\n"
    "ds_read_b128 a[112:115], v2 offset:12288\n"
    "s_add_u32 m0, s61, 65536\n"
    "s_add_u32 s86, s81, 4096\n"
    "v_accvgpr_read_b32 v62, a10\n"
    "v_mfma_f32_16x16x32_bf16 v[64:67], a[68:71], a[196:199], v[64:67]\n"
    "ds_read_b128 a[116:119], v2 offset:13312\n"
    "buffer_load_dwordx4 v4, s[48:51], s81 offen offset:0 sc1 lds\n"
    "v_mov_b32 v63, 0xff800000\n"
    "v_mfma_f32_16x16x32_bf16 v[64:67], a[72:75], a[200:203], v[64:67]\n"
    "ds_read_b128 a[120:123], v2 offset:14336\n"
    "s_bitcmp1_b32 s42, s83\n"
    "s_cselect_b32 s87, 0, 64\n"
    "v_add_u32 v62, s87, v62\n"
    "v_mfma_f32_16x16x32_bf16 v[64:67], a[76:79], a[204:207], v[64:67]\n"
    "ds_read_b128 a[124:127], v2 offset:15360\n"
    "buffer_load_dwordx4 v4, s[48:51], s81 offen offset:1024 sc1 lds\n"
    "v_mov_b32 v48, s16\n"
    "s_waitcnt lgkmcnt(8)\n"
    "v_mfma_f32_16x16x32_bf16 v[64:67], a[80:83], a[208:211], v[64:67]\n"
    "ds_read_b128 a[128:131], v2 offset:16384\n"
    "buffer_load_dwordx4 v4, s[48:51], s81 offen offset:2048 sc1 lds\n"
    "v_cmp_gt_i32 s[90:91], 0, v62\n"
    "v_mfma_f32_16x16x32_bf16 v[64:67], a[84:87], a[212:215], v[64:67]\n"
    "ds_read_b128 a[132:135], v2 offset:17408\n"
    "v_cmp_gt_i32 s[92:93], 1, v62\n"
    "v_cmp_gt_i32 s[94:95], 2, v62\n"
    "v_cmp_gt_i32 s[96:97], 3, v62\n"
    "v_mfma_f32_16x16x32_bf16 v[64:67], a[88:91], a[216:219], v[64:67]\n"
    "ds_read_b128 a[136:139], v2 offset:18432\n"
    "buffer_load_dwordx4 v4, s[48:51], s81 offen offset:3072 sc1 lds\n"
    "v_mov_b32 v49, s17\n"
    "v_mfma_f32_16x16x32_bf16 v[64:67], a[92:95], a[220:223], v[64:67]\n"
    "ds_read_b128 a[140:143], v2 offset:19456\n"
    "v_mfma_f32_16x16x32_bf16 v[68:71], a[64:67], a[224:227], 0\n"
    "ds_read_b128 a[144:147], v2 offset:20480\n"
    "s_add_u32 m0, s61, 69632\n"
    "s_add_u32 s81, s81, 32768\n"
    "v_mfma_f32_16x16x32_bf16 v[68:71], a[68:71], a[228:231], v[68:71]\n"
    "ds_read_b128 a[148:151], v2 offset:21504\n"
    "v_mfma_f32_16x16x32_bf16 v[68:71], a[72:75], a[232:235], v[68:71]\n"
    "ds_read_b128 a[152:155], v2 offset:22528\n"
    "buffer_load_dwordx4 v4, s[48:51], s86 offen offset:0 sc1 lds\n"
    "v_mfma_f32_16x16x32_bf16 v[68:71], a[76:79], a[236:239], v[68:71]\n"
    "ds_read_b128 a[156:159], v2 offset:23552\n"
    "v_accvgpr_read_b32 v50, a11\n"
    "v_mfma_f32_16x16x32_bf16 v[68:71], a[80:83], a[240:243], v[68:71]\n"
    "ds_read_b128 a[160:163], v2 offset:24576\n"
    "v_mfma_f32_16x16x32_bf16 v[68:71], a[84:87], a[244:247], v[68:71]\n"
    "ds_read_b128 a[164:167], v2 offset:25600\n"
    "v_cndmask_b32 v64, v64, v63, s[90:91]\n"
    "v_cndmask_b32 v65, v65, v63, s[92:93]\n"
    "v_cndmask_b32 v66, v66, v63, s[94:95]\n"
    "v_mfma_f32_16x16x32_bf16 v[68:71], a[88:91], a[248:251], v[68:71]\n"
    "ds_read_b128 a[168:171], v2 offset:26624\n"
    "v_cndmask_b32 v67, v67, v63, s[96:97]\n"
    "v_max3_f32 v60, v64, v65, v66\n"
    "v_max_f32 v60, v60, v67\n"
    "v_mfma_f32_16x16x32_bf16 v[68:71], a[92:95], a[252:255], v[68:71]\n"
    "ds_read_b128 a[172:175], v2 offset:27648\n"
    "buffer_load_dwordx4 v4, s[48:51], s86 offen offset:1024 sc1 lds\n"
    "s_waitcnt lgkmcnt(15)\n"
    "v_mfma_f32_16x16x32_bf16 v[72:75], a[96:99], a[192:195], 0\n"
    "ds_read_b128 a[176:179], v2 offset:28672\n"
    "buffer_load_dwordx4 v4, s[48:51], s86 offen offset:2048 sc1 lds\n"
    "v_mfma_f32_16x16x32_bf16 v[72:75], a[100:103], a[196:199], v[72:75]\n"
    "ds_read_b128 a[180:183], v2 offset:29696\n"
    "buffer_load_dwordx4 v4, s[48:51], s86 offen offset:3072 sc1 lds\n"
    "v_mfma_f32_16x16x32_bf16 v[72:75], a[104:107], a[200:203], v[72:75]\n"
    "ds_read_b128 a[184:187], v2 offset:30720\n"
    "v_mfma_f32_16x16x32_bf16 v[72:75], a[108:111], a[204:207], v[72:75]\n"
    "ds_read_b128 a[188:191], v2 offset:31744\n"
    "v_mfma_f32_16x16x32_bf16 v[72:75], a[112:115], a[208:211], v[72:75]\n"
    "s_waitcnt lgkmcnt(15)\n"
    "v_mfma_f32_16x16x32_bf16 v[72:75], a[116:119], a[212:215], v[72:75]\n"
    "v_max3_f32 v61, v68, v69, v70\n"
    "v_max_f32 v61, v61, v71\n"
    "v_mov_b32 v16, v60\n"
    "v_mov_b32 v17, v61\n"
    "v_mfma_f32_16x16x32_bf16 v[72:75], a[120:123], a[216:219], v[72:75]\n"
    "s_nop 0\n"
    "v_permlane32_swap_b32 v16, v60\n"
    "v_permlane32_swap_b32 v17, v61\n"
    "v_max_f32 v60, v60, v16\n"
    "v_mfma_f32_16x16x32_bf16 v[72:75], a[124:127], a[220:223], v[72:75]\n"
    "v_max_f32 v61, v61, v17\n"
    "v_mov_b32 v16, v60\n"
    "v_mov_b32 v17, v61\n"
    "s_nop 0\n"
    "v_mfma_f32_16x16x32_bf16 v[76:79], a[96:99], a[224:227], 0\n"
    "s_waitcnt vmcnt(8) lgkmcnt(0)\n"
    "s_barrier\n"
    "v_permlane16_swap_b32 v16, v60\n"
    "v_permlane16_swap_b32 v17, v61\n"
    "v_mfma_f32_16x16x32_bf16 v[76:79], a[100:103], a[228:231], v[76:79]\n"
    "buffer_store_dwordx2 v[48:49], v50, s[48:51], s84 offen sc1\n"
    "s_bitcmp1_b32 s40, s83\n"
    "s_cbranch_scc1 .Lk2_hook0\n"
    ".Lk2_hook0_ret:\n"
    "v_mfma_f32_16x16x32_bf16 v[76:79], a[104:107], a[232:235], v[76:79]\n"
    "s_cmp_eq_u32 s80, s82\n"
    "s_cselect_b32 s80, s58, s80\n"
    "v_max_f32 v60, v60, v16\n"
    "v_max_f32 v61, v61, v17\n"
    "v_mfma_f32_16x16x32_bf16 v[76:79], a[108:111], a[236:239], v[76:79]\n"
    "s_mov_b32 s84, 1073741824\n"
    "v_sub_f32 v24, 0, v60\n"
    "v_sub_f32 v28, 0, v61\n"
    "v_sub_f32 v25, 0, v60\n"
    "v_mfma_f32_16x16x32_bf16 v[76:79], a[112:115], a[240:243], v[76:79]\n"
    "s_add_u32 m0, s61, 0\n"
    "s_add_u32 s85, s80, 4096\n"
    "v_sub_f32 v29, 0, v61\n"
    "v_sub_f32 v26, 0, v60\n"
    "v_mfma_f32_16x16x32_bf16 v[76:79], a[116:119], a[244:247], v[76:79]\n"
    "buffer_load_dwordx4 v4, s[48:51], s80 offen offset:0 sc1 lds\n"
    "v_sub_f32 v30, 0, v61\n"
    "v_sub_f32 v27, 0, v60\n"
    "v_mfma_f32_16x16x32_bf16 v[76:79], a[120:123], a[248:251], v[76:79]\n"
    "v_sub_f32 v31, 0, v61\n"
    "v_mov_b32 v6, v60\n"
    "v_mov_b32 v7, v61\n"
    "v_mov_b64 v[8:9], 0\n"
    "v_mfma_f32_16x16x32_bf16 v[76:79], a[124:127], a[252:255], v[76:79]\n"
    "buffer_load_dwordx4 v4, s[48:51], s80 offen offset:1024 sc1 lds\n"
    "v_pk_add_f32 v[64:65], v[64:65], v[24:25]\n"
    "v_pk_add_f32 v[66:67], v[66:67], v[24:25]\n"
    "v_mfma_f32_16x16x32_bf16 v[80:83], a[128:131], a[192:195], 0\n"
    "buffer_load_dwordx4 v4, s[48:51], s80 offen offset:2048 sc1 lds\n"
    "v_pk_add_f32 v[68:69], v[68:69], v[28:29]\n"
    "v_pk_add_f32 v[70:71], v[70:71], v[28:29]\n"
    "v_mfma_f32_16x16x32_bf16 v[80:83], a[132:135], a[196:199], v[80:83]\n"
    "v_cmp_gt_i32 s[90:91], 16, v62\n"
    "v_cmp_gt_i32 s[92:93], 17, v62\n"
    "v_cmp_gt_i32 s[94:95], 18, v62\n"
    "v_cmp_gt_i32 s[96:97], 19, v62\n"
    "v_mfma_f32_16x16x32_bf16 v[80:83], a[136:139], a[200:203], v[80:83]\n"
    "buffer_load_dwordx4 v4, s[48:51], s80 offen offset:3072 sc1 lds\n"
    "v_cndmask_b32 v72, v72, v63, s[90:91]\n"
    "v_cndmask_b32 v73, v73, v63, s[92:93]\n"
    "v_mfma_f32_16x16x32_bf16 v[80:83], a[140:143], a[204:207], v[80:83]\n"
    "v_cndmask_b32 v74, v74, v63, s[94:95]\n"
    "v_cndmask_b32 v75, v75, v63, s[96:97]\n"
    "v_mfma_f32_16x16x32_bf16 v[80:83], a[144:147], a[208:211], v[80:83]\n"
    "s_add_u32 m0, s61, 4096\n"
    "s_add_u32 s80, s80, 32768\n"
    "v_pk_add_f32 v[72:73], v[72:73], v[24:25]\n"
    "v_pk_add_f32 v[74:75], v[74:75], v[24:25]\n"
    "v_mfma_f32_16x16x32_bf16 v[80:83], a[148:151], a[212:215], v[80:83]\n"
    "v_pk_add_f32 v[76:77], v[76:77], v[28:29]\n"
    "v_pk_add_f32 v[78:79], v[78:79], v[28:29]\n"
    "v_exp_f32 v16, v64\n"
    "v_mfma_f32_16x16x32_bf16 v[80:83], a[152:155], a[216:219], v[80:83]\n"
    "v_exp_f32 v17, v65\n"
    "v_exp_f32 v18, v66\n"
    "v_mfma_f32_16x16x32_bf16 v[80:83], a[156:159], a[220:223], v[80:83]\n"
    "buffer_load_dwordx4 v4, s[48:51], s85 offen offset:0 sc1 lds\n"
    "v_exp_f32 v19, v67\n"
    "v_mfma_f32_16x16x32_bf16 v[84:87], a[128:131], a[224:227], 0\n"
    "ds_read_b128 a[64:67], v2 offset:32768\n"
    "buffer_load_dwordx4 v4, s[48:51], s85 offen offset:1024 sc1 lds\n"
    "v_add_f32 v10, v16, v17\n"
    "v_mfma_f32_16x16x32_bf16 v[84:87], a[132:135], a[228:231], v[84:87]\n"
    "ds_read_b128 a[68:71], v2 offset:33792\n"
    "v_cvt_pk_bf16_f32 v32, v16, v17\n"
    "v_exp_f32 v20, v72\n"
    "v_mfma_f32_16x16x32_bf16 v[84:87], a[136:139], a[232:235], v[84:87]\n"
    "ds_read_b128 a[72:75], v2 offset:34816\n"
    "v_exp_f32 v21, v73\n"
    "v_mfma_f32_16x16x32_bf16 v[84:87], a[140:143], a[236:239], v[84:87]\n"
    "ds_read_b128 a[76:79], v2 offset:35840\n"
    "buffer_load_dwordx4 v4, s[48:51], s85 offen offset:2048 sc1 lds\n"
    "v_add_f32 v10, v10, v18\n"
    "v_mfma_f32_16x16x32_bf16 v[84:87], a[144:147], a[240:243], v[84:87]\n"
    "ds_read_b128 a[80:83], v2 offset:36864\n"
    "v_add_f32 v10, v10, v19\n"
    "v_cvt_pk_bf16_f32 v33, v18, v19\n"
    "v_mfma_f32_16x16x32_bf16 v[84:87], a[148:151], a[244:247], v[84:87]\n"
    "v_pk_add_f32 v[80:81], v[80:81], v[24:25]\n"
    "v_pk_add_f32 v[82:83], v[82:83], v[24:25]\n"
    "v_exp_f32 v22, v74\n"
    "v_mfma_f32_16x16x32_bf16 v[84:87], a[152:155], a[248:251], v[84:87]\n"
    "buffer_load_dwordx4 v4, s[48:51], s85 offen offset:3072 sc1 lds\n"
    "v_exp_f32 v23, v75\n"
    "v_mfma_f32_16x16x32_bf16 v[84:87], a[156:159], a[252:255], v[84:87]\n"
    "ds_read_b128 a[84:87], v2 offset:37888\n"
    "v_add_f32 v10, v10, v20\n"
    "v_add_f32 v10, v10, v21\n"
    "v_add_f32 v56, v56, v12\n"
    "v_mfma_f32_16x16x32_bf16 v[88:91], a[160:163], a[192:195], 0\n"
    "ds_read_b128 a[88:91], v2 offset:38912\n"
    "v_cvt_pk_bf16_f32 v34, v20, v21\n"
    "v_mfma_f32_16x16x32_bf16 v[88:91], a[164:167], a[196:199], v[88:91]\n"
    "v_exp_f32 v16, v68\n"
    "v_exp_f32 v17, v69\n"
    "v_mfma_f32_16x16x32_bf16 v[88:91], a[168:171], a[200:203], v[88:91]\n"
    "ds_read_b128 a[92:95], v2 offset:39936\n"
    "v_add_f32 v57, v57, v13\n"
    "v_add_f32 v10, v10, v22\n"
    "v_mfma_f32_16x16x32_bf16 v[88:91], a[172:175], a[204:207], v[88:91]\n"
    "v_add_f32 v10, v10, v23\n"
    "v_add_f32 v58, v58, v14\n"
    "v_mfma_f32_16x16x32_bf16 v[88:91], a[176:179], a[208:211], v[88:91]\n"
    "ds_read_b128 a[96:99], v2 offset:40960\n"
    "v_cvt_pk_bf16_f32 v35, v22, v23\n"
    "v_exp_f32 v18, v70\n"
    "v_mfma_f32_16x16x32_bf16 v[88:91], a[180:183], a[212:215], v[88:91]\n"
    "v_pk_add_f32 v[84:85], v[84:85], v[28:29]\n"
    "v_pk_add_f32 v[86:87], v[86:87], v[28:29]\n"
    "v_exp_f32 v19, v71\n"
    "v_mfma_f32_16x16x32_bf16 v[88:91], a[184:187], a[216:219], v[88:91]\n"
    "ds_read_b128 a[100:103], v2 offset:41984\n"
    "v_add_f32 v59, v59, v15\n"
    "v_add_f32 v11, v16, v17\n"
    "v_mfma_f32_16x16x32_bf16 v[88:91], a[188:191], a[220:223], v[88:91]\n"
    "buffer_load_dwordx4 v[12:15], v5, s[36:39], s88 offen sc1\n"
    "v_cvt_pk_bf16_f32 v36, v16, v17\n"
    "s_nop 0\n"
    "v_mfma_f32_16x16x32_bf16 v[92:95], a[160:163], a[224:227], 0\n"
    "ds_read_b128 a[104:107], v2 offset:43008\n"
    "v_add_f32 v11, v11, v18\n"
    "v_mfma_f32_16x16x32_bf16 v[92:95], a[164:167], a[228:231], v[92:95]\n"
    "v_add_f32 v11, v11, v19\n"
    "s_add_u32 s88, s88, s41\n"
    "v_mfma_f32_16x16x32_bf16 v[92:95], a[168:171], a[232:235], v[92:95]\n"
    "ds_read_b128 a[108:111], v2 offset:44032\n"
    "v_cvt_pk_bf16_f32 v37, v18, v19\n"
    "v_mfma_f32_16x16x32_bf16 v[92:95], a[172:175], a[236:239], v[92:95]\n"
    "v_mfma_f32_16x16x32_bf16 v[92:95], a[176:179], a[240:243], v[92:95]\n"
    "v_mfma_f32_16x16x32_bf16 v[92:95], a[180:183], a[244:247], v[92:95]\n"
    "v_pk_add_f32 v[88:89], v[88:89], v[24:25]\n"
    "v_pk_add_f32 v[90:91], v[90:91], v[24:25]\n"
    "v_mfma_f32_16x16x32_bf16 v[92:95], a[184:187], a[248:251], v[92:95]\n"
    "v_mfma_f32_16x16x32_bf16 v[92:95], a[188:191], a[252:255], v[92:95]\n"
    "s_bitcmp1_b32 s42, s83\n"
    "s_cbranch_scc1 .Lk2_mask0f\n"
    ".Lk2_mask0f_ret:\n"
    "s_sub_u32 s83, s83, 1\n"
    "s_nop 4\n"
    "v_pk_add_f32 v[92:93], v[92:93], v[28:29]\n"
    "v_pk_add_f32 v[94:95], v[94:95], v[28:29]\n"
    "s_waitcnt lgkmcnt(0)\n"
    "s_cmp_eq_u32 s83, s45\n"
    "s_cbranch_scc1 .Lk2_eg\n"
    ".p2align 5\n"
    "s_nop 0\n"
    "s_nop 0\n"
    "s_nop 0\n"
    ".Lk2_loop:\n"
    "s_waitcnt lgkmcnt(4)\n"
    "v_mfma_f32_16x16x32_bf16 v[96:99], a[64:67], a[192:195], v[24:27]\n"
    "v_exp_f32 v20, v76\n"
    "v_mfma_f32_16x16x32_bf16 v[96:99], a[68:71], a[196:199], v[96:99]\n"
    "ds_read_b128 a[112:115], v2 offset:45056\n"
    "v_add_f32 v11, v11, v20\n"
    "v_mfma_f32_16x16x32_bf16 v[96:99], a[72:75], a[200:203], v[96:99]\n"
    "v_exp_f32 v21, v77\n"
    "v_mfma_f32_16x16x32_bf16 v[96:99], a[76:79], a[204:207], v[96:99]\n"
    "ds_read_b128 a[116:119], v2 offset:46080\n"
    "v_add_f32 v11, v11, v21\n"
    "v_mfma_f32_16x16x32_bf16 v[96:99], a[80:83], a[208:211], v[96:99]\n"
    "v_exp_f32 v22, v78\n"
    "v_mfma_f32_16x16x32_bf16 v[96:99], a[84:87], a[212:215], v[96:99]\n"
    "ds_read_b128 a[120:123], v2 offset:47104\n"
    "v_cvt_pk_bf16_f32 v38, v20, v21\n"
    "v_mfma_f32_16x16x32_bf16 v[96:99], a[88:91], a[216:219], v[96:99]\n"
    "ds_read_b128 a[124:127], v2 offset:48128\n"
    "v_add_f32 v11, v11, v22\n"
    "v_mfma_f32_16x16x32_bf16 v[96:99], a[92:95], a[220:223], v[96:99]\n"
    "v_exp_f32 v23, v79\n"
    "v_mfma_f32_16x16x32_bf16 v[100:103], a[64:67], a[224:227], v[28:31]\n"
    "ds_read_b128 a[128:131], v2 offset:49152\n"
    "v_add_f32 v11, v11, v23\n"
    "v_mfma_f32_16x16x32_bf16 v[100:103], a[68:71], a[228:231], v[100:103]\n"
    "v_exp_f32 v16, v80\n"
    "v_mfma_f32_16x16x32_bf16 v[100:103], a[72:75], a[232:235], v[100:103]\n"
    "ds_read_b128 a[132:135], v2 offset:50176\n"
    "v_cvt_pk_bf16_f32 v39, v22, v23\n"
    "v_mfma_f32_16x16x32_bf16 v[100:103], a[76:79], a[236:239], v[100:103]\n"
    "ds_read_b128 a[136:139], v2 offset:51200\n"
    "v_add_f32 v10, v10, v16\n"
    "v_mfma_f32_16x16x32_bf16 v[100:103], a[80:83], a[240:243], v[100:103]\n"
    "v_exp_f32 v17, v81\n"
    "v_mfma_f32_16x16x32_bf16 v[100:103], a[84:87], a[244:247], v[100:103]\n"
    "v_exp_f32 v18, v82\n"
    "v_mfma_f32_16x16x32_bf16 v[100:103], a[88:91], a[248:251], v[100:103]\n"
    "ds_read_b128 a[140:143], v2 offset:52224\n"
    "v_add_f32 v10, v10, v17\n"
    "v_mfma_f32_16x16x32_bf16 v[100:103], a[92:95], a[252:255], v[100:103]\n"
    "v_cvt_pk_bf16_f32 v48, v16, v17\n"
    "s_waitcnt lgkmcnt(4)\n"
    "v_mfma_f32_16x16x32_bf16 v[104:107], a[96:99], a[192:195], v[24:27]\n"
    "ds_read_b128 a[144:147], v2 offset:53248\n"
    "v_add_f32 v10, v10, v18\n"
    "v_mfma_f32_16x16x32_bf16 v[104:107], a[100:103], a[196:199], v[104:107]\n"
    "v_exp_f32 v19, v83\n"
    "v_mfma_f32_16x16x32_bf16 v[104:107], a[104:107], a[200:203], v[104:107]\n"
    "ds_read_b128 a[148:151], v2 offset:54272\n"
    "v_add_f32 v10, v10, v19\n"
    "v_mfma_f32_16x16x32_bf16 v[104:107], a[108:111], a[204:207], v[104:107]\n"
    "v_exp_f32 v20, v88\n"
    "v_mfma_f32_16x16x32_bf16 v[104:107], a[112:115], a[208:211], v[104:107]\n"
    "ds_read_b128 a[152:155], v2 offset:55296\n"
    "v_cvt_pk_bf16_f32 v49, v18, v19\n"
    "v_mfma_f32_16x16x32_bf16 v[104:107], a[116:119], a[212:215], v[104:107]\n"
    "ds_read_b128 a[156:159], v2 offset:56320\n"
    "v_add_f32 v10, v10, v20\n"
    "v_mfma_f32_16x16x32_bf16 v[104:107], a[120:123], a[216:219], v[104:107]\n"
    "v_exp_f32 v21, v89\n"
    "v_mfma_f32_16x16x32_bf16 v[104:107], a[124:127], a[220:223], v[104:107]\n"
    "v_exp_f32 v22, v90\n"
    "v_mfma_f32_16x16x32_bf16 v[108:111], a[96:99], a[224:227], v[28:31]\n"
    "ds_read_b128 a[160:163], v2 offset:57344\n"
    "v_add_f32 v10, v10, v21\n"
    "v_mfma_f32_16x16x32_bf16 v[108:111], a[100:103], a[228:231], v[108:111]\n"
    "v_cvt_pk_bf16_f32 v50, v20, v21\n"
    "v_add_f32 v10, v10, v22\n"
    "v_mfma_f32_16x16x32_bf16 v[108:111], a[104:107], a[232:235], v[108:111]\n"
    "v_exp_f32 v23, v91\n"
    "v_mfma_f32_16x16x32_bf16 v[108:111], a[108:111], a[236:239], v[108:111]\n"
    "ds_read_b128 a[164:167], v2 offset:58368\n"
    "v_add_f32 v10, v10, v23\n"
    "v_mfma_f32_16x16x32_bf16 v[108:111], a[112:115], a[240:243], v[108:111]\n"
    "ds_read_b128 a[168:171], v2 offset:59392\n"
    "v_cvt_pk_bf16_f32 v51, v22, v23\n"
    "v_mfma_f32_16x16x32_bf16 v[108:111], a[116:119], a[244:247], v[108:111]\n"
    "v_exp_f32 v16, v84\n"
    "v_mfma_f32_16x16x32_bf16 v[108:111], a[120:123], a[248:251], v[108:111]\n"
    "ds_read_b128 a[172:175], v2 offset:60416\n"
    "v_add_f32 v11, v11, v16\n"
    "v_mfma_f32_16x16x32_bf16 v[108:111], a[124:127], a[252:255], v[108:111]\n"
    "ds_read_b128 a[176:179], v2 offset:61440\n"
    "s_waitcnt lgkmcnt(5)\n"
    "v_mfma_f32_16x16x32_bf16 v[112:115], a[128:131], a[192:195], v[24:27]\n"
    "v_exp_f32 v17, v85\n"
    "v_mfma_f32_16x16x32_bf16 v[112:115], a[132:135], a[196:199], v[112:115]\n"
    "ds_read_b128 a[180:183], v2 offset:62464\n"
    "v_add_f32 v11, v11, v17\n"
    "v_mfma_f32_16x16x32_bf16 v[112:115], a[136:139], a[200:203], v[112:115]\n"
    "v_exp_f32 v18, v86\n"
    "v_mfma_f32_16x16x32_bf16 v[112:115], a[140:143], a[204:207], v[112:115]\n"
    "v_cvt_pk_bf16_f32 v52, v16, v17\n"
    "v_add_f32 v11, v11, v18\n"
    "v_mfma_f32_16x16x32_bf16 v[112:115], a[144:147], a[208:211], v[112:115]\n"
    "v_exp_f32 v19, v87\n"
    "v_mfma_f32_16x16x32_bf16 v[112:115], a[148:151], a[212:215], v[112:115]\n"
    "ds_read_b128 a[184:187], v2 offset:63488\n"
    "v_add_f32 v11, v11, v19\n"
    "v_mfma_f32_16x16x32_bf16 v[112:115], a[152:155], a[216:219], v[112:115]\n"
    "ds_read_b128 a[188:191], v2 offset:64512\n"
    "v_cvt_pk_bf16_f32 v53, v18, v19\n"
    "v_mfma_f32_16x16x32_bf16 v[112:115], a[156:159], a[220:223], v[112:115]\n"
    "v_exp_f32 v20, v92\n"
    "v_mfma_f32_16x16x32_bf16 v[116:119], a[128:131], a[224:227], v[28:31]\n"
    "v_add_f32 v11, v11, v20\n"
    "v_mfma_f32_16x16x32_bf16 v[116:119], a[132:135], a[228:231], v[116:119]\n"
    "v_exp_f32 v21, v93\n"
    "v_mfma_f32_16x16x32_bf16 v[116:119], a[136:139], a[232:235], v[116:119]\n"
    "v_add_f32 v11, v11, v21\n"
    "v_cvt_pk_bf16_f32 v54, v20, v21\n"
    "v_mfma_f32_16x16x32_bf16 v[116:119], a[140:143], a[236:239], v[116:119]\n"
    "v_exp_f32 v22, v94\n"
    "v_mfma_f32_16x16x32_bf16 v[116:119], a[144:147], a[240:243], v[116:119]\n"
    "s_waitcnt vmcnt(0) lgkmcnt(0)\n"
    "s_barrier\n"
    "v_mfma_f32_16x16x32_bf16 v[116:119], a[148:151], a[244:247], v[116:119]\n"
    "ds_read_b128 a[64:67], v3 offset:0\n"
    "v_add_f32 v11, v11, v22\n"
    "v_mfma_f32_16x16x32_bf16 v[116:119], a[152:155], a[248:251], v[116:119]\n"
    "v_exp_f32 v23, v95\n"
    "v_mfma_f32_16x16x32_bf16 v[116:119], a[156:159], a[252:255], v[116:119]\n"
    "ds_read_b128 a[68:71], v3 offset:16384\n"
    "v_mfma_f32_16x16x32_bf16 v[120:123], a[160:163], a[192:195], v[24:27]\n"
    "s_add_u32 m0, s61, 98304\n"
    "s_add_u32 s86, s81, 4096\n"
    "v_mfma_f32_16x16x32_bf16 v[120:123], a[164:167], a[196:199], v[120:123]\n"
    "ds_read_b128 a[72:75], v3 offset:1024\n"
    "v_add_f32 v11, v11, v23\n"
    "v_mfma_f32_16x16x32_bf16 v[120:123], a[168:171], a[200:203], v[120:123]\n"
    "buffer_load_dwordx4 v4, s[48:51], s81 offen offset:0 sc1 lds\n"
    "v_mfma_f32_16x16x32_bf16 v[120:123], a[172:175], a[204:207], v[120:123]\n"
    "ds_read_b128 a[76:79], v3 offset:17408\n"
    "v_cvt_pk_bf16_f32 v55, v22, v23\n"
    "v_mfma_f32_16x16x32_bf16 v[120:123], a[176:179], a[208:211], v[120:123]\n"
    "ds_read_b128 a[80:83], v3 offset:2048\n"
    "v_max_f32 v60, v10, v11\n"
    "v_mfma_f32_16x16x32_bf16 v[120:123], a[180:183], a[212:215], v[120:123]\n"
    "buffer_load_dwordx4 v4, s[48:51], s81 offen offset:1024 sc1 lds\n"
    "v_mfma_f32_16x16x32_bf16 v[120:123], a[184:187], a[216:219], v[120:123]\n"
    "ds_read_b128 a[84:87], v3 offset:18432\n"
    "v_cmp_nge_f32 vcc, s89, v60\n"
    "v_mfma_f32_16x16x32_bf16 v[120:123], a[188:191], a[220:223], v[120:123]\n"
    "ds_read_b128 a[88:91], v3 offset:3072\n"
    "v_mfma_f32_16x16x32_bf16 v[124:127], a[160:163], a[224:227], v[28:31]\n"
    "ds_read_b128 a[92:95], v3 offset:19456\n"
    "v_mfma_f32_16x16x32_bf16 v[124:127], a[164:167], a[228:231], v[124:127]\n"
    "buffer_load_dwordx4 v4, s[48:51], s81 offen offset:2048 sc1 lds\n"
    "v_mfma_f32_16x16x32_bf16 v[124:127], a[168:171], a[232:235], v[124:127]\n"
    "ds_read_b128 a[96:99], v3 offset:4096\n"
    "s_cbranch_vccnz .Lk2_rare0\n"
    ".Lk2_rare0_ret:\n"
    "v_mfma_f32_16x16x32_bf16 v[124:127], a[172:175], a[236:239], v[124:127]\n"
    "ds_read_b128 a[100:103], v3 offset:20480\n"
    "v_add_f32 v8, v8, v10\n"
    "v_mfma_f32_16x16x32_bf16 v[124:127], a[176:179], a[240:243], v[124:127]\n"
    "buffer_load_dwordx4 v4, s[48:51], s81 offen offset:3072 sc1 lds\n"
    "v_mfma_f32_16x16x32_bf16 v[124:127], a[180:183], a[244:247], v[124:127]\n"
    "ds_read_b128 a[104:107], v3 offset:5120\n"
    "v_add_f32 v9, v9, v11\n"
    "v_mfma_f32_16x16x32_bf16 v[124:127], a[184:187], a[248:251], v[124:127]\n"
    "ds_read_b128 a[108:111], v3 offset:21504\n"
    "v_mfma_f32_16x16x32_bf16 v[124:127], a[188:191], a[252:255], v[124:127]\n"
    "s_waitcnt lgkmcnt(4)\n"
    "v_mfma_f32_16x16x32_bf16 v[128:131], a[64:67], v[32:35], v[128:131]\n"
    "s_add_u32 m0, s61, 102400\n"
    "s_add_u32 s81, s81, 32768\n"
    "v_mfma_f32_16x16x32_bf16 v[128:131], a[68:71], v[48:51], v[128:131]\n"
    "ds_read_b128 a[112:115], v3 offset:6144\n"
    "v_mfma_f32_16x16x32_bf16 v[192:195], a[68:71], v[52:55], v[192:195]\n"
    "s_bitcmp1_b32 s40, s83\n"
    "s_cbranch_scc1 .Lk2_mask1\n"
    ".Lk2_mask1_ret:\n"
    "v_mfma_f32_16x16x32_bf16 v[192:195], a[64:67], v[36:39], v[192:195]\n"
    "buffer_load_dwordx4 v4, s[48:51], s86 offen offset:0 sc1 lds\n"
    "v_mfma_f32_16x16x32_bf16 v[196:199], a[72:75], v[36:39], v[196:199]\n"
    "ds_read_b128 a[116:119], v3 offset:22528\n"
    "v_mfma_f32_16x16x32_bf16 v[196:199], a[76:79], v[52:55], v[196:199]\n"
    "v_exp_f32 v16, v96\n"
    "v_mfma_f32_16x16x32_bf16 v[132:135], a[76:79], v[48:51], v[132:135]\n"
    "ds_read_b128 a[120:123], v3 offset:7168\n"
    "v_mfma_f32_16x16x32_bf16 v[132:135], a[72:75], v[32:35], v[132:135]\n"
    "buffer_load_dwordx4 v4, s[48:51], s86 offen offset:1024 sc1 lds\n"
    "v_mfma_f32_16x16x32_bf16 v[136:139], a[80:83], v[32:35], v[136:139]\n"
    "ds_read_b128 a[124:127], v3 offset:23552\n"
    "v_mfma_f32_16x16x32_bf16 v[136:139], a[84:87], v[48:51], v[136:139]\n"
    "buffer_load_dwordx4 v4, s[48:51], s86 offen offset:2048 sc1 lds\n"
    "v_mfma_f32_16x16x32_bf16 v[200:203], a[84:87], v[52:55], v[200:203]\n"
    "v_exp_f32 v17, v97\n"
    "v_mfma_f32_16x16x32_bf16 v[200:203], a[80:83], v[36:39], v[200:203]\n"
    "ds_read_b128 a[128:131], v3 offset:8192\n"
    "v_add_f32 v10, v16, v17\n"
    "v_mfma_f32_16x16x32_bf16 v[204:207], a[88:91], v[36:39], v[204:207]\n"
    "buffer_load_dwordx4 v4, s[48:51], s86 offen offset:3072 sc1 lds\n"
    "v_mfma_f32_16x16x32_bf16 v[204:207], a[92:95], v[52:55], v[204:207]\n"
    "ds_read_b128 a[132:135], v3 offset:24576\n"
    "v_cvt_pk_bf16_f32 v40, v16, v17\n"
    "v_mfma_f32_16x16x32_bf16 v[140:143], a[92:95], v[48:51], v[140:143]\n"
    "s_cmp_eq_u32 s80, s82\n"
    "s_cselect_b32 s80, s58, s80\n"
    "v_mfma_f32_16x16x32_bf16 v[140:143], a[88:91], v[32:35], v[140:143]\n"
    "ds_read_b128 a[136:139], v3 offset:9216\n"
    "s_waitcnt lgkmcnt(3)\n"
    "v_mfma_f32_16x16x32_bf16 v[144:147], a[96:99], v[32:35], v[144:147]\n"
    "v_exp_f32 v18, v98\n"
    "v_mfma_f32_16x16x32_bf16 v[144:147], a[100:103], v[48:51], v[144:147]\n"
    "ds_read_b128 a[140:143], v3 offset:25600\n"
    "v_add_f32 v10, v10, v18\n"
    "v_mfma_f32_16x16x32_bf16 v[208:211], a[100:103], v[52:55], v[208:211]\n"
    "s_add_u32 m0, s61, 32768\n"
    "s_add_u32 s85, s80, 4096\n"
    "v_mfma_f32_16x16x32_bf16 v[208:211], a[96:99], v[36:39], v[208:211]\n"
    "ds_read_b128 a[144:147], v3 offset:10240\n"
    "v_mfma_f32_16x16x32_bf16 v[212:215], a[104:107], v[36:39], v[212:215]\n"
    "v_exp_f32 v19, v99\n"
    "v_mfma_f32_16x16x32_bf16 v[212:215], a[108:111], v[52:55], v[212:215]\n"
    "buffer_load_dwordx4 v4, s[48:51], s80 offen offset:0 sc1 lds\n"
    "v_mfma_f32_16x16x32_bf16 v[148:151], a[108:111], v[48:51], v[148:151]\n"
    "ds_read_b128 a[148:151], v3 offset:26624\n"
    "v_add_f32 v10, v10, v19\n"
    "v_mfma_f32_16x16x32_bf16 v[148:151], a[104:107], v[32:35], v[148:151]\n"
    "buffer_load_dwordx4 v4, s[48:51], s80 offen offset:1024 sc1 lds\n"
    "v_mfma_f32_16x16x32_bf16 v[152:155], a[112:115], v[32:35], v[152:155]\n"
    "ds_read_b128 a[152:155], v3 offset:11264\n"
    "v_cvt_pk_bf16_f32 v41, v18, v19\n"
    "v_mfma_f32_16x16x32_bf16 v[152:155], a[116:119], v[48:51], v[152:155]\n"
    "v_exp_f32 v20, v104\n"
    "v_mfma_f32_16x16x32_bf16 v[216:219], a[116:119], v[52:55], v[216:219]\n"
    "buffer_load_dwordx4 v4, s[48:51], s80 offen offset:2048 sc1 lds\n"
    "v_mfma_f32_16x16x32_bf16 v[216:219], a[112:115], v[36:39], v[216:219]\n"
    "ds_read_b128 a[156:159], v3 offset:27648\n"
    "v_add_f32 v10, v10, v20\n"
    "v_mfma_f32_16x16x32_bf16 v[220:223], a[120:123], v[36:39], v[220:223]\n"
    "ds_read_b128 a[160:163], v3 offset:12288\n"
    "v_mfma_f32_16x16x32_bf16 v[220:223], a[124:127], v[52:55], v[220:223]\n"
    "buffer_load_dwordx4 v4, s[48:51], s80 offen offset:3072 sc1 lds\n"
    "v_mfma_f32_16x16x32_bf16 v[156:159], a[124:127], v[48:51], v[156:159]\n"
    "v_exp_f32 v21, v105\n"
    "v_mfma_f32_16x16x32_bf16 v[156:159], a[120:123], v[32:35], v[156:159]\n"
    "ds_read_b128 a[164:167], v3 offset:28672\n"
    "s_waitcnt lgkmcnt(2)\n"
    "v_mfma_f32_16x16x32_bf16 v[160:163], a[128:131], v[32:35], v[160:163]\n"
    "s_add_u32 m0, s61, 36864\n"
    "s_add_u32 s80, s80, 32768\n"
    "v_mfma_f32_16x16x32_bf16 v[160:163], a[132:135], v[48:51], v[160:163]\n"
    "ds_read_b128 a[168:171], v3 offset:13312\n"
    "v_add_f32 v10, v10, v21\n"
    "v_mfma_f32_16x16x32_bf16 v[224:227], a[132:135], v[52:55], v[224:227]\n"
    "ds_read_b128 a[172:175], v3 offset:29696\n"
    "v_cvt_pk_bf16_f32 v42, v20, v21\n"
    "v_mfma_f32_16x16x32_bf16 v[224:227], a[128:131], v[36:39], v[224:227]\n"
    "buffer_load_dwordx4 v4, s[48:51], s85 offen offset:0 sc1 lds\n"
    "v_mfma_f32_16x16x32_bf16 v[228:231], a[136:139], v[36:39], v[228:231]\n"
    "v_exp_f32 v22, v106\n"
    "v_mfma_f32_16x16x32_bf16 v[228:231], a[140:143], v[52:55], v[228:231]\n"
    "ds_read_b128 a[176:179], v3 offset:14336\n"
    "v_add_f32 v10, v10, v22\n"
    "v_mfma_f32_16x16x32_bf16 v[164:167], a[140:143], v[48:51], v[164:167]\n"
    "buffer_load_dwordx4 v4, s[48:51], s85 offen offset:1024 sc1 lds\n"
    "v_mfma_f32_16x16x32_bf16 v[164:167], a[136:139], v[32:35], v[164:167]\n"
    "ds_read_b128 a[180:183], v3 offset:30720\n"
    "s_sub_u32 s83, s83, 1\n"
    "v_mfma_f32_16x16x32_bf16 v[168:171], a[144:147], v[32:35], v[168:171]\n"
    "buffer_load_dwordx4 v4, s[48:51], s85 offen offset:2048 sc1 lds\n"
    "v_mfma_f32_16x16x32_bf16 v[168:171], a[148:151], v[48:51], v[168:171]\n"
    "v_exp_f32 v23, v107\n"
    "v_mfma_f32_16x16x32_bf16 v[232:235], a[148:151], v[52:55], v[232:235]\n"
    "ds_read_b128 a[184:187], v3 offset:15360\n"
    "v_add_f32 v10, v10, v23\n"
    "v_mfma_f32_16x16x32_bf16 v[232:235], a[144:147], v[36:39], v[232:235]\n"
    "buffer_load_dwordx4 v4, s[48:51], s85 offen offset:3072 sc1 lds\n"
    "v_mfma_f32_16x16x32_bf16 v[236:239], a[152:155], v[36:39], v[236:239]\n"
    "ds_read_b128 a[188:191], v3 offset:31744\n"
    "v_cvt_pk_bf16_f32 v43, v22, v23\n"
    "v_mfma_f32_16x16x32_bf16 v[236:239], a[156:159], v[52:55], v[236:239]\n"
    "v_exp_f32 v16, v100\n"
    "v_mfma_f32_16x16x32_bf16 v[172:175], a[156:159], v[48:51], v[172:175]\n"
    "ds_read_b128 a[64:67], v2 offset:0\n"
    "v_add_f32 v56, v56, v12\n"
    "v_mfma_f32_16x16x32_bf16 v[172:175], a[152:155], v[32:35], v[172:175]\n"
    "ds_read_b128 a[68:71], v2 offset:1024\n"
    "s_waitcnt lgkmcnt(2)\n"
    "v_mfma_f32_16x16x32_bf16 v[176:179], a[160:163], v[32:35], v[176:179]\n"
    "ds_read_b128 a[72:75], v2 offset:2048\n"
    "v_add_f32 v57, v57, v13\n"
    "v_mfma_f32_16x16x32_bf16 v[176:179], a[164:167], v[48:51], v[176:179]\n"
    "v_exp_f32 v17, v101\n"
    "v_mfma_f32_16x16x32_bf16 v[240:243], a[164:167], v[52:55], v[240:243]\n"
    "ds_read_b128 a[76:79], v2 offset:3072\n"
    "v_add_f32 v58, v58, v14\n"
    "v_mfma_f32_16x16x32_bf16 v[240:243], a[160:163], v[36:39], v[240:243]\n"
    "ds_read_b128 a[80:83], v2 offset:4096\n"
    "v_add_f32 v11, v16, v17\n"
    "v_mfma_f32_16x16x32_bf16 v[244:247], a[168:171], v[36:39], v[244:247]\n"
    "v_cvt_pk_bf16_f32 v44, v16, v17\n"
    "v_add_f32 v59, v59, v15\n"
    "v_mfma_f32_16x16x32_bf16 v[244:247], a[172:175], v[52:55], v[244:247]\n"
    "v_exp_f32 v18, v102\n"
    "v_mfma_f32_16x16x32_bf16 v[180:183], a[172:175], v[48:51], v[180:183]\n"
    "ds_read_b128 a[84:87], v2 offset:5120\n"
    "v_add_f32 v11, v11, v18\n"
    "v_mfma_f32_16x16x32_bf16 v[180:183], a[168:171], v[32:35], v[180:183]\n"
    "buffer_load_dwordx4 v[12:15], v5, s[36:39], s88 offen sc1\n"
    "v_mfma_f32_16x16x32_bf16 v[184:187], a[176:179], v[32:35], v[184:187]\n"
    "ds_read_b128 a[88:91], v2 offset:6144\n"
    "s_add_u32 s88, s88, s41\n"
    "v_mfma_f32_16x16x32_bf16 v[184:187], a[180:183], v[48:51], v[184:187]\n"
    "ds_read_b128 a[92:95], v2 offset:7168\n"
    "v_mfma_f32_16x16x32_bf16 v[248:251], a[180:183], v[52:55], v[248:251]\n"
    "v_exp_f32 v19, v103\n"
    "v_mfma_f32_16x16x32_bf16 v[248:251], a[176:179], v[36:39], v[248:251]\n"
    "ds_read_b128 a[96:99], v2 offset:8192\n"
    "v_add_f32 v11, v11, v19\n"
    "v_mfma_f32_16x16x32_bf16 v[252:255], a[184:187], v[36:39], v[252:255]\n"
    "ds_read_b128 a[100:103], v2 offset:9216\n"
    "v_cvt_pk_bf16_f32 v45, v18, v19\n"
    "v_mfma_f32_16x16x32_bf16 v[252:255], a[188:191], v[52:55], v[252:255]\n"
    "ds_read_b128 a[104:107], v2 offset:10240\n"
    "v_mfma_f32_16x16x32_bf16 v[188:191], a[188:191], v[48:51], v[188:191]\n"
    "ds_read_b128 a[108:111], v2 offset:11264\n"
    "v_mfma_f32_16x16x32_bf16 v[188:191], a[184:187], v[32:35], v[188:191]\n"
    "s_cmp_eq_u32 s83, 0\n"
    "s_cbranch_scc1 .Lk2_drain\n"
    ".Lk2_loop2:\n"
    "s_waitcnt lgkmcnt(4)\n"
    "v_mfma_f32_16x16x32_bf16 v[64:67], a[64:67], a[192:195], v[24:27]\n"
    "v_exp_f32 v20, v108\n"
    "v_mfma_f32_16x16x32_bf16 v[64:67], a[68:71], a[196:199], v[64:67]\n"
    "ds_read_b128 a[112:115], v2 offset:12288\n"
    "v_add_f32 v11, v11, v20\n"
    "v_mfma_f32_16x16x32_bf16 v[64:67], a[72:75], a[200:203], v[64:67]\n"
    "v_exp_f32 v21, v109\n"
    "v_mfma_f32_16x16x32_bf16 v[64:67], a[76:79], a[204:207], v[64:67]\n"
    "ds_read_b128 a[116:119], v2 offset:13312\n"
    "v_add_f32 v11, v11, v21\n"
    "v_mfma_f32_16x16x32_bf16 v[64:67], a[80:83], a[208:211], v[64:67]\n"
    "v_exp_f32 v22, v110\n"
    "v_mfma_f32_16x16x32_bf16 v[64:67], a[84:87], a[212:215], v[64:67]\n"
    "ds_read_b128 a[120:123], v2 offset:14336\n"
    "v_cvt_pk_bf16_f32 v46, v20, v21\n"
    "v_mfma_f32_16x16x32_bf16 v[64:67], a[88:91], a[216:219], v[64:67]\n"
    "ds_read_b128 a[124:127], v2 offset:15360\n"
    "v_add_f32 v11, v11, v22\n"
    "v_mfma_f32_16x16x32_bf16 v[64:67], a[92:95], a[220:223], v[64:67]\n"
    "v_exp_f32 v23, v111\n"
    "v_mfma_f32_16x16x32_bf16 v[68:71], a[64:67], a[224:227], v[28:31]\n"
    "ds_read_b128 a[128:131], v2 offset:16384\n"
    "v_add_f32 v11, v11, v23\n"
    "v_mfma_f32_16x16x32_bf16 v[68:71], a[68:71], a[228:231], v[68:71]\n"
    "v_exp_f32 v16, v112\n"
    "v_mfma_f32_16x16x32_bf16 v[68:71], a[72:75], a[232:235], v[68:71]\n"
    "ds_read_b128 a[132:135], v2 offset:17408\n"
    "v_cvt_pk_bf16_f32 v47, v22, v23\n"
    "v_mfma_f32_16x16x32_bf16 v[68:71], a[76:79], a[236:239], v[68:71]\n"
    "ds_read_b128 a[136:139], v2 offset:18432\n"
    "v_add_f32 v10, v10, v16\n"
    "v_mfma_f32_16x16x32_bf16 v[68:71], a[80:83], a[240:243], v[68:71]\n"
    "v_exp_f32 v17, v113\n"
    "v_mfma_f32_16x16x32_bf16 v[68:71], a[84:87], a[244:247], v[68:71]\n"
    "v_exp_f32 v18, v114\n"
    "v_mfma_f32_16x16x32_bf16 v[68:71], a[88:91], a[248:251], v[68:71]\n"
    "ds_read_b128 a[140:143], v2 offset:19456\n"
    "v_add_f32 v10, v10, v17\n"
    "v_mfma_f32_16x16x32_bf16 v[68:71], a[92:95], a[252:255], v[68:71]\n"
    "v_cvt_pk_bf16_f32 v48, v16, v17\n"
    "s_waitcnt lgkmcnt(4)\n"
    "v_mfma_f32_16x16x32_bf16 v[72:75], a[96:99], a[192:195], v[24:27]\n"
    "ds_read_b128 a[144:147], v2 offset:20480\n"
    "v_add_f32 v10, v10, v18\n"
    "v_mfma_f32_16x16x32_bf16 v[72:75], a[100:103], a[196:199], v[72:75]\n"
    "v_exp_f32 v19, v115\n"
    "v_mfma_f32_16x16x32_bf16 v[72:75], a[104:107], a[200:203], v[72:75]\n"
    "ds_read_b128 a[148:151], v2 offset:21504\n"
    "v_add_f32 v10, v10, v19\n"
    "v_mfma_f32_16x16x32_bf16 v[72:75], a[108:111], a[204:207], v[72:75]\n"
    "v_exp_f32 v20, v120\n"
    "v_mfma_f32_16x16x32_bf16 v[72:75], a[112:115], a[208:211], v[72:75]\n"
    "ds_read_b128 a[152:155], v2 offset:22528\n"
    "v_cvt_pk_bf16_f32 v49, v18, v19\n"
    "v_mfma_f32_16x16x32_bf16 v[72:75], a[116:119], a[212:215], v[72:75]\n"
    "ds_read_b128 a[156:159], v2 offset:23552\n"
    "v_add_f32 v10, v10, v20\n"
    "v_mfma_f32_16x16x32_bf16 v[72:75], a[120:123], a[216:219], v[72:75]\n"
    "v_exp_f32 v21, v121\n"
    "v_mfma_f32_16x16x32_bf16 v[72:75], a[124:127], a[220:223], v[72:75]\n"
    "v_exp_f32 v22, v122\n"
    "v_mfma_f32_16x16x32_bf16 v[76:79], a[96:99], a[224:227], v[28:31]\n"
    "ds_read_b128 a[160:163], v2 offset:24576\n"
    "v_add_f32 v10, v10, v21\n"
    "v_mfma_f32_16x16x32_bf16 v[76:79], a[100:103], a[228:231], v[76:79]\n"
    "v_cvt_pk_bf16_f32 v50, v20, v21\n"
    "v_add_f32 v10, v10, v22\n"
    "v_mfma_f32_16x16x32_bf16 v[76:79], a[104:107], a[232:235], v[76:79]\n"
    "v_exp_f32 v23, v123\n"
    "v_mfma_f32_16x16x32_bf16 v[76:79], a[108:111], a[236:239], v[76:79]\n"
    "ds_read_b128 a[164:167], v2 offset:25600\n"
    "v_add_f32 v10, v10, v23\n"
    "v_mfma_f32_16x16x32_bf16 v[76:79], a[112:115], a[240:243], v[76:79]\n"
    "ds_read_b128 a[168:171], v2 offset:26624\n"
    "v_cvt_pk_bf16_f32 v51, v22, v23\n"
    "v_mfma_f32_16x16x32_bf16 v[76:79], a[116:119], a[244:247], v[76:79]\n"
    "v_exp_f32 v16, v116\n"
    "v_mfma_f32_16x16x32_bf16 v[76:79], a[120:123], a[248:251], v[76:79]\n"
    "ds_read_b128 a[172:175], v2 offset:27648\n"
    "v_add_f32 v11, v11, v16\n"
    "v_mfma_f32_16x16x32_bf16 v[76:79], a[124:127], a[252:255], v[76:79]\n"
    "ds_read_b128 a[176:179], v2 offset:28672\n"
    "s_waitcnt lgkmcnt(5)\n"
    "v_mfma_f32_16x16x32_bf16 v[80:83], a[128:131], a[192:195], v[24:27]\n"
    "v_exp_f32 v17, v117\n"
    "v_mfma_f32_16x16x32_bf16 v[80:83], a[132:135], a[196:199], v[80:83]\n"
    "ds_read_b128 a[180:183], v2 offset:29696\n"
    "v_add_f32 v11, v11, v17\n"
    "v_mfma_f32_16x16x32_bf16 v[80:83], a[136:139], a[200:203], v[80:83]\n"
    "v_exp_f32 v18, v118\n"
    "v_mfma_f32_16x16x32_bf16 v[80:83], a[140:143], a[204:207], v[80:83]\n"
    "v_cvt_pk_bf16_f32 v52, v16, v17\n"
    "v_add_f32 v11, v11, v18\n"
    "v_mfma_f32_16x16x32_bf16 v[80:83], a[144:147], a[208:211], v[80:83]\n"
    "v_exp_f32 v19, v119\n"
    "v_mfma_f32_16x16x32_bf16 v[80:83], a[148:151], a[212:215], v[80:83]\n"
    "ds_read_b128 a[184:187], v2 offset:30720\n"
    "v_add_f32 v11, v11, v19\n"
    "v_mfma_f32_16x16x32_bf16 v[80:83], a[152:155], a[216:219], v[80:83]\n"
    "ds_read_b128 a[188:191], v2 offset:31744\n"
    "v_cvt_pk_bf16_f32 v53, v18, v19\n"
    "v_mfma_f32_16x16x32_bf16 v[80:83], a[156:159], a[220:223], v[80:83]\n"
    "v_exp_f32 v20, v124\n"
    "v_mfma_f32_16x16x32_bf16 v[84:87], a[128:131], a[224:227], v[28:31]\n"
    "v_add_f32 v11, v11, v20\n"
    "v_mfma_f32_16x16x32_bf16 v[84:87], a[132:135], a[228:231], v[84:87]\n"
    "v_exp_f32 v21, v125\n"
    "v_mfma_f32_16x16x32_bf16 v[84:87], a[136:139], a[232:235], v[84:87]\n"
    "v_add_f32 v11, v11, v21\n"
    "v_cvt_pk_bf16_f32 v54, v20, v21\n"
    "v_mfma_f32_16x16x32_bf16 v[84:87], a[140:143], a[236:239], v[84:87]\n"
    "v_exp_f32 v22, v126\n"
    "v_mfma_f32_16x16x32_bf16 v[84:87], a[144:147], a[240:243], v[84:87]\n"
    "s_waitcnt vmcnt(0) lgkmcnt(0)\n"
    "s_barrier\n"
    "v_mfma_f32_16x16x32_bf16 v[84:87], a[148:151], a[244:247], v[84:87]\n"
    "ds_read_b128 a[64:67], v3 offset:32768\n"
    "v_add_f32 v11, v11, v22\n"
    "v_mfma_f32_16x16x32_bf16 v[84:87], a[152:155], a[248:251], v[84:87]\n"
    "v_exp_f32 v23, v127\n"
    "v_mfma_f32_16x16x32_bf16 v[84:87], a[156:159], a[252:255], v[84:87]\n"
    "ds_read_b128 a[68:71], v3 offset:49152\n"
    "v_mfma_f32_16x16x32_bf16 v[88:91], a[160:163], a[192:195], v[24:27]\n"
    "s_add_u32 m0, s61, 65536\n"
    "s_add_u32 s86, s81, 4096\n"
    "v_mfma_f32_16x16x32_bf16 v[88:91], a[164:167], a[196:199], v[88:91]\n"
    "ds_read_b128 a[72:75], v3 offset:33792\n"
    "v_add_f32 v11, v11, v23\n"
    "v_mfma_f32_16x16x32_bf16 v[88:91], a[168:171], a[200:203], v[88:91]\n"
    "buffer_load_dwordx4 v4, s[48:51], s81 offen offset:0 sc1 lds\n"
    "v_mfma_f32_16x16x32_bf16 v[88:91], a[172:175], a[204:207], v[88:91]\n"
    "ds_read_b128 a[76:79], v3 offset:50176\n"
    "v_cvt_pk_bf16_f32 v55, v22, v23\n"
    "v_mfma_f32_16x16x32_bf16 v[88:91], a[176:179], a[208:211], v[88:91]\n"
    "ds_read_b128 a[80:83], v3 offset:34816\n"
    "v_max_f32 v60, v10, v11\n"
    "v_mfma_f32_16x16x32_bf16 v[88:91], a[180:183], a[212:215], v[88:91]\n"
    "buffer_load_dwordx4 v4, s[48:51], s81 offen offset:1024 sc1 lds\n"
    "v_mfma_f32_16x16x32_bf16 v[88:91], a[184:187], a[216:219], v[88:91]\n"
    "ds_read_b128 a[84:87], v3 offset:51200\n"
    "v_cmp_nge_f32 vcc, s89, v60\n"
    "v_mfma_f32_16x16x32_bf16 v[88:91], a[188:191], a[220:223], v[88:91]\n"
    "ds_read_b128 a[88:91], v3 offset:35840\n"
    "v_mfma_f32_16x16x32_bf16 v[92:95], a[160:163], a[224:227], v[28:31]\n"
    "ds_read_b128 a[92:95], v3 offset:52224\n"
    "v_mfma_f32_16x16x32_bf16 v[92:95], a[164:167], a[228:231], v[92:95]\n"
    "buffer_load_dwordx4 v4, s[48:51], s81 offen offset:2048 sc1 lds\n"
    "v_mfma_f32_16x16x32_bf16 v[92:95], a[168:171], a[232:235], v[92:95]\n"
    "ds_read_b128 a[96:99], v3 offset:36864\n"
    "s_cbranch_vccnz .Lk2_rare1\n"
    ".Lk2_rare1_ret:\n"
    "v_mfma_f32_16x16x32_bf16 v[92:95], a[172:175], a[236:239], v[92:95]\n"
    "ds_read_b128 a[100:103], v3 offset:53248\n"
    "v_add_f32 v8, v8, v10\n"
    "v_mfma_f32_16x16x32_bf16 v[92:95], a[176:179], a[240:243], v[92:95]\n"
    "buffer_load_dwordx4 v4, s[48:51], s81 offen offset:3072 sc1 lds\n"
    "v_mfma_f32_16x16x32_bf16 v[92:95], a[180:183], a[244:247], v[92:95]\n"
    "ds_read_b128 a[104:107], v3 offset:37888\n"
    "v_add_f32 v9, v9, v11\n"
    "v_mfma_f32_16x16x32_bf16 v[92:95], a[184:187], a[248:251], v[92:95]\n"
    "ds_read_b128 a[108:111], v3 offset:54272\n"
    "v_mfma_f32_16x16x32_bf16 v[92:95], a[188:191], a[252:255], v[92:95]\n"
    "s_waitcnt lgkmcnt(4)\n"
    "v_mfma_f32_16x16x32_bf16 v[128:131], a[64:67], v[40:43], v[128:131]\n"
    "s_add_u32 m0, s61, 69632\n"
    "s_add_u32 s81, s81, 32768\n"
    "v_mfma_f32_16x16x32_bf16 v[128:131], a[68:71], v[48:51], v[128:131]\n"
    "ds_read_b128 a[112:115], v3 offset:38912\n"
    "v_mfma_f32_16x16x32_bf16 v[192:195], a[68:71], v[52:55], v[192:195]\n"
    "s_bitcmp1_b32 s40, s83\n"
    "s_cbranch_scc1 .Lk2_mask0\n"
    ".Lk2_mask0_ret:\n"
    "v_mfma_f32_16x16x32_bf16 v[192:195], a[64:67], v[44:47], v[192:195]\n"
    "buffer_load_dwordx4 v4, s[48:51], s86 offen offset:0 sc1 lds\n"
    "v_mfma_f32_16x16x32_bf16 v[196:199], a[72:75], v[44:47], v[196:199]\n"
    "ds_read_b128 a[116:119], v3 offset:55296\n"
    "v_mfma_f32_16x16x32_bf16 v[196:199], a[76:79], v[52:55], v[196:199]\n"
    "v_exp_f32 v16, v64\n"
    "v_mfma_f32_16x16x32_bf16 v[132:135], a[76:79], v[48:51], v[132:135]\n"
    "ds_read_b128 a[120:123], v3 offset:39936\n"
    "v_mfma_f32_16x16x32_bf16 v[132:135], a[72:75], v[40:43], v[132:135]\n"
    "buffer_load_dwordx4 v4, s[48:51], s86 offen offset:1024 sc1 lds\n"
    "v_mfma_f32_16x16x32_bf16 v[136:139], a[80:83], v[40:43], v[136:139]\n"
    "ds_read_b128 a[124:127], v3 offset:56320\n"
    "v_mfma_f32_16x16x32_bf16 v[136:139], a[84:87], v[48:51], v[136:139]\n"
    "buffer_load_dwordx4 v4, s[48:51], s86 offen offset:2048 sc1 lds\n"
    "v_mfma_f32_16x16x32_bf16 v[200:203], a[84:87], v[52:55], v[200:203]\n"
    "v_exp_f32 v17, v65\n"
    "v_mfma_f32_16x16x32_bf16 v[200:203], a[80:83], v[44:47], v[200:203]\n"
    "ds_read_b128 a[128:131], v3 offset:40960\n"
    "v_add_f32 v10, v16, v17\n"
    "v_mfma_f32_16x16x32_bf16 v[204:207], a[88:91], v[44:47], v[204:207]\n"
    "buffer_load_dwordx4 v4, s[48:51], s86 offen offset:3072 sc1 lds\n"
    "v_mfma_f32_16x16x32_bf16 v[204:207], a[92:95], v[52:55], v[204:207]\n"
    "ds_read_b128 a[132:135], v3 offset:57344\n"
    "v_cvt_pk_bf16_f32 v32, v16, v17\n"
    "v_mfma_f32_16x16x32_bf16 v[140:143], a[92:95], v[48:51], v[140:143]\n"
    "s_cmp_eq_u32 s80, s82\n"
    "s_cselect_b32 s80, s58, s80\n"
    "v_mfma_f32_16x16x32_bf16 v[140:143], a[88:91], v[40:43], v[140:143]\n"
    "ds_read_b128 a[136:139], v3 offset:41984\n"
    "s_waitcnt lgkmcnt(3)\n"
    "v_mfma_f32_16x16x32_bf16 v[144:147], a[96:99], v[40:43], v[144:147]\n"
    "v_exp_f32 v18, v66\n"
    "v_mfma_f32_16x16x32_bf16 v[144:147], a[100:103], v[48:51], v[144:147]\n"
    "ds_read_b128 a[140:143], v3 offset:58368\n"
    "v_add_f32 v10, v10, v18\n"
    "v_mfma_f32_16x16x32_bf16 v[208:211], a[100:103], v[52:55], v[208:211]\n"
    "s_add_u32 m0, s61, 0\n"
    "s_add_u32 s85, s80, 4096\n"
    "v_mfma_f32_16x16x32_bf16 v[208:211], a[96:99], v[44:47], v[208:211]\n"
    "ds_read_b128 a[144:147], v3 offset:43008\n"
    "v_mfma_f32_16x16x32_bf16 v[212:215], a[104:107], v[44:47], v[212:215]\n"
    "v_exp_f32 v19, v67\n"
    "v_mfma_f32_16x16x32_bf16 v[212:215], a[108:111], v[52:55], v[212:215]\n"
    "buffer_load_dwordx4 v4, s[48:51], s80 offen offset:0 sc1 lds\n"
    "v_mfma_f32_16x16x32_bf16 v[148:151], a[108:111], v[48:51], v[148:151]\n"
    "ds_read_b128 a[148:151], v3 offset:59392\n"
    "v_add_f32 v10, v10, v19\n"
    "v_mfma_f32_16x16x32_bf16 v[148:151], a[104:107], v[40:43], v[148:151]\n"
    "buffer_load_dwordx4 v4, s[48:51], s80 offen offset:1024 sc1 lds\n"
    "v_mfma_f32_16x16x32_bf16 v[152:155], a[112:115], v[40:43], v[152:155]\n"
    "ds_read_b128 a[152:155], v3 offset:44032\n"
    "v_cvt_pk_bf16_f32 v33, v18, v19\n"
    "v_mfma_f32_16x16x32_bf16 v[152:155], a[116:119], v[48:51], v[152:155]\n"
    "v_exp_f32 v20, v72\n"
    "v_mfma_f32_16x16x32_bf16 v[216:219], a[116:119], v[52:55], v[216:219]\n"
    "buffer_load_dwordx4 v4, s[48:51], s80 offen offset:2048 sc1 lds\n"
    "v_mfma_f32_16x16x32_bf16 v[216:219], a[112:115], v[44:47], v[216:219]\n"
    "ds_read_b128 a[156:159], v3 offset:60416\n"
    "v_add_f32 v10, v10, v20\n"
    "v_mfma_f32_16x16x32_bf16 v[220:223], a[120:123], v[44:47], v[220:223]\n"
    "ds_read_b128 a[160:163], v3 offset:45056\n"
    "v_mfma_f32_16x16x32_bf16 v[220:223], a[124:127], v[52:55], v[220:223]\n"
    "buffer_load_dwordx4 v4, s[48:51], s80 offen offset:3072 sc1 lds\n"
    "v_mfma_f32_16x16x32_bf16 v[156:159], a[124:127], v[48:51], v[156:159]\n"
    "v_exp_f32 v21, v73\n"
    "v_mfma_f32_16x16x32_bf16 v[156:159], a[120:123], v[40:43], v[156:159]\n"
    "ds_read_b128 a[164:167], v3 offset:61440\n"
    "s_waitcnt lgkmcnt(2)\n"
    "v_mfma_f32_16x16x32_bf16 v[160:163], a[128:131], v[40:43], v[160:163]\n"
    "s_add_u32 m0, s61, 4096\n"
    "s_add_u32 s80, s80, 32768\n"
    "v_mfma_f32_16x16x32_bf16 v[160:163], a[132:135], v[48:51], v[160:163]\n"
    "ds_read_b128 a[168:171], v3 offset:46080\n"
    "v_add_f32 v10, v10, v21\n"
    "v_mfma_f32_16x16x32_bf16 v[224:227], a[132:135], v[52:55], v[224:227]\n"
    "ds_read_b128 a[172:175], v3 offset:62464\n"
    "v_cvt_pk_bf16_f32 v34, v20, v21\n"
    "v_mfma_f32_16x16x32_bf16 v[224:227], a[128:131], v[44:47], v[224:227]\n"
    "buffer_load_dwordx4 v4, s[48:51], s85 offen offset:0 sc1 lds\n"
    "v_mfma_f32_16x16x32_bf16 v[228:231], a[136:139], v[44:47], v[228:231]\n"
    "v_exp_f32 v22, v74\n"
    "v_mfma_f32_16x16x32_bf16 v[228:231], a[140:143], v[52:55], v[228:231]\n"
    "ds_read_b128 a[176:179], v3 offset:47104\n"
    "v_add_f32 v10, v10, v22\n"
    "v_mfma_f32_16x16x32_bf16 v[164:167], a[140:143], v[48:51], v[164:167]\n"
    "buffer_load_dwordx4 v4, s[48:51], s85 offen offset:1024 sc1 lds\n"
    "v_mfma_f32_16x16x32_bf16 v[164:167], a[136:139], v[40:43], v[164:167]\n"
    "ds_read_b128 a[180:183], v3 offset:63488\n"
    "s_sub_u32 s83, s83, 1\n"
    "v_mfma_f32_16x16x32_bf16 v[168:171], a[144:147], v[40:43], v[168:171]\n"
    "buffer_load_dwordx4 v4, s[48:51], s85 offen offset:2048 sc1 lds\n"
    "v_mfma_f32_16x16x32_bf16 v[168:171], a[148:151], v[48:51], v[168:171]\n"
    "v_exp_f32 v23, v75\n"
    "v_mfma_f32_16x16x32_bf16 v[232:235], a[148:151], v[52:55], v[232:235]\n"
    "ds_read_b128 a[184:187], v3 offset:48128\n"
    "v_add_f32 v10, v10, v23\n"
    "v_mfma_f32_16x16x32_bf16 v[232:235], a[144:147], v[44:47], v[232:235]\n"
    "buffer_load_dwordx4 v4, s[48:51], s85 offen offset:3072 sc1 lds\n"
    "v_mfma_f32_16x16x32_bf16 v[236:239], a[152:155], v[44:47], v[236:239]\n"
    "ds_read_b128 a[188:191], v3 offset:64512\n"
    "v_cvt_pk_bf16_f32 v35, v22, v23\n"
    "v_mfma_f32_16x16x32_bf16 v[236:239], a[156:159], v[52:55], v[236:239]\n"
    "v_exp_f32 v16, v68\n"
    "v_mfma_f32_16x16x32_bf16 v[172:175], a[156:159], v[48:51], v[172:175]\n"
    "ds_read_b128 a[64:67], v2 offset:32768\n"
    "v_add_f32 v56, v56, v12\n"
    "v_mfma_f32_16x16x32_bf16 v[172:175], a[152:155], v[40:43], v[172:175]\n"
    "ds_read_b128 a[68:71], v2 offset:33792\n"
    "s_waitcnt lgkmcnt(2)\n"
    "v_mfma_f32_16x16x32_bf16 v[176:179], a[160:163], v[40:43], v[176:179]\n"
    "ds_read_b128 a[72:75], v2 offset:34816\n"
    "v_add_f32 v57, v57, v13\n"
    "v_mfma_f32_16x16x32_bf16 v[176:179], a[164:167], v[48:51], v[176:179]\n"
    "v_exp_f32 v17, v69\n"
    "v_mfma_f32_16x16x32_bf16 v[240:243], a[164:167], v[52:55], v[240:243]\n"
    "ds_read_b128 a[76:79], v2 offset:35840\n"
    "v_add_f32 v58, v58, v14\n"
    "v_mfma_f32_16x16x32_bf16 v[240:243], a[160:163], v[44:47], v[240:243]\n"
    "ds_read_b128 a[80:83], v2 offset:36864\n"
    "v_add_f32 v11, v16, v17\n"
    "v_mfma_f32_16x16x32_bf16 v[244:247], a[168:171], v[44:47], v[244:247]\n"
    "v_cvt_pk_bf16_f32 v36, v16, v17\n"
    "v_add_f32 v59, v59, v15\n"
    "v_mfma_f32_16x16x32_bf16 v[244:247], a[172:175], v[52:55], v[244:247]\n"
    "v_exp_f32 v18, v70\n"
    "v_mfma_f32_16x16x32_bf16 v[180:183], a[172:175], v[48:51], v[180:183]\n"
    "ds_read_b128 a[84:87], v2 offset:37888\n"
    "v_add_f32 v11, v11, v18\n"
    "v_mfma_f32_16x16x32_bf16 v[180:183], a[168:171], v[40:43], v[180:183]\n"
    "buffer_load_dwordx4 v[12:15], v5, s[36:39], s88 offen sc1\n"
    "v_mfma_f32_16x16x32_bf16 v[184:187], a[176:179], v[40:43], v[184:187]\n"
    "ds_read_b128 a[88:91], v2 offset:38912\n"
    "s_add_u32 s88, s88, s41\n"
    "v_mfma_f32_16x16x32_bf16 v[184:187], a[180:183], v[48:51], v[184:187]\n"
    "ds_read_b128 a[92:95], v2 offset:39936\n"
    "v_mfma_f32_16x16x32_bf16 v[248:251], a[180:183], v[52:55], v[248:251]\n"
    "v_exp_f32 v19, v71\n"
    "v_mfma_f32_16x16x32_bf16 v[248:251], a[176:179], v[44:47], v[248:251]\n"
    "ds_read_b128 a[96:99], v2 offset:40960\n"
    "v_add_f32 v11, v11, v19\n"
    "v_mfma_f32_16x16x32_bf16 v[252:255], a[184:187], v[44:47], v[252:255]\n"
    "ds_read_b128 a[100:103], v2 offset:41984\n"
    "v_cvt_pk_bf16_f32 v37, v18, v19\n"
    "v_mfma_f32_16x16x32_bf16 v[252:255], a[188:191], v[52:55], v[252:255]\n"
    "ds_read_b128 a[104:107], v2 offset:43008\n"
    "v_mfma_f32_16x16x32_bf16 v[188:191], a[188:191], v[48:51], v[188:191]\n"
    "ds_read_b128 a[108:111], v2 offset:44032\n"
    "v_mfma_f32_16x16x32_bf16 v[188:191], a[184:187], v[40:43], v[188:191]\n"
    "s_cmp_lg_u32 s83, s45\n"
    "s_cbranch_scc1 .Lk2_loop\n"
    ".Lk2_late:\n"
    ".Lk2_eg:\n"
    "s_barrier\n"
    "s_waitcnt lgkmcnt(8)\n"
    "v_mfma_f32_16x16x32_bf16 v[100:103], a[64:67], a[224:227], v[28:31]\n"
    "s_add_u32 m0, s61, 98304\n"
    "s_add_u32 s86, s81, 4096\n"
    "v_exp_f32 v20, v76\n"
    "v_exp_f32 v21, v77\n"
    "v_mfma_f32_16x16x32_bf16 v[100:103], a[68:71], a[228:231], v[100:103]\n"
    "ds_read_b128 a[112:115], v2 offset:45056\n"
    "v_exp_f32 v22, v78\n"
    "v_exp_f32 v23, v79\n"
    "v_add_f32 v11, v11, v20\n"
    "v_mfma_f32_16x16x32_bf16 v[100:103], a[72:75], a[232:235], v[100:103]\n"
    "ds_read_b128 a[116:119], v2 offset:46080\n"
    "buffer_load_dwordx4 v4, s[48:51], s81 offen offset:0 sc1 lds\n"
    "v_add_f32 v11, v11, v21\n"
    "v_cvt_pk_bf16_f32 v38, v20, v21\n"
    "v_mfma_f32_16x16x32_bf16 v[100:103], a[76:79], a[236:239], v[100:103]\n"
    "ds_read_b128 a[120:123], v2 offset:47104\n"
    "buffer_load_dwordx4 v4, s[48:51], s81 offen offset:1024 sc1 lds\n"
    "v_exp_f32 v16, v80\n"
    "s_waitcnt lgkmcnt(7)\n"
    "v_mfma_f32_16x16x32_bf16 v[100:103], a[80:83], a[240:243], v[100:103]\n"
    "ds_read_b128 a[124:127], v2 offset:48128\n"
    "v_exp_f32 v17, v81\n"
    "v_add_f32 v11, v11, v22\n"
    "v_add_f32 v11, v11, v23\n"
    "v_cvt_pk_bf16_f32 v39, v22, v23\n"
    "v_mfma_f32_16x16x32_bf16 v[100:103], a[84:87], a[244:247], v[100:103]\n"
    "ds_read_b128 a[128:131], v2 offset:49152\n"
    "buffer_load_dwordx4 v4, s[48:51], s81 offen offset:2048 sc1 lds\n"
    "v_exp_f32 v18, v82\n"
    "v_mfma_f32_16x16x32_bf16 v[100:103], a[88:91], a[248:251], v[100:103]\n"
    "ds_read_b128 a[132:135], v2 offset:50176\n"
    "v_exp_f32 v19, v83\n"
    "v_add_f32 v10, v10, v16\n"
    "v_add_f32 v10, v10, v17\n"
    "v_cvt_pk_bf16_f32 v48, v16, v17\n"
    "v_mfma_f32_16x16x32_bf16 v[100:103], a[92:95], a[252:255], v[100:103]\n"
    "ds_read_b128 a[136:139], v2 offset:51200\n"
    "buffer_load_dwordx4 v4, s[48:51], s81 offen offset:3072 sc1 lds\n"
    "v_exp_f32 v20, v88\n"
    "s_waitcnt lgkmcnt(7)\n"
    "v_mfma_f32_16x16x32_bf16 v[108:111], a[96:99], a[224:227], v[28:31]\n"
    "ds_read_b128 a[140:143], v2 offset:52224\n"
    "v_exp_f32 v21, v89\n"
    "s_add_u32 m0, s61, 102400\n"
    "s_add_u32 s81, s81, 32768\n"
    "v_add_f32 v10, v10, v18\n"
    "v_mfma_f32_16x16x32_bf16 v[108:111], a[100:103], a[228:231], v[108:111]\n"
    "ds_read_b128 a[144:147], v2 offset:53248\n"
    "v_add_f32 v10, v10, v19\n"
    "v_cvt_pk_bf16_f32 v49, v18, v19\n"
    "v_exp_f32 v22, v90\n"
    "v_mfma_f32_16x16x32_bf16 v[108:111], a[104:107], a[232:235], v[108:111]\n"
    "ds_read_b128 a[148:151], v2 offset:54272\n"
    "buffer_load_dwordx4 v4, s[48:51], s86 offen offset:0 sc1 lds\n"
    "v_exp_f32 v23, v91\n"
    "v_add_f32 v10, v10, v20\n"
    "v_mfma_f32_16x16x32_bf16 v[108:111], a[108:111], a[236:239], v[108:111]\n"
    "ds_read_b128 a[152:155], v2 offset:55296\n"
    "buffer_load_dwordx4 v4, s[48:51], s86 offen offset:1024 sc1 lds\n"
    "v_add_f32 v10, v10, v21\n"
    "v_cvt_pk_bf16_f32 v50, v20, v21\n"
    "s_waitcnt lgkmcnt(7)\n"
    "v_mfma_f32_16x16x32_bf16 v[108:111], a[112:115], a[240:243], v[108:111]\n"
    "ds_read_b128 a[156:159], v2 offset:56320\n"
    "v_exp_f32 v16, v84\n"
    "v_exp_f32 v17, v85\n"
    "v_add_f32 v10, v10, v22\n"
    "v_mfma_f32_16x16x32_bf16 v[108:111], a[116:119], a[244:247], v[108:111]\n"
    "ds_read_b128 a[160:163], v2 offset:57344\n"
    "buffer_load_dwordx4 v4, s[48:51], s86 offen offset:2048 sc1 lds\n"
    "v_add_f32 v10, v10, v23\n"
    "v_cvt_pk_bf16_f32 v51, v22, v23\n"
    "v_mfma_f32_16x16x32_bf16 v[108:111], a[120:123], a[248:251], v[108:111]\n"
    "ds_read_b128 a[164:167], v2 offset:58368\n"
    "v_exp_f32 v18, v86\n"
    "v_exp_f32 v19, v87\n"
    "v_mfma_f32_16x16x32_bf16 v[108:111], a[124:127], a[252:255], v[108:111]\n"
    "ds_read_b128 a[168:171], v2 offset:59392\n"
    "buffer_load_dwordx4 v4, s[48:51], s86 offen offset:3072 sc1 lds\n"
    "v_add_f32 v11, v11, v16\n"
    "v_add_f32 v11, v11, v17\n"
    "s_waitcnt lgkmcnt(7)\n"
    "v_mfma_f32_16x16x32_bf16 v[116:119], a[128:131], a[224:227], v[28:31]\n"
    "ds_read_b128 a[172:175], v2 offset:60416\n"
    "v_cvt_pk_bf16_f32 v52, v16, v17\n"
    "v_exp_f32 v20, v92\n"
    "v_mfma_f32_16x16x32_bf16 v[116:119], a[132:135], a[228:231], v[116:119]\n"
    "ds_read_b128 a[176:179], v2 offset:61440\n"
    "v_exp_f32 v21, v93\n"
    "v_add_f32 v11, v11, v18\n"
    "v_mfma_f32_16x16x32_bf16 v[116:119], a[136:139], a[232:235], v[116:119]\n"
    "ds_read_b128 a[180:183], v2 offset:62464\n"
    "v_add_f32 v11, v11, v19\n"
    "v_cvt_pk_bf16_f32 v53, v18, v19\n"
    "v_mfma_f32_16x16x32_bf16 v[116:119], a[140:143], a[236:239], v[116:119]\n"
    "ds_read_b128 a[184:187], v2 offset:63488\n"
    "v_exp_f32 v22, v94\n"
    "v_exp_f32 v23, v95\n"
    "s_waitcnt lgkmcnt(7)\n"
    "v_mfma_f32_16x16x32_bf16 v[116:119], a[144:147], a[240:243], v[116:119]\n"
    "ds_read_b128 a[188:191], v2 offset:64512\n"
    "v_add_f32 v11, v11, v20\n"
    "v_add_f32 v11, v11, v21\n"
    "v_mfma_f32_16x16x32_bf16 v[116:119], a[148:151], a[244:247], v[116:119]\n"
    "v_cvt_pk_bf16_f32 v54, v20, v21\n"
    "s_nop 0\n"
    "v_mfma_f32_16x16x32_bf16 v[116:119], a[152:155], a[248:251], v[116:119]\n"
    "v_add_f32 v11, v11, v22\n"
    "v_add_f32 v11, v11, v23\n"
    "v_mfma_f32_16x16x32_bf16 v[116:119], a[156:159], a[252:255], v[116:119]\n"
    "s_waitcnt vmcnt(8) lgkmcnt(0)\n"
    "s_barrier\n"
    "v_cvt_pk_bf16_f32 v55, v22, v23\n"
    "v_mfma_f32_16x16x32_bf16 v[124:127], a[160:163], a[224:227], v[28:31]\n"
    "ds_read_b128 a[64:67], v3 offset:0\n"
    "v_max_f32 v60, v10, v11\n"
    "v_cmp_nge_f32 vcc, s89, v60\n"
    "v_mfma_f32_16x16x32_bf16 v[124:127], a[164:167], a[228:231], v[124:127]\n"
    "ds_read_b128 a[68:71], v3 offset:16384\n"
    "s_cbranch_vccnz .Lk2_rare0e\n"
    ".Lk2_rare0e_ret:\n"
    "v_mfma_f32_16x16x32_bf16 v[124:127], a[168:171], a[232:235], v[124:127]\n"
    "ds_read_b128 a[72:75], v3 offset:1024\n"
    "v_add_f32 v8, v8, v10\n"
    "v_mfma_f32_16x16x32_bf16 v[124:127], a[172:175], a[236:239], v[124:127]\n"
    "ds_read_b128 a[76:79], v3 offset:17408\n"
    "v_add_f32 v9, v9, v11\n"
    "v_mfma_f32_16x16x32_bf16 v[124:127], a[176:179], a[240:243], v[124:127]\n"
    "ds_read_b128 a[80:83], v3 offset:2048\n"
    "v_mfma_f32_16x16x32_bf16 v[124:127], a[180:183], a[244:247], v[124:127]\n"
    "ds_read_b128 a[84:87], v3 offset:18432\n"
    "v_mfma_f32_16x16x32_bf16 v[124:127], a[184:187], a[248:251], v[124:127]\n"
    "ds_read_b128 a[88:91], v3 offset:3072\n"
    "v_mfma_f32_16x16x32_bf16 v[124:127], a[188:191], a[252:255], v[124:127]\n"
    "ds_read_b128 a[92:95], v3 offset:19456\n"
    "v_add_f32 v56, v56, v12\n"
    "v_add_f32 v57, v57, v13\n"
    "v_add_f32 v58, v58, v14\n"
    "v_add_f32 v59, v59, v15\n"
    "s_cmp_lt_u32 s88, 65536\n"
    "s_cbranch_scc1 .Lk2_kvc\n"
    ".Lk2_kvc_ret:\n"
    "buffer_load_dwordx2 v[24:25], off, s[48:51], s73 sc1\n"
    "s_waitcnt lgkmcnt(6)\n"
    "v_mfma_f32_16x16x32_bf16 v[128:131], a[64:67], v[32:35], v[128:131]\n"
    "ds_read_b128 a[96:99], v3 offset:4096\n"
    "s_cmp_eq_u32 s80, s82\n"
    "s_cselect_b32 s80, s58, s80\n"
    "v_mfma_f32_16x16x32_bf16 v[128:131], a[68:71], v[48:51], v[128:131]\n"
    "ds_read_b128 a[100:103], v3 offset:20480\n"
    "v_accvgpr_read_b32 v60, a10\n"
    "v_mov_b32 v61, 0xff800000\n"
    "s_lshl_b32 s87, s62, 5\n"
    "v_mfma_f32_16x16x32_bf16 v[192:195], a[68:71], v[52:55], v[192:195]\n"
    "ds_read_b128 a[104:107], v3 offset:5120\n"
    "s_sub_u32 s87, 48, s87\n"
    "v_add_u32 v60, s87, v60\n"
    "v_cmp_gt_i32 s[90:91], 0, v60\n"
    "v_mfma_f32_16x16x32_bf16 v[192:195], a[64:67], v[36:39], v[192:195]\n"
    "ds_read_b128 a[108:111], v3 offset:21504\n"
    "s_add_u32 m0, s61, 32768\n"
    "s_add_u32 s85, s80, 4096\n"
    "v_cmp_gt_i32 s[92:93], 1, v60\n"
    "s_waitcnt lgkmcnt(8)\n"
    "v_mfma_f32_16x16x32_bf16 v[196:199], a[72:75], v[36:39], v[196:199]\n"
    "ds_read_b128 a[112:115], v3 offset:6144\n"
    "v_cmp_gt_i32 s[94:95], 2, v60\n"
    "v_cmp_gt_i32 s[96:97], 3, v60\n"
    "v_cndmask_b32 v100, v100, v61, s[90:91]\n"
    "v_mfma_f32_16x16x32_bf16 v[196:199], a[76:79], v[52:55], v[196:199]\n"
    "ds_read_b128 a[116:119], v3 offset:22528\n"
    "v_cndmask_b32 v101, v101, v61, s[92:93]\n"
    "v_mfma_f32_16x16x32_bf16 v[132:135], a[76:79], v[48:51], v[132:135]\n"
    "buffer_load_dwordx4 v4, s[48:51], s80 offen offset:0 sc1 lds\n"
    "v_cndmask_b32 v102, v102, v61, s[94:95]\n"
    "v_mfma_f32_16x16x32_bf16 v[132:135], a[72:75], v[32:35], v[132:135]\n"
    "ds_read_b128 a[120:123], v3 offset:7168\n"
    "v_cndmask_b32 v103, v103, v61, s[96:97]\n"
    "s_waitcnt lgkmcnt(9)\n"
    "v_mfma_f32_16x16x32_bf16 v[136:139], a[80:83], v[32:35], v[136:139]\n"
    "ds_read_b128 a[124:127], v3 offset:23552\n"
    "v_cmp_gt_i32 s[90:91], 16, v60\n"
    "v_cmp_gt_i32 s[92:93], 17, v60\n"
    "v_mfma_f32_16x16x32_bf16 v[136:139], a[84:87], v[48:51], v[136:139]\n"
    "v_cmp_gt_i32 s[94:95], 18, v60\n"
    "v_mfma_f32_16x16x32_bf16 v[200:203], a[84:87], v[52:55], v[200:203]\n"
    "buffer_load_dwordx4 v4, s[48:51], s80 offen offset:1024 sc1 lds\n"
    "v_cmp_gt_i32 s[96:97], 19, v60\n"
    "v_mfma_f32_16x16x32_bf16 v[200:203], a[80:83], v[36:39], v[200:203]\n"
    "ds_read_b128 a[128:131], v3 offset:8192\n"
    "v_cndmask_b32 v108, v108, v61, s[90:91]\n"
    "v_cndmask_b32 v109, v109, v61, s[92:93]\n"
    "s_waitcnt lgkmcnt(9)\n"
    "v_mfma_f32_16x16x32_bf16 v[204:207], a[88:91], v[36:39], v[204:207]\n"
    "ds_read_b128 a[132:135], v3 offset:24576\n"
    "v_cndmask_b32 v110, v110, v61, s[94:95]\n"
    "v_mfma_f32_16x16x32_bf16 v[204:207], a[92:95], v[52:55], v[204:207]\n"
    "ds_read_b128 a[136:139], v3 offset:9216\n"
    "v_cndmask_b32 v111, v111, v61, s[96:97]\n"
    "v_mfma_f32_16x16x32_bf16 v[140:143], a[92:95], v[48:51], v[140:143]\n"
    "buffer_load_dwordx4 v4, s[48:51], s80 offen offset:2048 sc1 lds\n"
    "v_exp_f32 v16, v100\n"
    "v_mfma_f32_16x16x32_bf16 v[140:143], a[88:91], v[32:35], v[140:143]\n"
    "v_exp_f32 v17, v101\n"
    "v_exp_f32 v18, v102\n"
    "s_waitcnt lgkmcnt(9)\n"
    "v_mfma_f32_16x16x32_bf16 v[144:147], a[96:99], v[32:35], v[144:147]\n"
    "ds_read_b128 a[140:143], v3 offset:25600\n"
    "v_exp_f32 v19, v103\n"
    "v_mfma_f32_16x16x32_bf16 v[144:147], a[100:103], v[48:51], v[144:147]\n"
    "ds_read_b128 a[144:147], v3 offset:10240\n"
    "v_add_f32 v11, v16, v17\n"
    "v_mfma_f32_16x16x32_bf16 v[208:211], a[100:103], v[52:55], v[208:211]\n"
    "buffer_load_dwordx4 v4, s[48:51], s80 offen offset:3072 sc1 lds\n"
    "v_cvt_pk_bf16_f32 v44, v16, v17\n"
    "v_cmp_gt_i32 s[90:91], 32, v60\n"
    "v_mfma_f32_16x16x32_bf16 v[208:211], a[96:99], v[36:39], v[208:211]\n"
    "ds_read_b128 a[148:151], v3 offset:26624\n"
    "s_lshr_b32 s86, s62, 1\n"
    "v_cmp_gt_i32 s[92:93], 33, v60\n"
    "s_waitcnt lgkmcnt(10)\n"
    "v_mfma_f32_16x16x32_bf16 v[212:215], a[104:107], v[36:39], v[212:215]\n"
    "ds_read_b128 a[152:155], v3 offset:11264\n"
    "s_lshl_b32 s86, s86, 10\n"
    "v_cmp_gt_i32 s[94:95], 34, v60\n"
    "v_mfma_f32_16x16x32_bf16 v[212:215], a[108:111], v[52:55], v[212:215]\n"
    "s_add_u32 m0, s61, 36864\n"
    "s_add_u32 s80, s80, 32768\n"
    "s_and_b32 s81, s62, 1\n"
    "v_cmp_gt_i32 s[96:97], 35, v60\n"
    "v_mfma_f32_16x16x32_bf16 v[148:151], a[108:111], v[48:51], v[148:151]\n"
    "ds_read_b128 a[156:159], v3 offset:27648\n"
    "s_lshl_b32 s81, s81, 3\n"
    "v_cndmask_b32 v116, v116, v61, s[90:91]\n"
    "v_cndmask_b32 v117, v117, v61, s[92:93]\n"
    "v_mfma_f32_16x16x32_bf16 v[148:151], a[104:107], v[32:35], v[148:151]\n"
    "ds_read_b128 a[160:163], v3 offset:12288\n"
    "v_cndmask_b32 v118, v118, v61, s[94:95]\n"
    "s_waitcnt lgkmcnt(11)\n"
    "v_mfma_f32_16x16x32_bf16 v[152:155], a[112:115], v[32:35], v[152:155]\n"
    "s_add_u32 s86, s86, s81\n"
    "v_cndmask_b32 v119, v119, v61, s[96:97]\n"
    "v_mfma_f32_16x16x32_bf16 v[152:155], a[116:119], v[48:51], v[152:155]\n"
    "buffer_load_dwordx4 v4, s[48:51], s85 offen offset:0 sc1 lds\n"
    "v_exp_f32 v20, v108\n"
    "v_mfma_f32_16x16x32_bf16 v[216:219], a[116:119], v[52:55], v[216:219]\n"
    "ds_read_b128 a[164:167], v3 offset:28672\n"
    "v_exp_f32 v21, v109\n"
    "v_add_u32 v5, 65536, v3\n"
    "v_mfma_f32_16x16x32_bf16 v[216:219], a[112:115], v[36:39], v[216:219]\n"
    "ds_read_b128 a[168:171], v3 offset:13312\n"
    "v_add_u32 v27, s86, v5\n"
    "v_add_f32 v11, v11, v18\n"
    "v_accvgpr_read_b32 v26, a8\n"
    "s_waitcnt lgkmcnt(11)\n"
    "v_mfma_f32_16x16x32_bf16 v[220:223], a[120:123], v[36:39], v[220:223]\n"
    "ds_read_b128 a[172:175], v3 offset:29696\n"
    "v_add_f32 v11, v11, v19\n"
    "v_cvt_pk_bf16_f32 v45, v18, v19\n"
    "v_mfma_f32_16x16x32_bf16 v[220:223], a[124:127], v[52:55], v[220:223]\n"
    "buffer_load_dwordx4 v4, s[48:51], s85 offen offset:1024 sc1 lds\n"
    "v_exp_f32 v22, v110\n"
    "v_mfma_f32_16x16x32_bf16 v[156:159], a[124:127], v[48:51], v[156:159]\n"
    "ds_read_b128 a[176:179], v3 offset:14336\n"
    "v_exp_f32 v23, v111\n"
    "v_add_f32 v11, v11, v20\n"
    "v_mfma_f32_16x16x32_bf16 v[156:159], a[120:123], v[32:35], v[156:159]\n"
    "v_add_f32 v11, v11, v21\n"
    "s_waitcnt lgkmcnt(11)\n"
    "v_mfma_f32_16x16x32_bf16 v[160:163], a[128:131], v[32:35], v[160:163]\n"
    "ds_read_b128 a[180:183], v3 offset:30720\n"
    "v_cvt_pk_bf16_f32 v46, v20, v21\n"
    "v_cmp_gt_i32 s[90:91], 48, v60\n"
    "v_mfma_f32_16x16x32_bf16 v[160:163], a[132:135], v[48:51], v[160:163]\n"
    "buffer_load_dwordx4 v4, s[48:51], s85 offen offset:2048 sc1 lds\n"
    "v_cmp_gt_i32 s[92:93], 49, v60\n"
    "v_mfma_f32_16x16x32_bf16 v[224:227], a[132:135], v[52:55], v[224:227]\n"
    "ds_read_b128 a[184:187], v3 offset:15360\n"
    "v_cmp_gt_i32 s[94:95], 50, v60\n"
    "v_mfma_f32_16x16x32_bf16 v[224:227], a[128:131], v[36:39], v[224:227]\n"
    "ds_read_b128 a[188:191], v3 offset:31744\n"
    "v_cmp_gt_i32 s[96:97], 51, v60\n"
    "s_waitcnt lgkmcnt(12)\n"
    "v_mfma_f32_16x16x32_bf16 v[228:231], a[136:139], v[36:39], v[228:231]\n"
    "v_cndmask_b32 v124, v124, v61, s[90:91]\n"
    "v_cndmask_b32 v125, v125, v61, s[92:93]\n"
    "v_mfma_f32_16x16x32_bf16 v[228:231], a[140:143], v[52:55], v[228:231]\n"
    "s_waitcnt vmcnt(7)\n"
    "s_bitcmp1_b32 s55, 4\n"
    "s_cbranch_scc1 .Lk2_kva\n"
    ".Lk2_kva_ret:\n"
    "v_cvt_pk_bf16_f32 v56, v56, v57\n"
    "v_mfma_f32_16x16x32_bf16 v[164:167], a[140:143], v[48:51], v[164:167]\n"
    "buffer_load_dwordx4 v4, s[48:51], s85 offen offset:3072 sc1 lds\n"
    "v_cvt_pk_bf16_f32 v57, v58, v59\n"
    "v_cndmask_b32 v126, v126, v61, s[94:95]\n"
    "v_mfma_f32_16x16x32_bf16 v[164:167], a[136:139], v[32:35], v[164:167]\n"
    "ds_write_b64 v27, v[56:57]\n"
    "v_cndmask_b32 v127, v127, v61, s[96:97]\n"
    "v_exp_f32 v16, v116\n"
    "s_waitcnt lgkmcnt(11)\n"
    "v_mfma_f32_16x16x32_bf16 v[168:171], a[144:147], v[32:35], v[168:171]\n"
    "v_exp_f32 v17, v117\n"
    "v_add_f32 v11, v11, v22\n"
    "v_mfma_f32_16x16x32_bf16 v[168:171], a[148:151], v[48:51], v[168:171]\n"
    "v_add_f32 v11, v11, v23\n"
    "v_mfma_f32_16x16x32_bf16 v[232:235], a[148:151], v[52:55], v[232:235]\n"
    "v_cvt_pk_bf16_f32 v47, v22, v23\n"
    "v_mfma_f32_16x16x32_bf16 v[232:235], a[144:147], v[36:39], v[232:235]\n"
    "v_exp_f32 v18, v118\n"
    "v_exp_f32 v19, v119\n"
    "s_waitcnt lgkmcnt(9)\n"
    "v_mfma_f32_16x16x32_bf16 v[236:239], a[152:155], v[36:39], v[236:239]\n"
    "v_add_f32 v11, v11, v16\n"
    "v_mfma_f32_16x16x32_bf16 v[236:239], a[156:159], v[52:55], v[236:239]\n"
    "v_add_f32 v11, v11, v17\n"
    "v_mfma_f32_16x16x32_bf16 v[172:175], a[156:159], v[48:51], v[172:175]\n"
    "s_waitcnt lgkmcnt(0)\n"
    "s_barrier\n"
    "s_bitcmp1_b32 s55, 2\n"
    "s_cbranch_scc1 .Lk2_poll\n"
    ".Lk2_poll_ret:\n"
    "v_mfma_f32_16x16x32_bf16 v[172:175], a[152:155], v[32:35], v[172:175]\n"
    "ds_read_b128 a[24:27], v5\n"
    "s_bitcmp1_b32 s55, 2\n"
    "s_cselect_b32 s64, s64, 1073741824\n"
    "s_cselect_b32 s65, s65, 1073741824\n"
    "v_mfma_f32_16x16x32_bf16 v[176:179], a[160:163], v[32:35], v[176:179]\n"
    "ds_read_b128 a[28:31], v5 offset:1024\n"
    "buffer_load_dwordx4 a[0:3], v1, s[48:51], s65 offen sc1\n"
    "v_cvt_pk_bf16_f32 v40, v16, v17\n"
    "v_mfma_f32_16x16x32_bf16 v[176:179], a[164:167], v[48:51], v[176:179]\n"
    "ds_read_b128 a[64:67], v3 offset:32768\n"
    "v_exp_f32 v20, v124\n"
    "s_add_u32 s81, s64, 0\n"
    "v_mfma_f32_16x16x32_bf16 v[240:243], a[164:167], v[52:55], v[240:243]\n"
    "ds_read_b128 a[68:71], v3 offset:49152\n"
    "v_exp_f32 v21, v125\n"
    "v_add_f32 v11, v11, v18\n"
    "v_mfma_f32_16x16x32_bf16 v[240:243], a[160:163], v[36:39], v[240:243]\n"
    "ds_read_b128 a[72:75], v3 offset:33792\n"
    "buffer_load_dwordx4 v[64:67], v26, s[48:51], s81 offen offset:0 sc1\n"
    "v_add_f32 v11, v11, v19\n"
    "v_mfma_f32_16x16x32_bf16 v[244:247], a[168:171], v[36:39], v[244:247]\n"
    "ds_read_b128 a[76:79], v3 offset:50176\n"
    "buffer_load_dwordx4 v[68:71], v26, s[48:51], s81 offen offset:1024 sc1\n"
    "v_cvt_pk_bf16_f32 v41, v18, v19\n"
    "v_mfma_f32_16x16x32_bf16 v[244:247], a[172:175], v[52:55], v[244:247]\n"
    "ds_read_b128 a[80:83], v3 offset:34816\n"
    "v_exp_f32 v22, v126\n"
    "v_mfma_f32_16x16x32_bf16 v[180:183], a[172:175], v[48:51], v[180:183]\n"
    "buffer_load_dwordx4 v[72:75], v26, s[48:51], s81 offen offset:2048 sc1\n"
    "v_exp_f32 v23, v127\n"
    "v_mfma_f32_16x16x32_bf16 v[180:183], a[168:171], v[32:35], v[180:183]\n"
    "ds_read_b128 a[84:87], v3 offset:51200\n"
    "buffer_load_dwordx4 v[76:79], v26, s[48:51], s81 offen offset:3072 sc1\n"
    "v_add_f32 v11, v11, v20\n"
    "v_mfma_f32_16x16x32_bf16 v[184:187], a[176:179], v[32:35], v[184:187]\n"
    "ds_read_b128 a[88:91], v3 offset:35840\n"
    "s_add_u32 s81, s64, 4096\n"
    "v_add_f32 v11, v11, v21\n"
    "v_cvt_pk_bf16_f32 v42, v20, v21\n"
    "v_mfma_f32_16x16x32_bf16 v[184:187], a[180:183], v[48:51], v[184:187]\n"
    "ds_read_b128 a[92:95], v3 offset:52224\n"
    "buffer_load_dwordx4 v[80:83], v26, s[48:51], s81 offen offset:0 sc1\n"
    "v_add_f32 v11, v11, v22\n"
    "v_mfma_f32_16x16x32_bf16 v[248:251], a[180:183], v[52:55], v[248:251]\n"
    "ds_read_b128 a[96:99], v3 offset:36864\n"
    "buffer_load_dwordx4 v[84:87], v26, s[48:51], s81 offen offset:1024 sc1\n"
    "v_add_f32 v11, v11, v23\n"
    "v_mfma_f32_16x16x32_bf16 v[248:251], a[176:179], v[36:39], v[248:251]\n"
    "ds_read_b128 a[100:103], v3 offset:53248\n"
    "buffer_load_dwordx4 v[88:91], v26, s[48:51], s81 offen offset:2048 sc1\n"
    "v_cvt_pk_bf16_f32 v43, v22, v23\n"
    "v_mfma_f32_16x16x32_bf16 v[252:255], a[184:187], v[36:39], v[252:255]\n"
    "ds_read_b128 a[104:107], v3 offset:37888\n"
    "buffer_load_dwordx4 v[92:95], v26, s[48:51], s81 offen offset:3072 sc1\n"
    "v_mfma_f32_16x16x32_bf16 v[252:255], a[188:191], v[52:55], v[252:255]\n"
    "ds_read_b128 a[108:111], v3 offset:54272\n"
    "v_mfma_f32_16x16x32_bf16 v[188:191], a[188:191], v[48:51], v[188:191]\n"
    "v_mfma_f32_16x16x32_bf16 v[188:191], a[184:187], v[32:35], v[188:191]\n"
    "v_cmp_nge_f32 vcc, s89, v11\n"
    "v_accvgpr_read_b32 v35, a9\n"
    "v_accvgpr_read_b32 v32, a12\n"
    "s_cbranch_vccnz .Lk2_raree\n"
    ".Lk2_raree_ret:\n"
    "v_add_f32 v9, v9, v11\n"
    "v_accvgpr_read_b32 v33, a13\n"
    "v_mov_b32 v34, v26\n"
    "s_waitcnt lgkmcnt(12)\n"
    "v_mfma_f32_16x16x32_bf16 v[12:15], a[24:27], a[16:19], 0\n"
    "ds_read_b128 a[112:115], v3 offset:38912\n"
    "v_and_b32 v39, 0xf0, v1\n"
    "v_lshrrev_b32 v39, 2, v39\n"
    "v_mfma_f32_16x16x32_bf16 v[56:59], a[28:31], a[16:19], 0\n"
    "s_cmp_eq_u32 s60, 0\n"
    "s_cselect_b64 s[90:91], 0, -1\n"
    "s_cselect_b32 s92, 0, 0xffff\n"
    "s_mov_b32 s93, 0\n"
    "v_mfma_f32_16x16x32_bf16 v[16:19], a[24:27], a[20:23], 0\n"
    "ds_read_b128 a[116:119], v3 offset:55296\n"
    "s_mov_b32 s94, 0\n"
    "s_cselect_b32 s95, 0xffff0000, 0\n"
    "s_cselect_b32 s87, 192, 0\n"
    "v_mfma_f32_16x16x32_bf16 v[20:23], a[28:31], a[20:23], 0\n"
    "ds_read_b128 a[120:123], v3 offset:39936\n"
    "v_add_u32 v39, s87, v39\n"
    "v_mov_b32 v60, v8\n"
    "v_mov_b32 v61, v9\n"
    "s_waitcnt lgkmcnt(11)\n"
    "v_mfma_f32_16x16x32_bf16 v[192:195], a[64:67], v[44:47], v[192:195]\n"
    "ds_read_b128 a[124:127], v3 offset:56320\n"
    "s_nop 0\n"
    "s_add_u32 s81, s64, 8192\n"
    "v_permlane32_swap_b32 v60, v8\n"
    "v_mfma_f32_16x16x32_bf16 v[192:195], a[68:71], v[40:43], v[192:195]\n"
    "ds_read_b128 a[128:131], v3 offset:40960\n"
    "buffer_load_dwordx4 v[96:99], v34, s[48:51], s81 offen offset:0 sc1\n"
    "v_permlane32_swap_b32 v61, v9\n"
    "v_mfma_f32_16x16x32_bf16 v[196:199], a[72:75], v[44:47], v[196:199]\n"
    "buffer_load_dwordx4 v[100:103], v34, s[48:51], s81 offen offset:1024 sc1\n"
    "v_add_f32 v8, v8, v60\n"
    "v_add_f32 v9, v9, v61\n"
    "v_mfma_f32_16x16x32_bf16 v[196:199], a[76:79], v[40:43], v[196:199]\n"
    "ds_read_b128 a[132:135], v3 offset:57344\n"
    "v_mov_b32 v60, v8\n"
    "v_mov_b32 v61, v9\n"
    "s_nop 0\n"
    "s_waitcnt lgkmcnt(10)\n"
    "v_mfma_f32_16x16x32_bf16 v[200:203], a[80:83], v[44:47], v[200:203]\n"
    "ds_read_b128 a[136:139], v3 offset:41984\n"
    "buffer_load_dwordx4 v[104:107], v34, s[48:51], s81 offen offset:2048 sc1\n"
    "v_permlane16_swap_b32 v60, v8\n"
    "v_mfma_f32_16x16x32_bf16 v[200:203], a[84:87], v[40:43], v[200:203]\n"
    "ds_read_b128 a[140:143], v3 offset:58368\n"
    "v_permlane16_swap_b32 v61, v9\n"
    "v_add_f32 v8, v8, v60\n"
    "v_add_f32 v9, v9, v61\n"
    "v_mfma_f32_16x16x32_bf16 v[204:207], a[88:91], v[44:47], v[204:207]\n"
    "ds_read_b128 a[144:147], v3 offset:43008\n"
    "buffer_load_dwordx4 v[108:111], v34, s[48:51], s81 offen offset:3072 sc1\n"
    "v_cndmask_b32 v60, v56, v12, s[90:91]\n"
    "v_mfma_f32_16x16x32_bf16 v[204:207], a[92:95], v[40:43], v[204:207]\n"
    "ds_read_b128 a[148:151], v3 offset:59392\n"
    "v_cndmask_b32 v61, v20, v16, s[90:91]\n"
    "s_add_u32 s81, s64, 12288\n"
    "s_waitcnt lgkmcnt(10)\n"
    "v_mfma_f32_16x16x32_bf16 v[208:211], a[96:99], v[44:47], v[208:211]\n"
    "ds_bpermute_b32 v60, v39, v60\n"
    "buffer_load_dwordx4 v[112:115], v34, s[48:51], s81 offen offset:0 sc1\n"
    "v_mfma_f32_16x16x32_bf16 v[208:211], a[100:103], v[40:43], v[208:211]\n"
    "ds_bpermute_b32 v61, v39, v61\n"
    "v_sub_f32 v62, 1.0, v32\n"
    "v_sub_f32 v63, 1.0, v33\n"
    "s_waitcnt lgkmcnt(1)\n"
    "v_add_f32 v60, 0x358637bd, v60\n"
    "v_mfma_f32_16x16x32_bf16 v[212:215], a[104:107], v[44:47], v[212:215]\n"
    "ds_read_b128 a[152:155], v3 offset:44032\n"
    "buffer_load_dwordx4 v[116:119], v34, s[48:51], s81 offen offset:1024 sc1\n"
    "s_waitcnt lgkmcnt(1)\n"
    "v_add_f32 v61, 0x358637bd, v61\n"
    "v_mfma_f32_16x16x32_bf16 v[212:215], a[108:111], v[40:43], v[212:215]\n"
    "ds_read_b128 a[156:159], v3 offset:60416\n"
    "v_rcp_f32 v60, v60\n"
    "v_mfma_f32_16x16x32_bf16 v[216:219], a[112:115], v[44:47], v[216:219]\n"
    "ds_read_b128 a[160:163], v3 offset:45056\n"
    "v_rcp_f32 v61, v61\n"
    "v_mul_f32 v60, v60, v62\n"
    "v_mfma_f32_16x16x32_bf16 v[216:219], a[116:119], v[40:43], v[216:219]\n"
    "ds_read_b128 a[164:167], v3 offset:61440\n"
    "buffer_load_dwordx4 v[120:123], v34, s[48:51], s81 offen offset:2048 sc1\n"
    "v_mul_f32 v61, v61, v63\n"
    "v_mfma_f32_16x16x32_bf16 v[220:223], a[120:123], v[44:47], v[220:223]\n"
    "ds_read_b128 a[168:171], v3 offset:46080\n"
    "buffer_load_dwordx4 v[124:127], v34, s[48:51], s81 offen offset:3072 sc1\n"
    "v_mul_f32 v12, v12, v60\n"
    "v_mfma_f32_16x16x32_bf16 v[220:223], a[124:127], v[40:43], v[220:223]\n"
    "ds_read_b128 a[172:175], v3 offset:62464\n"
    "v_mul_f32 v13, v13, v60\n"
    "v_mul_f32 v14, v14, v60\n"
    "v_mul_f32 v15, v15, v60\n"
    "v_mfma_f32_16x16x32_bf16 v[224:227], a[128:131], v[44:47], v[224:227]\n"
    "ds_read_b128 a[176:179], v3 offset:47104\n"
    "s_add_u32 m0, s61, 65536\n"
    "s_add_u32 s85, s63, 65536\n"
    "v_cvt_pk_bf16_f32 v24, v12, v13\n"
    "v_mfma_f32_16x16x32_bf16 v[224:227], a[132:135], v[40:43], v[224:227]\n"
    "buffer_load_dwordx4 v4, s[48:51], s85 offen offset:0 sc1 lds\n"
    "v_cvt_pk_bf16_f32 v25, v14, v15\n"
    "v_cndmask_b32 v24, v24, 0, s[92:93]\n"
    "v_mfma_f32_16x16x32_bf16 v[228:231], a[136:139], v[44:47], v[228:231]\n"
    "ds_read_b128 a[180:183], v3 offset:63488\n"
    "v_cndmask_b32 v25, v25, 0, s[92:93]\n"
    "v_mul_f32 v56, v56, v60\n"
    "v_mul_f32 v57, v57, v60\n"
    "v_mfma_f32_16x16x32_bf16 v[228:231], a[140:143], v[40:43], v[228:231]\n"
    "ds_read_b128 a[184:187], v3 offset:48128\n"
    "buffer_load_dwordx4 v4, s[48:51], s85 offen offset:1024 sc1 lds\n"
    "v_mul_f32 v58, v58, v60\n"
    "v_mfma_f32_16x16x32_bf16 v[232:235], a[144:147], v[44:47], v[232:235]\n"
    "ds_read_b128 a[188:191], v3 offset:64512\n"
    "buffer_load_dwordx4 v4, s[48:51], s85 offen offset:2048 sc1 lds\n"
    "v_mul_f32 v59, v59, v60\n"
    "v_mfma_f32_16x16x32_bf16 v[232:235], a[148:151], v[40:43], v[232:235]\n"
    "buffer_load_dwordx4 v4, s[48:51], s85 offen offset:3072 sc1 lds\n"
    "v_cvt_pk_bf16_f32 v26, v56, v57\n"
    "v_cvt_pk_bf16_f32 v27, v58, v59\n"
    "s_waitcnt lgkmcnt(6)\n"
    "v_mfma_f32_16x16x32_bf16 v[236:239], a[152:155], v[44:47], v[236:239]\n"
    "v_cndmask_b32 v26, v26, 0, s[94:95]\n"
    "v_cndmask_b32 v27, v27, 0, s[94:95]\n"
    "v_mul_f32 v16, v16, v61\n"
    "v_mul_f32 v17, v17, v61\n"
    "v_mfma_f32_16x16x32_bf16 v[236:239], a[156:159], v[40:43], v[236:239]\n"
    "s_add_u32 m0, s61, 69632\n"
    "s_add_u32 s85, s63, 69632\n"
    "v_mul_f32 v18, v18, v61\n"
    "v_mul_f32 v19, v19, v61\n"
    "v_mfma_f32_16x16x32_bf16 v[240:243], a[160:163], v[44:47], v[240:243]\n"
    "buffer_load_dwordx4 v4, s[48:51], s85 offen offset:0 sc1 lds\n"
    "v_cvt_pk_bf16_f32 v28, v16, v17\n"
    "v_cvt_pk_bf16_f32 v29, v18, v19\n"
    "v_mfma_f32_16x16x32_bf16 v[240:243], a[164:167], v[40:43], v[240:243]\n"
    "buffer_load_dwordx4 v4, s[48:51], s85 offen offset:1024 sc1 lds\n"
    "v_cndmask_b32 v28, v28, 0, s[92:93]\n"
    "v_cndmask_b32 v29, v29, 0, s[92:93]\n"
    "s_waitcnt lgkmcnt(2)\n"
    "v_mfma_f32_16x16x32_bf16 v[244:247], a[168:171], v[44:47], v[244:247]\n"
    "buffer_load_dwordx4 v4, s[48:51], s85 offen offset:2048 sc1 lds\n"
    "v_mul_f32 v20, v20, v61\n"
    "v_mul_f32 v21, v21, v61\n"
    "v_mfma_f32_16x16x32_bf16 v[244:247], a[172:175], v[40:43], v[244:247]\n"
    "v_mul_f32 v22, v22, v61\n"
    "v_mul_f32 v23, v23, v61\n"
    "v_cvt_pk_bf16_f32 v30, v20, v21\n"
    "v_cvt_pk_bf16_f32 v31, v22, v23\n"
    "v_mfma_f32_16x16x32_bf16 v[248:251], a[176:179], v[44:47], v[248:251]\n"
    "buffer_load_dwordx4 v4, s[48:51], s85 offen offset:3072 sc1 lds\n"
    "v_cndmask_b32 v30, v30, 0, s[94:95]\n"
    "v_cndmask_b32 v31, v31, 0, s[94:95]\n"
    "v_mfma_f32_16x16x32_bf16 v[248:251], a[180:183], v[40:43], v[248:251]\n"
    "s_waitcnt lgkmcnt(0)\n"
    "v_mfma_f32_16x16x32_bf16 v[252:255], a[184:187], v[44:47], v[252:255]\n"
    "v_mfma_f32_16x16x32_bf16 v[252:255], a[188:191], v[40:43], v[252:255]\n"
    "s_bitcmp1_b32 s55, 2\n"
    "s_cbranch_scc1 .Lk2_ep_comb\n"
    "v_rcp_f32 v60, v8\n"
    "v_rcp_f32 v62, v9\n"
    "v_mul_f32 v60, v60, v32\n"
    "v_mul_f32 v62, v62, v33\n"
    "s_nop 3\n"
    "v_pk_mul_f32 v[128:129], v[128:129], v[60:61] op_sel_hi:[1,0]\n"
    "v_pk_mul_f32 v[130:131], v[130:131], v[60:61] op_sel_hi:[1,0]\n"
    "v_pk_mul_f32 v[132:133], v[132:133], v[60:61] op_sel_hi:[1,0]\n"
    "v_pk_mul_f32 v[134:135], v[134:135], v[60:61] op_sel_hi:[1,0]\n"
    "v_pk_mul_f32 v[136:137], v[136:137], v[60:61] op_sel_hi:[1,0]\n"
    "v_pk_mul_f32 v[138:139], v[138:139], v[60:61] op_sel_hi:[1,0]\n"
    "v_pk_mul_f32 v[140:141], v[140:141], v[60:61] op_sel_hi:[1,0]\n"
    "v_pk_mul_f32 v[142:143], v[142:143], v[60:61] op_sel_hi:[1,0]\n"
    "v_pk_mul_f32 v[144:145], v[144:145], v[60:61] op_sel_hi:[1,0]\n"
    "v_pk_mul_f32 v[146:147], v[146:147], v[60:61] op_sel_hi:[1,0]\n"
    "v_pk_mul_f32 v[148:149], v[148:149], v[60:61] op_sel_hi:[1,0]\n"
    "v_pk_mul_f32 v[150:151], v[150:151], v[60:61] op_sel_hi:[1,0]\n"
    "v_pk_mul_f32 v[152:153], v[152:153], v[60:61] op_sel_hi:[1,0]\n"
    "v_pk_mul_f32 v[154:155], v[154:155], v[60:61] op_sel_hi:[1,0]\n"
    "v_pk_mul_f32 v[156:157], v[156:157], v[60:61] op_sel_hi:[1,0]\n"
    "v_pk_mul_f32 v[158:159], v[158:159], v[60:61] op_sel_hi:[1,0]\n"
    "v_pk_mul_f32 v[160:161], v[160:161], v[60:61] op_sel_hi:[1,0]\n"
    "v_pk_mul_f32 v[162:163], v[162:163], v[60:61] op_sel_hi:[1,0]\n"
    "v_pk_mul_f32 v[164:165], v[164:165], v[60:61] op_sel_hi:[1,0]\n"
    "v_pk_mul_f32 v[166:167], v[166:167], v[60:61] op_sel_hi:[1,0]\n"
    "v_pk_mul_f32 v[168:169], v[168:169], v[60:61] op_sel_hi:[1,0]\n"
    "v_pk_mul_f32 v[170:171], v[170:171], v[60:61] op_sel_hi:[1,0]\n"
    "v_pk_mul_f32 v[172:173], v[172:173], v[60:61] op_sel_hi:[1,0]\n"
    "v_pk_mul_f32 v[174:175], v[174:175], v[60:61] op_sel_hi:[1,0]\n"
    "v_pk_mul_f32 v[176:177], v[176:177], v[60:61] op_sel_hi:[1,0]\n"
    "v_pk_mul_f32 v[178:179], v[178:179], v[60:61] op_sel_hi:[1,0]\n"
    "v_pk_mul_f32 v[180:181], v[180:181], v[60:61] op_sel_hi:[1,0]\n"
    "v_pk_mul_f32 v[182:183], v[182:183], v[60:61] op_sel_hi:[1,0]\n"
    "v_pk_mul_f32 v[184:185], v[184:185], v[60:61] op_sel_hi:[1,0]\n"
    "v_pk_mul_f32 v[186:187], v[186:187], v[60:61] op_sel_hi:[1,0]\n"
    "v_pk_mul_f32 v[188:189], v[188:189], v[60:61] op_sel_hi:[1,0]\n"
    "v_pk_mul_f32 v[190:191], v[190:191], v[60:61] op_sel_hi:[1,0]\n"
    "v_pk_mul_f32 v[192:193], v[192:193], v[62:63] op_sel_hi:[1,0]\n"
    "v_pk_mul_f32 v[194:195], v[194:195], v[62:63] op_sel_hi:[1,0]\n"
    "v_pk_mul_f32 v[196:197], v[196:197], v[62:63] op_sel_hi:[1,0]\n"
    "v_pk_mul_f32 v[198:199], v[198:199], v[62:63] op_sel_hi:[1,0]\n"
    "v_pk_mul_f32 v[200:201], v[200:201], v[62:63] op_sel_hi:[1,0]\n"
    "v_pk_mul_f32 v[202:203], v[202:203], v[62:63] op_sel_hi:[1,0]\n"
    "v_pk_mul_f32 v[204:205], v[204:205], v[62:63] op_sel_hi:[1,0]\n"
    "v_pk_mul_f32 v[206:207], v[206:207], v[62:63] op_sel_hi:[1,0]\n"
    "v_pk_mul_f32 v[208:209], v[208:209], v[62:63] op_sel_hi:[1,0]\n"
    "v_pk_mul_f32 v[210:211], v[210:211], v[62:63] op_sel_hi:[1,0]\n"
    "v_pk_mul_f32 v[212:213], v[212:213], v[62:63] op_sel_hi:[1,0]\n"
    "v_pk_mul_f32 v[214:215], v[214:215], v[62:63] op_sel_hi:[1,0]\n"
    "v_pk_mul_f32 v[216:217], v[216:217], v[62:63] op_sel_hi:[1,0]\n"
    "v_pk_mul_f32 v[218:219], v[218:219], v[62:63] op_sel_hi:[1,0]\n"
    "v_pk_mul_f32 v[220:221], v[220:221], v[62:63] op_sel_hi:[1,0]\n"
    "v_pk_mul_f32 v[222:223], v[222:223], v[62:63] op_sel_hi:[1,0]\n"
    "v_pk_mul_f32 v[224:225], v[224:225], v[62:63] op_sel_hi:[1,0]\n"
    "v_pk_mul_f32 v[226:227], v[226:227], v[62:63] op_sel_hi:[1,0]\n"
    "v_pk_mul_f32 v[228:229], v[228:229], v[62:63] op_sel_hi:[1,0]\n"
    "v_pk_mul_f32 v[230:231], v[230:231], v[62:63] op_sel_hi:[1,0]\n"
    "v_pk_mul_f32 v[232:233], v[232:233], v[62:63] op_sel_hi:[1,0]\n"
    "v_pk_mul_f32 v[234:235], v[234:235], v[62:63] op_sel_hi:[1,0]\n"
    "v_pk_mul_f32 v[236:237], v[236:237], v[62:63] op_sel_hi:[1,0]\n"
    "v_pk_mul_f32 v[238:239], v[238:239], v[62:63] op_sel_hi:[1,0]\n"
    "v_pk_mul_f32 v[240:241], v[240:241], v[62:63] op_sel_hi:[1,0]\n"
    "v_pk_mul_f32 v[242:243], v[242:243], v[62:63] op_sel_hi:[1,0]\n"
    "v_pk_mul_f32 v[244:245], v[244:245], v[62:63] op_sel_hi:[1,0]\n"
    "v_pk_mul_f32 v[246:247], v[246:247], v[62:63] op_sel_hi:[1,0]\n"
    "v_pk_mul_f32 v[248:249], v[248:249], v[62:63] op_sel_hi:[1,0]\n"
    "v_pk_mul_f32 v[250:251], v[250:251], v[62:63] op_sel_hi:[1,0]\n"
    "v_pk_mul_f32 v[252:253], v[252:253], v[62:63] op_sel_hi:[1,0]\n"
    "v_pk_mul_f32 v[254:255], v[254:255], v[62:63] op_sel_hi:[1,0]\n"
    "s_waitcnt vmcnt(0)\n"
    ".Lk2_ep_store:\n"
    "s_barrier\n"
    "ds_read_b128 a[64:67], v2 offset:0\n"
    "v_cvt_pk_bf16_f32 v128, v128, v129\n"
    "v_cvt_pk_bf16_f32 v129, v130, v131\n"
    "v_cvt_pk_bf16_f32 v130, v132, v133\n"
    "v_cvt_pk_bf16_f32 v131, v134, v135\n"
    "ds_read_b128 a[68:71], v2 offset:1024\n"
    "v_cvt_pk_bf16_f32 v136, v136, v137\n"
    "v_cvt_pk_bf16_f32 v137, v138, v139\n"
    "v_cvt_pk_bf16_f32 v138, v140, v141\n"
    "v_cvt_pk_bf16_f32 v139, v142, v143\n"
    "ds_read_b128 a[72:75], v2 offset:2048\n"
    "v_cvt_pk_bf16_f32 v144, v144, v145\n"
    "v_cvt_pk_bf16_f32 v145, v146, v147\n"
    "v_cvt_pk_bf16_f32 v146, v148, v149\n"
    "v_cvt_pk_bf16_f32 v147, v150, v151\n"
    "ds_read_b128 a[76:79], v2 offset:3072\n"
    "v_cvt_pk_bf16_f32 v152, v152, v153\n"
    "v_cvt_pk_bf16_f32 v153, v154, v155\n"
    "v_cvt_pk_bf16_f32 v154, v156, v157\n"
    "v_cvt_pk_bf16_f32 v155, v158, v159\n"
    "ds_read_b128 a[80:83], v2 offset:4096\n"
    "v_cvt_pk_bf16_f32 v160, v160, v161\n"
    "v_cvt_pk_bf16_f32 v161, v162, v163\n"
    "v_cvt_pk_bf16_f32 v162, v164, v165\n"
    "v_cvt_pk_bf16_f32 v163, v166, v167\n"
    "ds_read_b128 a[84:87], v2 offset:5120\n"
    "v_cvt_pk_bf16_f32 v168, v168, v169\n"
    "v_cvt_pk_bf16_f32 v169, v170, v171\n"
    "v_cvt_pk_bf16_f32 v170, v172, v173\n"
    "v_cvt_pk_bf16_f32 v171, v174, v175\n"
    "ds_read_b128 a[88:91], v2 offset:6144\n"
    "v_cvt_pk_bf16_f32 v176, v176, v177\n"
    "v_cvt_pk_bf16_f32 v177, v178, v179\n"
    "v_cvt_pk_bf16_f32 v178, v180, v181\n"
    "v_cvt_pk_bf16_f32 v179, v182, v183\n"
    "ds_read_b128 a[92:95], v2 offset:7168\n"
    "v_cvt_pk_bf16_f32 v184, v184, v185\n"
    "v_cvt_pk_bf16_f32 v185, v186, v187\n"
    "v_cvt_pk_bf16_f32 v186, v188, v189\n"
    "v_cvt_pk_bf16_f32 v187, v190, v191\n"
    "ds_read_b128 a[96:99], v2 offset:8192\n"
    "v_cvt_pk_bf16_f32 v192, v192, v193\n"
    "v_cvt_pk_bf16_f32 v193, v194, v195\n"
    "v_cvt_pk_bf16_f32 v194, v196, v197\n"
    "v_cvt_pk_bf16_f32 v195, v198, v199\n"
    "ds_read_b128 a[100:103], v2 offset:9216\n"
    "v_cvt_pk_bf16_f32 v200, v200, v201\n"
    "v_cvt_pk_bf16_f32 v201, v202, v203\n"
    "v_cvt_pk_bf16_f32 v202, v204, v205\n"
    "v_cvt_pk_bf16_f32 v203, v206, v207\n"
    "ds_read_b128 a[104:107], v2 offset:10240\n"
    "v_cvt_pk_bf16_f32 v208, v208, v209\n"
    "v_cvt_pk_bf16_f32 v209, v210, v211\n"
    "v_cvt_pk_bf16_f32 v210, v212, v213\n"
    "v_cvt_pk_bf16_f32 v211, v214, v215\n"
    "ds_read_b128 a[108:111], v2 offset:11264\n"
    "v_cvt_pk_bf16_f32 v216, v216, v217\n"
    "v_cvt_pk_bf16_f32 v217, v218, v219\n"
    "v_cvt_pk_bf16_f32 v218, v220, v221\n"
    "v_cvt_pk_bf16_f32 v219, v222, v223\n"
    "ds_read_b128 a[112:115], v2 offset:12288\n"
    "v_cvt_pk_bf16_f32 v224, v224, v225\n"
    "v_cvt_pk_bf16_f32 v225, v226, v227\n"
    "v_cvt_pk_bf16_f32 v226, v228, v229\n"
    "v_cvt_pk_bf16_f32 v227, v230, v231\n"
    "ds_read_b128 a[116:119], v2 offset:13312\n"
    "v_cvt_pk_bf16_f32 v232, v232, v233\n"
    "v_cvt_pk_bf16_f32 v233, v234, v235\n"
    "v_cvt_pk_bf16_f32 v234, v236, v237\n"
    "v_cvt_pk_bf16_f32 v235, v238, v239\n"
    "v_cvt_pk_bf16_f32 v240, v240, v241\n"
    "v_cvt_pk_bf16_f32 v241, v242, v243\n"
    "v_cvt_pk_bf16_f32 v242, v244, v245\n"
    "v_cvt_pk_bf16_f32 v243, v246, v247\n"
    "v_cvt_pk_bf16_f32 v248, v248, v249\n"
    "v_cvt_pk_bf16_f32 v249, v250, v251\n"
    "v_cvt_pk_bf16_f32 v250, v252, v253\n"
    "v_cvt_pk_bf16_f32 v251, v254, v255\n"
    "s_nop 1\n"
    ".p2align 3\n"
    "s_waitcnt lgkmcnt(5)\n"
    "v_mfma_f32_16x16x32_bf16 v[64:67], a[64:67], v[128:131], 0\n"
    "ds_read_b128 a[120:123], v2 offset:14336\n"
    "v_mfma_f32_16x16x32_bf16 v[64:67], a[68:71], v[136:139], v[64:67]\n"
    "s_add_u32 m0, s61, 98304\n"
    "s_add_u32 s85, s63, 98304\n"
    "v_mfma_f32_16x16x32_bf16 v[64:67], a[72:75], v[144:147], v[64:67]\n"
    "ds_read_b128 a[124:127], v2 offset:15360\n"
    "v_mfma_f32_16x16x32_bf16 v[64:67], a[76:79], v[152:155], v[64:67]\n"
    "buffer_load_dwordx4 v4, s[48:51], s85 offen offset:0 sc1 lds\n"
    "v_mfma_f32_16x16x32_bf16 v[64:67], a[80:83], v[160:163], v[64:67]\n"
    "ds_read_b128 a[128:131], v2 offset:16384\n"
    "v_mfma_f32_16x16x32_bf16 v[64:67], a[84:87], v[168:171], v[64:67]\n"
    "v_mfma_f32_16x16x32_bf16 v[64:67], a[88:91], v[176:179], v[64:67]\n"
    "ds_read_b128 a[132:135], v2 offset:17408\n"
    "v_mfma_f32_16x16x32_bf16 v[64:67], a[92:95], v[184:187], v[64:67]\n"
    "buffer_load_dwordx4 v4, s[48:51], s85 offen offset:1024 sc1 lds\n"
    "v_mfma_f32_16x16x32_bf16 v[64:67], a[96:99], v[24:27], v[64:67]\n"
    "ds_read_b128 a[136:139], v2 offset:18432\n"
    "v_mfma_f32_16x16x32_bf16 v[72:75], a[64:67], v[192:195], 0\n"
    "v_mfma_f32_16x16x32_bf16 v[72:75], a[68:71], v[200:203], v[72:75]\n"
    "ds_read_b128 a[140:143], v2 offset:19456\n"
    "v_mfma_f32_16x16x32_bf16 v[72:75], a[72:75], v[208:211], v[72:75]\n"
    "buffer_load_dwordx4 v4, s[48:51], s85 offen offset:2048 sc1 lds\n"
    "v_mfma_f32_16x16x32_bf16 v[72:75], a[76:79], v[216:219], v[72:75]\n"
    "ds_read_b128 a[144:147], v2 offset:20480\n"
    "v_mfma_f32_16x16x32_bf16 v[72:75], a[80:83], v[224:227], v[72:75]\n"
    "v_mfma_f32_16x16x32_bf16 v[72:75], a[84:87], v[232:235], v[72:75]\n"
    "ds_read_b128 a[148:151], v2 offset:21504\n"
    "v_mfma_f32_16x16x32_bf16 v[72:75], a[88:91], v[240:243], v[72:75]\n"
    "buffer_load_dwordx4 v4, s[48:51], s85 offen offset:3072 sc1 lds\n"
    "v_mfma_f32_16x16x32_bf16 v[72:75], a[92:95], v[248:251], v[72:75]\n"
    "ds_read_b128 a[152:155], v2 offset:22528\n"
    "v_mfma_f32_16x16x32_bf16 v[72:75], a[96:99], v[28:31], v[72:75]\n"
    "s_waitcnt lgkmcnt(5)\n"
    "v_mfma_f32_16x16x32_bf16 v[68:71], a[100:103], v[128:131], 0\n"
    "s_add_u32 m0, s61, 102400\n"
    "s_add_u32 s85, s63, 102400\n"
    "v_mfma_f32_16x16x32_bf16 v[68:71], a[104:107], v[136:139], v[68:71]\n"
    "ds_read_b128 a[156:159], v2 offset:23552\n"
    "v_mfma_f32_16x16x32_bf16 v[68:71], a[108:111], v[144:147], v[68:71]\n"
    "ds_read_b128 a[160:163], v2 offset:24576\n"
    "v_mfma_f32_16x16x32_bf16 v[68:71], a[112:115], v[152:155], v[68:71]\n"
    "buffer_load_dwordx4 v4, s[48:51], s85 offen offset:0 sc1 lds\n"
    "v_mfma_f32_16x16x32_bf16 v[68:71], a[116:119], v[160:163], v[68:71]\n"
    "ds_read_b128 a[164:167], v2 offset:25600\n"
    "v_mfma_f32_16x16x32_bf16 v[68:71], a[120:123], v[168:171], v[68:71]\n"
    "v_mfma_f32_16x16x32_bf16 v[68:71], a[124:127], v[176:179], v[68:71]\n"
    "ds_read_b128 a[168:171], v2 offset:26624\n"
    "v_mfma_f32_16x16x32_bf16 v[68:71], a[128:131], v[184:187], v[68:71]\n"
    "buffer_load_dwordx4 v4, s[48:51], s85 offen offset:1024 sc1 lds\n"
    "v_mfma_f32_16x16x32_bf16 v[68:71], a[132:135], v[24:27], v[68:71]\n"
    "ds_read_b128 a[172:175], v2 offset:27648\n"
    "v_mfma_f32_16x16x32_bf16 v[76:79], a[100:103], v[192:195], 0\n"
    "v_mfma_f32_16x16x32_bf16 v[76:79], a[104:107], v[200:203], v[76:79]\n"
    "ds_read_b128 a[176:179], v2 offset:28672\n"
    "v_mfma_f32_16x16x32_bf16 v[76:79], a[108:111], v[208:211], v[76:79]\n"
    "buffer_load_dwordx4 v4, s[48:51], s85 offen offset:2048 sc1 lds\n"
    "v_mfma_f32_16x16x32_bf16 v[76:79], a[112:115], v[216:219], v[76:79]\n"
    "ds_read_b128 a[180:183], v2 offset:29696\n"
    "v_mfma_f32_16x16x32_bf16 v[76:79], a[116:119], v[224:227], v[76:79]\n"
    "buffer_load_dwordx4 v4, s[48:51], s85 offen offset:3072 sc1 lds\n"
    "v_mfma_f32_16x16x32_bf16 v[76:79], a[120:123], v[232:235], v[76:79]\n"
    "ds_read_b128 a[184:187], v2 offset:30720\n"
    "v_mfma_f32_16x16x32_bf16 v[76:79], a[124:127], v[240:243], v[76:79]\n"
    "v_mfma_f32_16x16x32_bf16 v[76:79], a[128:131], v[248:251], v[76:79]\n"
    "ds_read_b128 a[188:191], v2 offset:31744\n"
    "v_mfma_f32_16x16x32_bf16 v[76:79], a[132:135], v[28:31], v[76:79]\n"
    "s_waitcnt lgkmcnt(5)\n"
    "v_mfma_f32_16x16x32_bf16 v[80:83], a[136:139], v[128:131], 0\n"
    "s_add_u32 m0, s61, 131072\n"
    "s_add_u32 s85, s63, 131072\n"
    "v_mfma_f32_16x16x32_bf16 v[80:83], a[140:143], v[136:139], v[80:83]\n"
    "ds_read_b128 a[64:67], v2 offset:32768\n"
    "v_mfma_f32_16x16x32_bf16 v[80:83], a[144:147], v[144:147], v[80:83]\n"
    "ds_read_b128 a[68:71], v2 offset:33792\n"
    "v_mfma_f32_16x16x32_bf16 v[80:83], a[148:151], v[152:155], v[80:83]\n"
    "buffer_load_dwordx4 v4, s[48:51], s85 offen offset:0 sc1 lds\n"
    "v_mfma_f32_16x16x32_bf16 v[80:83], a[152:155], v[160:163], v[80:83]\n"
    "ds_read_b128 a[72:75], v2 offset:34816\n"
    "v_cvt_pk_bf16_f32 v64, v64, v65\n"
    "v_mfma_f32_16x16x32_bf16 v[80:83], a[156:159], v[168:171], v[80:83]\n"
    "v_mfma_f32_16x16x32_bf16 v[80:83], a[160:163], v[176:179], v[80:83]\n"
    "ds_read_b128 a[76:79], v2 offset:35840\n"
    "v_cvt_pk_bf16_f32 v65, v66, v67\n"
    "v_mfma_f32_16x16x32_bf16 v[80:83], a[164:167], v[184:187], v[80:83]\n"
    "buffer_load_dwordx4 v4, s[48:51], s85 offen offset:1024 sc1 lds\n"
    "v_mfma_f32_16x16x32_bf16 v[80:83], a[168:171], v[24:27], v[80:83]\n"
    "ds_read_b128 a[80:83], v2 offset:36864\n"
    "v_mfma_f32_16x16x32_bf16 v[88:91], a[136:139], v[192:195], 0\n"
    "buffer_load_dwordx4 v4, s[48:51], s85 offen offset:2048 sc1 lds\n"
    "v_mfma_f32_16x16x32_bf16 v[88:91], a[140:143], v[200:203], v[88:91]\n"
    "ds_read_b128 a[84:87], v2 offset:37888\n"
    "v_cvt_pk_bf16_f32 v66, v68, v69\n"
    "v_mfma_f32_16x16x32_bf16 v[88:91], a[144:147], v[208:211], v[88:91]\n"
    "v_mfma_f32_16x16x32_bf16 v[88:91], a[148:151], v[216:219], v[88:91]\n"
    "ds_read_b128 a[88:91], v2 offset:38912\n"
    "v_cvt_pk_bf16_f32 v67, v70, v71\n"
    "v_mfma_f32_16x16x32_bf16 v[88:91], a[152:155], v[224:227], v[88:91]\n"
    "buffer_load_dwordx4 v4, s[48:51], s85 offen offset:3072 sc1 lds\n"
    "v_mfma_f32_16x16x32_bf16 v[88:91], a[156:159], v[232:235], v[88:91]\n"
    "ds_read_b128 a[92:95], v2 offset:39936\n"
    "s_add_u32 s87, s56, 0\n"
    "v_mfma_f32_16x16x32_bf16 v[88:91], a[160:163], v[240:243], v[88:91]\n"
    "v_mfma_f32_16x16x32_bf16 v[88:91], a[164:167], v[248:251], v[88:91]\n"
    "ds_read_b128 a[96:99], v2 offset:40960\n"
    "v_mfma_f32_16x16x32_bf16 v[88:91], a[168:171], v[28:31], v[88:91]\n"
    "s_waitcnt lgkmcnt(5)\n"
    "v_mfma_f32_16x16x32_bf16 v[84:87], a[172:175], v[128:131], 0\n"
    "s_add_u32 m0, s61, 135168\n"
    "s_add_u32 s85, s63, 135168\n"
    "v_mfma_f32_16x16x32_bf16 v[84:87], a[176:179], v[136:139], v[84:87]\n"
    "buffer_store_dwordx4 v[64:67], v35, s[48:51], s87 offen sc1\n"
    "v_mfma_f32_16x16x32_bf16 v[84:87], a[180:183], v[144:147], v[84:87]\n"
    "ds_read_b128 a[100:103], v2 offset:41984\n"
    "v_cvt_pk_bf16_f32 v72, v72, v73\n"
    "v_mfma_f32_16x16x32_bf16 v[84:87], a[184:187], v[152:155], v[84:87]\n"
    "ds_read_b128 a[104:107], v2 offset:43008\n"
    "v_mfma_f32_16x16x32_bf16 v[84:87], a[188:191], v[160:163], v[84:87]\n"
    "buffer_load_dwordx4 v4, s[48:51], s85 offen offset:0 sc1 lds\n"
    "v_mfma_f32_16x16x32_bf16 v[84:87], a[64:67], v[168:171], v[84:87]\n"
    "ds_read_b128 a[108:111], v2 offset:44032\n"
    "v_cvt_pk_bf16_f32 v73, v74, v75\n"
    "v_mfma_f32_16x16x32_bf16 v[84:87], a[68:71], v[176:179], v[84:87]\n"
    "buffer_load_dwordx4 v4, s[48:51], s85 offen offset:1024 sc1 lds\n"
    "v_mfma_f32_16x16x32_bf16 v[84:87], a[72:75], v[184:187], v[84:87]\n"
    "ds_read_b128 a[112:115], v2 offset:45056\n"
    "v_cvt_pk_bf16_f32 v74, v76, v77\n"
    "v_mfma_f32_16x16x32_bf16 v[84:87], a[76:79], v[24:27], v[84:87]\n"
    "ds_read_b128 a[116:119], v2 offset:46080\n"
    "v_mfma_f32_16x16x32_bf16 v[92:95], a[172:175], v[192:195], 0\n"
    "buffer_load_dwordx4 v4, s[48:51], s85 offen offset:2048 sc1 lds\n"
    "v_mfma_f32_16x16x32_bf16 v[92:95], a[176:179], v[200:203], v[92:95]\n"
    "ds_read_b128 a[120:123], v2 offset:47104\n"
    "v_cvt_pk_bf16_f32 v75, v78, v79\n"
    "v_mfma_f32_16x16x32_bf16 v[92:95], a[180:183], v[208:211], v[92:95]\n"
    "v_mfma_f32_16x16x32_bf16 v[92:95], a[184:187], v[216:219], v[92:95]\n"
    "ds_read_b128 a[124:127], v2 offset:48128\n"
    "s_add_u32 s87, s56, 14336\n"
    "v_mfma_f32_16x16x32_bf16 v[92:95], a[188:191], v[224:227], v[92:95]\n"
    "buffer_load_dwordx4 v4, s[48:51], s85 offen offset:3072 sc1 lds\n"
    "v_mfma_f32_16x16x32_bf16 v[92:95], a[64:67], v[232:235], v[92:95]\n"
    "ds_read_b128 a[128:131], v2 offset:49152\n"
    "v_mfma_f32_16x16x32_bf16 v[92:95], a[68:71], v[240:243], v[92:95]\n"
    "buffer_store_dwordx4 v[72:75], v35, s[48:51], s87 offen sc1\n"
    "v_mfma_f32_16x16x32_bf16 v[92:95], a[72:75], v[248:251], v[92:95]\n"
    "ds_read_b128 a[132:135], v2 offset:50176\n"
    "v_mfma_f32_16x16x32_bf16 v[92:95], a[76:79], v[28:31], v[92:95]\n"
    "s_waitcnt lgkmcnt(5)\n"
    "v_mfma_f32_16x16x32_bf16 v[96:99], a[80:83], v[128:131], 0\n"
    "ds_read_b128 a[136:139], v2 offset:51200\n"
    "v_mfma_f32_16x16x32_bf16 v[96:99], a[84:87], v[136:139], v[96:99]\n"
    "v_mfma_f32_16x16x32_bf16 v[96:99], a[88:91], v[144:147], v[96:99]\n"
    "ds_read_b128 a[140:143], v2 offset:52224\n"
    "v_mfma_f32_16x16x32_bf16 v[96:99], a[92:95], v[152:155], v[96:99]\n"
    "v_mfma_f32_16x16x32_bf16 v[96:99], a[96:99], v[160:163], v[96:99]\n"
    "ds_read_b128 a[144:147], v2 offset:53248\n"
    "v_cvt_pk_bf16_f32 v80, v80, v81\n"
    "v_mfma_f32_16x16x32_bf16 v[96:99], a[100:103], v[168:171], v[96:99]\n"
    "v_mfma_f32_16x16x32_bf16 v[96:99], a[104:107], v[176:179], v[96:99]\n"
    "ds_read_b128 a[148:151], v2 offset:54272\n"
    "v_cvt_pk_bf16_f32 v81, v82, v83\n"
    "v_mfma_f32_16x16x32_bf16 v[96:99], a[108:111], v[184:187], v[96:99]\n"
    "v_mfma_f32_16x16x32_bf16 v[96:99], a[112:115], v[24:27], v[96:99]\n"
    "ds_read_b128 a[152:155], v2 offset:55296\n"
    "v_mfma_f32_16x16x32_bf16 v[104:107], a[80:83], v[192:195], 0\n"
    "v_cvt_pk_bf16_f32 v82, v84, v85\n"
    "v_mfma_f32_16x16x32_bf16 v[104:107], a[84:87], v[200:203], v[104:107]\n"
    "ds_read_b128 a[156:159], v2 offset:56320\n"
    "v_mfma_f32_16x16x32_bf16 v[104:107], a[88:91], v[208:211], v[104:107]\n"
    "v_mfma_f32_16x16x32_bf16 v[104:107], a[92:95], v[216:219], v[104:107]\n"
    "ds_read_b128 a[160:163], v2 offset:57344\n"
    "v_cvt_pk_bf16_f32 v83, v86, v87\n"
    "v_mfma_f32_16x16x32_bf16 v[104:107], a[96:99], v[224:227], v[104:107]\n"
    "v_mfma_f32_16x16x32_bf16 v[104:107], a[100:103], v[232:235], v[104:107]\n"
    "ds_read_b128 a[164:167], v2 offset:58368\n"
    "s_add_u32 s87, s56, 1024\n"
    "v_mfma_f32_16x16x32_bf16 v[104:107], a[104:107], v[240:243], v[104:107]\n"
    "v_mfma_f32_16x16x32_bf16 v[104:107], a[108:111], v[248:251], v[104:107]\n"
    "ds_read_b128 a[168:171], v2 offset:59392\n"
    "v_mfma_f32_16x16x32_bf16 v[104:107], a[112:115], v[28:31], v[104:107]\n"
    "s_waitcnt lgkmcnt(5)\n"
    "v_mfma_f32_16x16x32_bf16 v[100:103], a[116:119], v[128:131], 0\n"
    "buffer_store_dwordx4 v[80:83], v35, s[48:51], s87 offen sc1\n"
    "v_mfma_f32_16x16x32_bf16 v[100:103], a[120:123], v[136:139], v[100:103]\n"
    "ds_read_b128 a[172:175], v2 offset:60416\n"
    "v_mfma_f32_16x16x32_bf16 v[100:103], a[124:127], v[144:147], v[100:103]\n"
    "ds_read_b128 a[176:179], v2 offset:61440\n"
    "v_cvt_pk_bf16_f32 v88, v88, v89\n"
    "v_mfma_f32_16x16x32_bf16 v[100:103], a[128:131], v[152:155], v[100:103]\n"
    "v_mfma_f32_16x16x32_bf16 v[100:103], a[132:135], v[160:163], v[100:103]\n"
    "ds_read_b128 a[180:183], v2 offset:62464\n"
    "v_cvt_pk_bf16_f32 v89, v90, v91\n"
    "v_mfma_f32_16x16x32_bf16 v[100:103], a[136:139], v[168:171], v[100:103]\n"
    "v_mfma_f32_16x16x32_bf16 v[100:103], a[140:143], v[176:179], v[100:103]\n"
    "ds_read_b128 a[184:187], v2 offset:63488\n"
    "v_mfma_f32_16x16x32_bf16 v[100:103], a[144:147], v[184:187], v[100:103]\n"
    "v_cvt_pk_bf16_f32 v90, v92, v93\n"
    "v_mfma_f32_16x16x32_bf16 v[100:103], a[148:151], v[24:27], v[100:103]\n"
    "ds_read_b128 a[188:191], v2 offset:64512\n"
    "v_mfma_f32_16x16x32_bf16 v[108:111], a[116:119], v[192:195], 0\n"
    "v_mfma_f32_16x16x32_bf16 v[108:111], a[120:123], v[200:203], v[108:111]\n"
    "ds_read_b128 a[64:67], v3 offset:0\n"
    "v_cvt_pk_bf16_f32 v91, v94, v95\n"
    "v_mfma_f32_16x16x32_bf16 v[108:111], a[124:127], v[208:211], v[108:111]\n"
    "v_mfma_f32_16x16x32_bf16 v[108:111], a[128:131], v[216:219], v[108:111]\n"
    "ds_read_b128 a[68:71], v3 offset:1024\n"
    "s_add_u32 s87, s56, 15360\n"
    "v_mfma_f32_16x16x32_bf16 v[108:111], a[132:135], v[224:227], v[108:111]\n"
    "v_mfma_f32_16x16x32_bf16 v[108:111], a[136:139], v[232:235], v[108:111]\n"
    "ds_read_b128 a[72:75], v3 offset:2048\n"
    "v_mfma_f32_16x16x32_bf16 v[108:111], a[140:143], v[240:243], v[108:111]\n"
    "buffer_store_dwordx4 v[88:91], v35, s[48:51], s87 offen sc1\n"
    "v_mfma_f32_16x16x32_bf16 v[108:111], a[144:147], v[248:251], v[108:111]\n"
    "ds_read_b128 a[76:79], v3 offset:3072\n"
    "v_mfma_f32_16x16x32_bf16 v[108:111], a[148:151], v[28:31], v[108:111]\n"
    "s_waitcnt lgkmcnt(5)\n"
    "v_mfma_f32_16x16x32_bf16 v[112:115], a[152:155], v[128:131], 0\n"
    "s_waitcnt vmcnt(3) lgkmcnt(4)\n"
    "s_barrier\n"
    "v_mfma_f32_16x16x32_bf16 v[112:115], a[156:159], v[136:139], v[112:115]\n"
    "ds_read_b128 a[80:83], v3 offset:4096\n"
    "v_mfma_f32_16x16x32_bf16 v[112:115], a[160:163], v[144:147], v[112:115]\n"
    "ds_read_b128 a[84:87], v3 offset:5120\n"
    "v_mfma_f32_16x16x32_bf16 v[112:115], a[164:167], v[152:155], v[112:115]\n"
    "v_mfma_f32_16x16x32_bf16 v[112:115], a[168:171], v[160:163], v[112:115]\n"
    "ds_read_b128 a[88:91], v3 offset:6144\n"
    "v_cvt_pk_bf16_f32 v96, v96, v97\n"
    "v_mfma_f32_16x16x32_bf16 v[112:115], a[172:175], v[168:171], v[112:115]\n"
    "s_add_u32 m0, s61, 0\n"
    "s_add_u32 s85, s63, 163840\n"
    "v_mfma_f32_16x16x32_bf16 v[112:115], a[176:179], v[176:179], v[112:115]\n"
    "ds_read_b128 a[92:95], v3 offset:7168\n"
    "v_cvt_pk_bf16_f32 v97, v98, v99\n"
    "v_mfma_f32_16x16x32_bf16 v[112:115], a[180:183], v[184:187], v[112:115]\n"
    "v_mfma_f32_16x16x32_bf16 v[112:115], a[184:187], v[24:27], v[112:115]\n"
    "ds_read_b128 a[96:99], v3 offset:8192\n"
    "v_mfma_f32_16x16x32_bf16 v[120:123], a[152:155], v[192:195], 0\n"
    "buffer_load_dwordx4 v4, s[48:51], s85 offen offset:0 sc1 lds\n"
    "v_mfma_f32_16x16x32_bf16 v[120:123], a[156:159], v[200:203], v[120:123]\n"
    "ds_read_b128 a[100:103], v3 offset:9216\n"
    "v_cvt_pk_bf16_f32 v98, v100, v101\n"
    "v_mfma_f32_16x16x32_bf16 v[120:123], a[160:163], v[208:211], v[120:123]\n"
    "v_mfma_f32_16x16x32_bf16 v[120:123], a[164:167], v[216:219], v[120:123]\n"
    "ds_read_b128 a[104:107], v3 offset:10240\n"
    "v_cvt_pk_bf16_f32 v99, v102, v103\n"
    "v_mfma_f32_16x16x32_bf16 v[120:123], a[168:171], v[224:227], v[120:123]\n"
    "buffer_load_dwordx4 v4, s[48:51], s85 offen offset:1024 sc1 lds\n"
    "v_mfma_f32_16x16x32_bf16 v[120:123], a[172:175], v[232:235], v[120:123]\n"
    "ds_read_b128 a[108:111], v3 offset:11264\n"
    "s_add_u32 s87, s56, 2048\n"
    "v_mfma_f32_16x16x32_bf16 v[120:123], a[176:179], v[240:243], v[120:123]\n"
    "v_mfma_f32_16x16x32_bf16 v[120:123], a[180:183], v[248:251], v[120:123]\n"
    "ds_read_b128 a[112:115], v3 offset:12288\n"
    "v_mfma_f32_16x16x32_bf16 v[120:123], a[184:187], v[28:31], v[120:123]\n"
    "v_mfma_f32_16x16x32_bf16 v[116:119], a[188:191], v[128:131], 0\n"
    "buffer_load_dwordx4 v4, s[48:51], s85 offen offset:2048 sc1 lds\n"
    "s_waitcnt lgkmcnt(5)\n"
    "v_mfma_f32_16x16x32_bf16 v[116:119], a[64:67], v[136:139], v[116:119]\n"
    "buffer_store_dwordx4 v[96:99], v35, s[48:51], s87 offen sc1\n"
    "v_mfma_f32_16x16x32_bf16 v[116:119], a[68:71], v[144:147], v[116:119]\n"
    "ds_read_b128 a[116:119], v3 offset:13312\n"
    "v_cvt_pk_bf16_f32 v104, v104, v105\n"
    "v_mfma_f32_16x16x32_bf16 v[116:119], a[72:75], v[152:155], v[116:119]\n"
    "ds_read_b128 a[120:123], v3 offset:14336\n"
    "v_mfma_f32_16x16x32_bf16 v[116:119], a[76:79], v[160:163], v[116:119]\n"
    "buffer_load_dwordx4 v4, s[48:51], s85 offen offset:3072 sc1 lds\n"
    "v_mfma_f32_16x16x32_bf16 v[116:119], a[80:83], v[168:171], v[116:119]\n"
    "ds_read_b128 a[124:127], v3 offset:15360\n"
    "v_cvt_pk_bf16_f32 v105, v106, v107\n"
    "v_mfma_f32_16x16x32_bf16 v[116:119], a[84:87], v[176:179], v[116:119]\n"
    "ds_read_b128 a[128:131], v3 offset:16384\n"
    "v_mfma_f32_16x16x32_bf16 v[116:119], a[88:91], v[184:187], v[116:119]\n"
    "s_add_u32 m0, s61, 4096\n"
    "s_add_u32 s85, s63, 167936\n"
    "v_mfma_f32_16x16x32_bf16 v[116:119], a[92:95], v[24:27], v[116:119]\n"
    "ds_read_b128 a[132:135], v3 offset:17408\n"
    "v_cvt_pk_bf16_f32 v106, v108, v109\n"
    "v_mfma_f32_16x16x32_bf16 v[124:127], a[188:191], v[192:195], 0\n"
    "v_mfma_f32_16x16x32_bf16 v[124:127], a[64:67], v[200:203], v[124:127]\n"
    "ds_read_b128 a[136:139], v3 offset:18432\n"
    "v_cvt_pk_bf16_f32 v107, v110, v111\n"
    "v_mfma_f32_16x16x32_bf16 v[124:127], a[68:71], v[208:211], v[124:127]\n"
    "buffer_load_dwordx4 v4, s[48:51], s85 offen offset:0 sc1 lds\n"
    "v_mfma_f32_16x16x32_bf16 v[124:127], a[72:75], v[216:219], v[124:127]\n"
    "ds_read_b128 a[140:143], v3 offset:19456\n"
    "s_add_u32 s87, s56, 16384\n"
    "v_mfma_f32_16x16x32_bf16 v[124:127], a[76:79], v[224:227], v[124:127]\n"
    "v_mfma_f32_16x16x32_bf16 v[124:127], a[80:83], v[232:235], v[124:127]\n"
    "ds_read_b128 a[144:147], v3 offset:20480\n"
    "v_mfma_f32_16x16x32_bf16 v[124:127], a[84:87], v[240:243], v[124:127]\n"
    "buffer_load_dwordx4 v4, s[48:51], s85 offen offset:1024 sc1 lds\n"
    "v_mfma_f32_16x16x32_bf16 v[124:127], a[88:91], v[248:251], v[124:127]\n"
    "buffer_store_dwordx4 v[104:107], v35, s[48:51], s87 offen sc1\n"
    "v_mfma_f32_16x16x32_bf16 v[124:127], a[92:95], v[28:31], v[124:127]\n"
    "ds_read_b128 a[148:151], v3 offset:21504\n"
    "s_waitcnt lgkmcnt(5)\n"
    "v_mfma_f32_16x16x32_bf16 v[64:67], a[96:99], v[128:131], 0\n"
    "ds_read_b128 a[152:155], v3 offset:22528\n"
    "v_mfma_f32_16x16x32_bf16 v[64:67], a[100:103], v[136:139], v[64:67]\n"
    "buffer_load_dwordx4 v4, s[48:51], s85 offen offset:2048 sc1 lds\n"
    "v_mfma_f32_16x16x32_bf16 v[64:67], a[104:107], v[144:147], v[64:67]\n"
    "ds_read_b128 a[156:159], v3 offset:23552\n"
    "v_mfma_f32_16x16x32_bf16 v[64:67], a[108:111], v[152:155], v[64:67]\n"
    "v_mfma_f32_16x16x32_bf16 v[64:67], a[112:115], v[160:163], v[64:67]\n"
    "ds_read_b128 a[160:163], v3 offset:24576\n"
    "v_cvt_pk_bf16_f32 v112, v112, v113\n"
    "v_mfma_f32_16x16x32_bf16 v[64:67], a[116:119], v[168:171], v[64:67]\n"
    "buffer_load_dwordx4 v4, s[48:51], s85 offen offset:3072 sc1 lds\n"
    "v_mfma_f32_16x16x32_bf16 v[64:67], a[120:123], v[176:179], v[64:67]\n"
    "ds_read_b128 a[164:167], v3 offset:25600\n"
    "v_cvt_pk_bf16_f32 v113, v114, v115\n"
    "v_mfma_f32_16x16x32_bf16 v[64:67], a[124:127], v[184:187], v[64:67]\n"
    "v_mfma_f32_16x16x32_bf16 v[64:67], a[128:131], v[24:27], v[64:67]\n"
    "ds_read_b128 a[168:171], v3 offset:26624\n"
    "v_mfma_f32_16x16x32_bf16 v[72:75], a[96:99], v[192:195], 0\n"
    "s_add_u32 m0, s61, 32768\n"
    "s_add_u32 s85, s63, 196608\n"
    "v_mfma_f32_16x16x32_bf16 v[72:75], a[100:103], v[200:203], v[72:75]\n"
    "ds_read_b128 a[172:175], v3 offset:27648\n"
    "v_cvt_pk_bf16_f32 v114, v116, v117\n"
    "v_mfma_f32_16x16x32_bf16 v[72:75], a[104:107], v[208:211], v[72:75]\n"
    "v_mfma_f32_16x16x32_bf16 v[72:75], a[108:111], v[216:219], v[72:75]\n"
    "ds_read_b128 a[176:179], v3 offset:28672\n"
    "v_cvt_pk_bf16_f32 v115, v118, v119\n"
    "v_mfma_f32_16x16x32_bf16 v[72:75], a[112:115], v[224:227], v[72:75]\n"
    "buffer_load_dwordx4 v4, s[48:51], s85 offen offset:0 sc1 lds\n"
    "v_mfma_f32_16x16x32_bf16 v[72:75], a[116:119], v[232:235], v[72:75]\n"
    "ds_read_b128 a[180:183], v3 offset:29696\n"
    "s_add_u32 s87, s56, 3072\n"
    "v_mfma_f32_16x16x32_bf16 v[72:75], a[120:123], v[240:243], v[72:75]\n"
    "v_mfma_f32_16x16x32_bf16 v[72:75], a[124:127], v[248:251], v[72:75]\n"
    "ds_read_b128 a[184:187], v3 offset:30720\n"
    "v_mfma_f32_16x16x32_bf16 v[72:75], a[128:131], v[28:31], v[72:75]\n"
    "s_waitcnt lgkmcnt(5)\n"
    "v_mfma_f32_16x16x32_bf16 v[68:71], a[132:135], v[128:131], 0\n"
    "buffer_load_dwordx4 v4, s[48:51], s85 offen offset:1024 sc1 lds\n"
    "v_mfma_f32_16x16x32_bf16 v[68:71], a[136:139], v[136:139], v[68:71]\n"
    "buffer_store_dwordx4 v[112:115], v35, s[48:51], s87 offen sc1\n"
    "v_mfma_f32_16x16x32_bf16 v[68:71], a[140:143], v[144:147], v[68:71]\n"
    "ds_read_b128 a[188:191], v3 offset:31744\n"
    "v_cvt_pk_bf16_f32 v120, v120, v121\n"
    "v_mfma_f32_16x16x32_bf16 v[68:71], a[144:147], v[152:155], v[68:71]\n"
    "ds_read_b128 a[64:67], v3 offset:32768\n"
    "v_mfma_f32_16x16x32_bf16 v[68:71], a[148:151], v[160:163], v[68:71]\n"
    "buffer_load_dwordx4 v4, s[48:51], s85 offen offset:2048 sc1 lds\n"
    "v_mfma_f32_16x16x32_bf16 v[68:71], a[152:155], v[168:171], v[68:71]\n"
    "ds_read_b128 a[68:71], v3 offset:33792\n"
    "v_cvt_pk_bf16_f32 v121, v122, v123\n"
    "v_mfma_f32_16x16x32_bf16 v[68:71], a[156:159], v[176:179], v[68:71]\n"
    "ds_read_b128 a[72:75], v3 offset:34816\n"
    "v_mfma_f32_16x16x32_bf16 v[68:71], a[160:163], v[184:187], v[68:71]\n"
    "buffer_load_dwordx4 v4, s[48:51], s85 offen offset:3072 sc1 lds\n"
    "v_mfma_f32_16x16x32_bf16 v[68:71], a[164:167], v[24:27], v[68:71]\n"
    "ds_read_b128 a[76:79], v3 offset:35840\n"
    "v_cvt_pk_bf16_f32 v122, v124, v125\n"
    "v_mfma_f32_16x16x32_bf16 v[76:79], a[132:135], v[192:195], 0\n"
    "v_mfma_f32_16x16x32_bf16 v[76:79], a[136:139], v[200:203], v[76:79]\n"
    "s_waitcnt lgkmcnt(4)\n"
    "s_barrier\n"
    "v_mfma_f32_16x16x32_bf16 v[76:79], a[140:143], v[208:211], v[76:79]\n"
    "ds_read_b128 a[80:83], v3 offset:36864\n"
    "v_cvt_pk_bf16_f32 v123, v126, v127\n"
    "v_mfma_f32_16x16x32_bf16 v[76:79], a[144:147], v[216:219], v[76:79]\n"
    "s_add_u32 m0, s61, 36864\n"
    "s_add_u32 s85, s63, 200704\n"
    "v_mfma_f32_16x16x32_bf16 v[76:79], a[148:151], v[224:227], v[76:79]\n"
    "ds_read_b128 a[84:87], v3 offset:37888\n"
    "s_add_u32 s87, s56, 17408\n"
    "v_mfma_f32_16x16x32_bf16 v[76:79], a[152:155], v[232:235], v[76:79]\n"
    "ds_read_b128 a[88:91], v3 offset:38912\n"
    "v_mfma_f32_16x16x32_bf16 v[76:79], a[156:159], v[240:243], v[76:79]\n"
    "buffer_load_dwordx4 v4, s[48:51], s85 offen offset:0 sc1 lds\n"
    "v_mfma_f32_16x16x32_bf16 v[76:79], a[160:163], v[248:251], v[76:79]\n"
    "buffer_store_dwordx4 v[120:123], v35, s[48:51], s87 offen sc1\n"
    "v_mfma_f32_16x16x32_bf16 v[76:79], a[164:167], v[28:31], v[76:79]\n"
    "ds_read_b128 a[92:95], v3 offset:39936\n"
    "v_mfma_f32_16x16x32_bf16 v[80:83], a[168:171], v[128:131], 0\n"
    "ds_read_b128 a[96:99], v3 offset:40960\n"
    "v_mfma_f32_16x16x32_bf16 v[80:83], a[172:175], v[136:139], v[80:83]\n"
    "buffer_load_dwordx4 v4, s[48:51], s85 offen offset:1024 sc1 lds\n"
    "v_mfma_f32_16x16x32_bf16 v[80:83], a[176:179], v[144:147], v[80:83]\n"
    "ds_read_b128 a[100:103], v3 offset:41984\n"
    "v_mfma_f32_16x16x32_bf16 v[80:83], a[180:183], v[152:155], v[80:83]\n"
    "v_mfma_f32_16x16x32_bf16 v[80:83], a[184:187], v[160:163], v[80:83]\n"
    "ds_read_b128 a[104:107], v3 offset:43008\n"
    "v_cvt_pk_bf16_f32 v64, v64, v65\n"
    "v_mfma_f32_16x16x32_bf16 v[80:83], a[188:191], v[168:171], v[80:83]\n"
    "buffer_load_dwordx4 v4, s[48:51], s85 offen offset:2048 sc1 lds\n"
    "s_waitcnt lgkmcnt(8)\n"
    "v_mfma_f32_16x16x32_bf16 v[80:83], a[64:67], v[176:179], v[80:83]\n"
    "ds_read_b128 a[108:111], v3 offset:44032\n"
    "v_cvt_pk_bf16_f32 v65, v66, v67\n"
    "v_mfma_f32_16x16x32_bf16 v[80:83], a[68:71], v[184:187], v[80:83]\n"
    "v_mfma_f32_16x16x32_bf16 v[80:83], a[72:75], v[24:27], v[80:83]\n"
    "ds_read_b128 a[112:115], v3 offset:45056\n"
    "v_mfma_f32_16x16x32_bf16 v[88:91], a[168:171], v[192:195], 0\n"
    "buffer_load_dwordx4 v4, s[48:51], s85 offen offset:3072 sc1 lds\n"
    "v_mfma_f32_16x16x32_bf16 v[88:91], a[172:175], v[200:203], v[88:91]\n"
    "ds_read_b128 a[116:119], v3 offset:46080\n"
    "v_cvt_pk_bf16_f32 v66, v68, v69\n"
    "v_mfma_f32_16x16x32_bf16 v[88:91], a[176:179], v[208:211], v[88:91]\n"
    "v_accvgpr_read_b32 v22, a15\n"
    "v_mfma_f32_16x16x32_bf16 v[88:91], a[180:183], v[216:219], v[88:91]\n"
    "ds_read_b128 a[120:123], v3 offset:47104\n"
    "v_cvt_pk_bf16_f32 v67, v70, v71\n"
    "v_mfma_f32_16x16x32_bf16 v[88:91], a[184:187], v[224:227], v[88:91]\n"
    "s_add_u32 m0, s61, 65536\n"
    "s_add_u32 s85, s63, 229376\n"
    "v_mfma_f32_16x16x32_bf16 v[88:91], a[188:191], v[232:235], v[88:91]\n"
    "ds_read_b128 a[124:127], v3 offset:48128\n"
    "v_lshrrev_b32 v16, 1, v1\n"
    "v_mfma_f32_16x16x32_bf16 v[88:91], a[64:67], v[240:243], v[88:91]\n"
    "s_add_u32 s87, s56, 4096\n"
    "v_mfma_f32_16x16x32_bf16 v[88:91], a[68:71], v[248:251], v[88:91]\n"
    "ds_read_b128 a[128:131], v3 offset:49152\n"
    "v_mfma_f32_16x16x32_bf16 v[88:91], a[72:75], v[28:31], v[88:91]\n"
    "v_readlane_b32 s36, v22, 28\n"
    "s_waitcnt lgkmcnt(5)\n"
    "v_mfma_f32_16x16x32_bf16 v[84:87], a[76:79], v[128:131], 0\n"
    "buffer_load_dwordx4 v4, s[48:51], s85 offen offset:0 sc1 lds\n"
    "v_mfma_f32_16x16x32_bf16 v[84:87], a[80:83], v[136:139], v[84:87]\n"
    "buffer_store_dwordx4 v[64:67], v35, s[48:51], s87 offen sc1\n"
    "v_mfma_f32_16x16x32_bf16 v[84:87], a[84:87], v[144:147], v[84:87]\n"
    "ds_read_b128 a[132:135], v3 offset:50176\n"
    "v_cvt_pk_bf16_f32 v72, v72, v73\n"
    "v_mfma_f32_16x16x32_bf16 v[84:87], a[88:91], v[152:155], v[84:87]\n"
    "ds_read_b128 a[136:139], v3 offset:51200\n"
    "v_readlane_b32 s37, v22, 29\n"
    "v_mfma_f32_16x16x32_bf16 v[84:87], a[92:95], v[160:163], v[84:87]\n"
    "buffer_load_dwordx4 v4, s[48:51], s85 offen offset:1024 sc1 lds\n"
    "v_mfma_f32_16x16x32_bf16 v[84:87], a[96:99], v[168:171], v[84:87]\n"
    "ds_read_b128 a[140:143], v3 offset:52224\n"
    "v_cvt_pk_bf16_f32 v73, v74, v75\n"
    "v_mfma_f32_16x16x32_bf16 v[84:87], a[100:103], v[176:179], v[84:87]\n"
    "ds_read_b128 a[144:147], v3 offset:53248\n"
    "v_readlane_b32 s52, v22, 30\n"
    "v_mfma_f32_16x16x32_bf16 v[84:87], a[104:107], v[184:187], v[84:87]\n"
    "buffer_load_dwordx4 v4, s[48:51], s85 offen offset:2048 sc1 lds\n"
    "v_mfma_f32_16x16x32_bf16 v[84:87], a[108:111], v[24:27], v[84:87]\n"
    "ds_read_b128 a[148:151], v3 offset:54272\n"
    "v_cvt_pk_bf16_f32 v74, v76, v77\n"
    "v_mfma_f32_16x16x32_bf16 v[92:95], a[76:79], v[192:195], 0\n"
    "v_readlane_b32 s53, v22, 31\n"
    "v_mfma_f32_16x16x32_bf16 v[92:95], a[80:83], v[200:203], v[92:95]\n"
    "ds_read_b128 a[152:155], v3 offset:55296\n"
    "v_cvt_pk_bf16_f32 v75, v78, v79\n"
    "v_mfma_f32_16x16x32_bf16 v[92:95], a[84:87], v[208:211], v[92:95]\n"
    "buffer_load_dwordx4 v4, s[48:51], s85 offen offset:3072 sc1 lds\n"
    "v_mfma_f32_16x16x32_bf16 v[92:95], a[88:91], v[216:219], v[92:95]\n"
    "ds_read_b128 a[156:159], v3 offset:56320\n"
    "s_add_u32 s87, s56, 18432\n"
    "v_mfma_f32_16x16x32_bf16 v[92:95], a[92:95], v[224:227], v[92:95]\n"
    "v_readlane_b32 s54, v22, 32\n"
    "v_mfma_f32_16x16x32_bf16 v[92:95], a[96:99], v[232:235], v[92:95]\n"
    "ds_read_b128 a[160:163], v3 offset:57344\n"
    "v_mfma_f32_16x16x32_bf16 v[92:95], a[100:103], v[240:243], v[92:95]\n"
    "s_add_u32 m0, s61, 69632\n"
    "s_add_u32 s85, s63, 233472\n"
    "v_mfma_f32_16x16x32_bf16 v[92:95], a[104:107], v[248:251], v[92:95]\n"
    "buffer_store_dwordx4 v[72:75], v35, s[48:51], s87 offen sc1\n"
    "v_mfma_f32_16x16x32_bf16 v[92:95], a[108:111], v[28:31], v[92:95]\n"
    "ds_read_b128 a[164:167], v3 offset:58368\n"
    "s_waitcnt lgkmcnt(5)\n"
    "v_mfma_f32_16x16x32_bf16 v[96:99], a[112:115], v[128:131], 0\n"
    "ds_read_b128 a[168:171], v3 offset:59392\n"
    "v_readlane_b32 s57, v22, 33\n"
    "v_mfma_f32_16x16x32_bf16 v[96:99], a[116:119], v[136:139], v[96:99]\n"
    "buffer_load_dwordx4 v4, s[48:51], s85 offen offset:0 sc1 lds\n"
    "v_mfma_f32_16x16x32_bf16 v[96:99], a[120:123], v[144:147], v[96:99]\n"
    "ds_read_b128 a[172:175], v3 offset:60416\n"
    "v_readlane_b32 s58, v22, 34\n"
    "v_mfma_f32_16x16x32_bf16 v[96:99], a[124:127], v[152:155], v[96:99]\n"
    "v_mfma_f32_16x16x32_bf16 v[96:99], a[128:131], v[160:163], v[96:99]\n"
    "ds_read_b128 a[176:179], v3 offset:61440\n"
    "v_cvt_pk_bf16_f32 v80, v80, v81\n"
    "v_mfma_f32_16x16x32_bf16 v[96:99], a[132:135], v[168:171], v[96:99]\n"
    "buffer_load_dwordx4 v4, s[48:51], s85 offen offset:1024 sc1 lds\n"
    "v_mfma_f32_16x16x32_bf16 v[96:99], a[136:139], v[176:179], v[96:99]\n"
    "ds_read_b128 a[180:183], v3 offset:62464\n"
    "v_cvt_pk_bf16_f32 v81, v82, v83\n"
    "v_mfma_f32_16x16x32_bf16 v[96:99], a[140:143], v[184:187], v[96:99]\n"
    "v_and_b32 v16, 0xf8, v16\n"
    "v_mfma_f32_16x16x32_bf16 v[96:99], a[144:147], v[24:27], v[96:99]\n"
    "ds_read_b128 a[184:187], v3 offset:63488\n"
    "v_mfma_f32_16x16x32_bf16 v[104:107], a[112:115], v[192:195], 0\n"
    "buffer_load_dwordx4 v4, s[48:51], s85 offen offset:2048 sc1 lds\n"
    "v_mfma_f32_16x16x32_bf16 v[104:107], a[116:119], v[200:203], v[104:107]\n"
    "v_cvt_pk_bf16_f32 v82, v84, v85\n"
    "s_and_b32 s37, s37, 0xffff\n"
    "v_mfma_f32_16x16x32_bf16 v[104:107], a[120:123], v[208:211], v[104:107]\n"
    "ds_read_b128 a[188:191], v3 offset:64512\n"
    "v_mfma_f32_16x16x32_bf16 v[104:107], a[124:127], v[216:219], v[104:107]\n"
    "ds_read_b128 a[64:67], v5 offset:0\n"
    "v_cvt_pk_bf16_f32 v83, v86, v87\n"
    "v_mfma_f32_16x16x32_bf16 v[104:107], a[128:131], v[224:227], v[104:107]\n"
    "buffer_load_dwordx4 v4, s[48:51], s85 offen offset:3072 sc1 lds\n"
    "v_mfma_f32_16x16x32_bf16 v[104:107], a[132:135], v[232:235], v[104:107]\n"
    "ds_read_b128 a[68:71], v5 offset:1024\n"
    "s_mov_b32 s38, 1835008\n"
    "v_mfma_f32_16x16x32_bf16 v[104:107], a[136:139], v[240:243], v[104:107]\n"
    "s_add_u32 s87, s56, 5120\n"
    "v_mfma_f32_16x16x32_bf16 v[104:107], a[140:143], v[248:251], v[104:107]\n"
    "ds_read_b128 a[72:75], v5 offset:2048\n"
    "s_mov_b32 s39, 0x20000\n"
    "v_mfma_f32_16x16x32_bf16 v[104:107], a[144:147], v[28:31], v[104:107]\n"
    "s_waitcnt lgkmcnt(5)\n"
    "v_mfma_f32_16x16x32_bf16 v[100:103], a[148:151], v[128:131], 0\n"
    "buffer_store_dwordx4 v[80:83], v35, s[48:51], s87 offen sc1\n"
    "v_mfma_f32_16x16x32_bf16 v[100:103], a[152:155], v[136:139], v[100:103]\n"
    "ds_read_b128 a[76:79], v5 offset:3072\n"
    "v_mfma_f32_16x16x32_bf16 v[100:103], a[156:159], v[144:147], v[100:103]\n"
    "ds_read_b128 a[80:83], v5 offset:4096\n"
    "v_cvt_pk_bf16_f32 v88, v88, v89\n"
    "v_mfma_f32_16x16x32_bf16 v[100:103], a[160:163], v[152:155], v[100:103]\n"
    "v_mov_b32 v12, 0x3f80\n"
    "v_mfma_f32_16x16x32_bf16 v[100:103], a[164:167], v[160:163], v[100:103]\n"
    "ds_read_b128 a[84:87], v5 offset:5120\n"
    "v_cvt_pk_bf16_f32 v89, v90, v91\n"
    "v_mfma_f32_16x16x32_bf16 v[100:103], a[168:171], v[168:171], v[100:103]\n"
    "v_mfma_f32_16x16x32_bf16 v[100:103], a[172:175], v[176:179], v[100:103]\n"
    "ds_read_b128 a[88:91], v5 offset:6144\n"
    "v_mov_b32 v13, 0x3f800000\n"
    "v_mfma_f32_16x16x32_bf16 v[100:103], a[176:179], v[184:187], v[100:103]\n"
    "v_cvt_pk_bf16_f32 v90, v92, v93\n"
    "v_mfma_f32_16x16x32_bf16 v[100:103], a[180:183], v[24:27], v[100:103]\n"
    "ds_read_b128 a[92:95], v5 offset:7168\n"
    "v_mfma_f32_16x16x32_bf16 v[108:111], a[148:151], v[192:195], 0\n"
    "v_bfe_u32 v20, v1, 4, 4\n"
    "v_mfma_f32_16x16x32_bf16 v[108:111], a[152:155], v[200:203], v[108:111]\n"
    "ds_read_b128 a[96:99], v5 offset:8192\n"
    "v_cvt_pk_bf16_f32 v91, v94, v95\n"
    "v_mfma_f32_16x16x32_bf16 v[108:111], a[156:159], v[208:211], v[108:111]\n"
    "v_mfma_f32_16x16x32_bf16 v[108:111], a[160:163], v[216:219], v[108:111]\n"
    "ds_read_b128 a[100:103], v5 offset:9216\n"
    "s_add_u32 s87, s56, 19456\n"
    "v_mfma_f32_16x16x32_bf16 v[108:111], a[164:167], v[224:227], v[108:111]\n"
    "v_mul_u32_u24 v20, 896, v20\n"
    "v_mfma_f32_16x16x32_bf16 v[108:111], a[168:171], v[232:235], v[108:111]\n"
    "ds_read_b128 a[104:107], v5 offset:10240\n"
    "v_mfma_f32_16x16x32_bf16 v[108:111], a[172:175], v[240:243], v[108:111]\n"
    "buffer_store_dwordx4 v[88:91], v35, s[48:51], s87 offen sc1\n"
    "v_mfma_f32_16x16x32_bf16 v[108:111], a[176:179], v[248:251], v[108:111]\n"
    "ds_read_b128 a[108:111], v5 offset:11264\n"
    "v_lshrrev_b32 v21, 4, v1\n"
    "v_mfma_f32_16x16x32_bf16 v[108:111], a[180:183], v[28:31], v[108:111]\n"
    "s_waitcnt lgkmcnt(5)\n"
    "v_mfma_f32_16x16x32_bf16 v[112:115], a[184:187], v[128:131], 0\n"
    "ds_read_b128 a[112:115], v5 offset:12288\n"
    "v_mfma_f32_16x16x32_bf16 v[112:115], a[188:191], v[136:139], v[112:115]\n"
    "v_mfma_f32_16x16x32_bf16 v[112:115], a[64:67], v[144:147], v[112:115]\n"
    "ds_read_b128 a[116:119], v5 offset:13312\n"
    "v_and_b32 v21, 48, v21\n"
    "v_mfma_f32_16x16x32_bf16 v[112:115], a[68:71], v[152:155], v[112:115]\n"
    "v_mfma_f32_16x16x32_bf16 v[112:115], a[72:75], v[160:163], v[112:115]\n"
    "ds_read_b128 a[120:123], v5 offset:14336\n"
    "v_cvt_pk_bf16_f32 v96, v96, v97\n"
    "v_mfma_f32_16x16x32_bf16 v[112:115], a[76:79], v[168:171], v[112:115]\n"
    "v_add_u32 v20, v20, v21\n"
    "v_mfma_f32_16x16x32_bf16 v[112:115], a[80:83], v[176:179], v[112:115]\n"
    "ds_read_b128 a[124:127], v5 offset:15360\n"
    "v_cvt_pk_bf16_f32 v97, v98, v99\n"
    "v_mfma_f32_16x16x32_bf16 v[112:115], a[84:87], v[184:187], v[112:115]\n"
    "v_mfma_f32_16x16x32_bf16 v[112:115], a[88:91], v[24:27], v[112:115]\n"
    "ds_read_b128 a[128:131], v5 offset:16384\n"
    "v_mfma_f32_16x16x32_bf16 v[120:123], a[184:187], v[192:195], 0\n"
    "v_cvt_pk_bf16_f32 v98, v100, v101\n"
    "v_and_b32 v21, 0xffff0000, v2\n"
    "v_mfma_f32_16x16x32_bf16 v[120:123], a[188:191], v[200:203], v[120:123]\n"
    "ds_read_b128 a[132:135], v5 offset:17408\n"
    "v_mfma_f32_16x16x32_bf16 v[120:123], a[64:67], v[208:211], v[120:123]\n"
    "v_mfma_f32_16x16x32_bf16 v[120:123], a[68:71], v[216:219], v[120:123]\n"
    "ds_read_b128 a[136:139], v5 offset:18432\n"
    "v_cvt_pk_bf16_f32 v99, v102, v103\n"
    "v_mfma_f32_16x16x32_bf16 v[120:123], a[72:75], v[224:227], v[120:123]\n"
    "s_mov_b32 s81, 0\n"
    "v_mfma_f32_16x16x32_bf16 v[120:123], a[76:79], v[232:235], v[120:123]\n"
    "ds_read_b128 a[140:143], v5 offset:19456\n"
    "s_add_u32 s87, s56, 6144\n"
    "v_mfma_f32_16x16x32_bf16 v[120:123], a[80:83], v[240:243], v[120:123]\n"
    "v_mfma_f32_16x16x32_bf16 v[120:123], a[84:87], v[248:251], v[120:123]\n"
    "ds_read_b128 a[144:147], v5 offset:20480\n"
    "s_mov_b32 s83, 0\n"
    "v_mfma_f32_16x16x32_bf16 v[120:123], a[88:91], v[28:31], v[120:123]\n"
    "s_waitcnt lgkmcnt(5)\n"
    "v_mfma_f32_16x16x32_bf16 v[116:119], a[92:95], v[128:131], 0\n"
    "buffer_store_dwordx4 v[96:99], v35, s[48:51], s87 offen sc1\n"
    "v_mfma_f32_16x16x32_bf16 v[116:119], a[96:99], v[136:139], v[116:119]\n"
    "ds_read_b128 a[148:151], v5 offset:21504\n"
    "v_mfma_f32_16x16x32_bf16 v[116:119], a[100:103], v[144:147], v[116:119]\n"
    "ds_read_b128 a[152:155], v5 offset:22528\n"
    "v_cvt_pk_bf16_f32 v104, v104, v105\n"
    "v_mfma_f32_16x16x32_bf16 v[116:119], a[104:107], v[152:155], v[116:119]\n"
    "s_mov_b32 s86, 0\n"
    "v_mfma_f32_16x16x32_bf16 v[116:119], a[108:111], v[160:163], v[116:119]\n"
    "ds_read_b128 a[156:159], v5 offset:23552\n"
    "v_cvt_pk_bf16_f32 v105, v106, v107\n"
    "v_mfma_f32_16x16x32_bf16 v[116:119], a[112:115], v[168:171], v[116:119]\n"
    "s_mov_b32 s84, 0\n"
    "v_mfma_f32_16x16x32_bf16 v[116:119], a[116:119], v[176:179], v[116:119]\n"
    "ds_read_b128 a[160:163], v5 offset:24576\n"
    "v_mfma_f32_16x16x32_bf16 v[116:119], a[120:123], v[184:187], v[116:119]\n"
    "v_cvt_pk_bf16_f32 v106, v108, v109\n"
    "v_mfma_f32_16x16x32_bf16 v[116:119], a[124:127], v[24:27], v[116:119]\n"
    "ds_read_b128 a[164:167], v5 offset:25600\n"
    "v_mfma_f32_16x16x32_bf16 v[124:127], a[92:95], v[192:195], 0\n"
    "v_mov_b32 v60, s16\n"
    "v_mfma_f32_16x16x32_bf16 v[124:127], a[96:99], v[200:203], v[124:127]\n"
    "ds_read_b128 a[168:171], v5 offset:26624\n"
    "v_cvt_pk_bf16_f32 v107, v110, v111\n"
    "v_mfma_f32_16x16x32_bf16 v[124:127], a[100:103], v[208:211], v[124:127]\n"
    "v_mfma_f32_16x16x32_bf16 v[124:127], a[104:107], v[216:219], v[124:127]\n"
    "ds_read_b128 a[172:175], v5 offset:27648\n"
    "s_add_u32 s87, s56, 20480\n"
    "v_mfma_f32_16x16x32_bf16 v[124:127], a[108:111], v[224:227], v[124:127]\n"
    "v_mov_b32 v61, s17\n"
    "v_mfma_f32_16x16x32_bf16 v[124:127], a[112:115], v[232:235], v[124:127]\n"
    "ds_read_b128 a[176:179], v5 offset:28672\n"
    "v_mfma_f32_16x16x32_bf16 v[124:127], a[116:119], v[240:243], v[124:127]\n"
    "buffer_store_dwordx4 v[104:107], v35, s[48:51], s87 offen sc1\n"
    "v_mfma_f32_16x16x32_bf16 v[124:127], a[120:123], v[248:251], v[124:127]\n"
    "ds_read_b128 a[180:183], v5 offset:29696\n"
    "v_accvgpr_read_b32 v62, a11\n"
    "v_mfma_f32_16x16x32_bf16 v[124:127], a[124:127], v[28:31], v[124:127]\n"
    "s_waitcnt lgkmcnt(5)\n"
    "v_mfma_f32_16x16x32_bf16 v[64:67], a[128:131], v[128:131], 0\n"
    "ds_read_b128 a[184:187], v5 offset:30720\n"
    "v_mfma_f32_16x16x32_bf16 v[64:67], a[132:135], v[136:139], v[64:67]\n"
    "v_mfma_f32_16x16x32_bf16 v[64:67], a[136:139], v[144:147], v[64:67]\n"
    "ds_read_b128 a[188:191], v5 offset:31744\n"
    "v_mfma_f32_16x16x32_bf16 v[64:67], a[140:143], v[152:155], v[64:67]\n"
    "v_mfma_f32_16x16x32_bf16 v[64:67], a[144:147], v[160:163], v[64:67]\n"
    "s_waitcnt vmcnt(4)\n"
    "s_barrier\n"
    "v_mfma_f32_16x16x32_bf16 v[64:67], a[148:151], v[168:171], v[64:67]\n"
    "ds_read_b128 a[64:67], v2 offset:0\n"
    "v_cvt_pk_bf16_f32 v112, v112, v113\n"
    "v_mfma_f32_16x16x32_bf16 v[64:67], a[152:155], v[176:179], v[64:67]\n"
    "ds_read_b128 a[68:71], v2 offset:1024\n"
    "v_cvt_pk_bf16_f32 v113, v114, v115\n"
    "v_mfma_f32_16x16x32_bf16 v[64:67], a[156:159], v[184:187], v[64:67]\n"
    "v_mfma_f32_16x16x32_bf16 v[64:67], a[160:163], v[24:27], v[64:67]\n"
    "ds_read_b128 a[72:75], v2 offset:2048\n"
    "v_mfma_f32_16x16x32_bf16 v[72:75], a[128:131], v[192:195], 0\n"
    "v_cvt_pk_bf16_f32 v114, v116, v117\n"
    "v_mfma_f32_16x16x32_bf16 v[72:75], a[132:135], v[200:203], v[72:75]\n"
    "ds_read_b128 a[76:79], v2 offset:3072\n"
    "v_mfma_f32_16x16x32_bf16 v[72:75], a[136:139], v[208:211], v[72:75]\n"
    "v_mfma_f32_16x16x32_bf16 v[72:75], a[140:143], v[216:219], v[72:75]\n"
    "ds_read_b128 a[80:83], v2 offset:4096\n"
    "v_cvt_pk_bf16_f32 v115, v118, v119\n"
    "v_mfma_f32_16x16x32_bf16 v[72:75], a[144:147], v[224:227], v[72:75]\n"
    "v_mfma_f32_16x16x32_bf16 v[72:75], a[148:151], v[232:235], v[72:75]\n"
    "ds_read_b128 a[84:87], v2 offset:5120\n"
    "s_add_u32 s87, s56, 7168\n"
    "v_mfma_f32_16x16x32_bf16 v[72:75], a[152:155], v[240:243], v[72:75]\n"
    "v_mfma_f32_16x16x32_bf16 v[72:75], a[156:159], v[248:251], v[72:75]\n"
    "ds_read_b128 a[88:91], v2 offset:6144\n"
    "v_mfma_f32_16x16x32_bf16 v[72:75], a[160:163], v[28:31], v[72:75]\n"
    "s_waitcnt lgkmcnt(5)\n"
    "v_mfma_f32_16x16x32_bf16 v[68:71], a[164:167], v[128:131], 0\n"
    "buffer_store_dwordx4 v[112:115], v35, s[48:51], s87 offen sc1\n"
    "v_mfma_f32_16x16x32_bf16 v[68:71], a[168:171], v[136:139], v[68:71]\n"
    "ds_read_b128 a[92:95], v2 offset:7168\n"
    "v_mfma_f32_16x16x32_bf16 v[68:71], a[172:175], v[144:147], v[68:71]\n"
    "ds_read_b128 a[96:99], v2 offset:8192\n"
    "v_cvt_pk_bf16_f32 v120, v120, v121\n"
    "v_mfma_f32_16x16x32_bf16 v[68:71], a[176:179], v[152:155], v[68:71]\n"
    "v_mfma_f32_16x16x32_bf16 v[68:71], a[180:183], v[160:163], v[68:71]\n"
    "ds_read_b128 a[100:103], v2 offset:9216\n"
    "v_cvt_pk_bf16_f32 v121, v122, v123\n"
    "v_mfma_f32_16x16x32_bf16 v[68:71], a[184:187], v[168:171], v[68:71]\n"
    "v_mfma_f32_16x16x32_bf16 v[68:71], a[188:191], v[176:179], v[68:71]\n"
    "ds_read_b128 a[104:107], v2 offset:10240\n"
    "v_mfma_f32_16x16x32_bf16 v[68:71], a[64:67], v[184:187], v[68:71]\n"
    "v_cvt_pk_bf16_f32 v122, v124, v125\n"
    "v_mfma_f32_16x16x32_bf16 v[68:71], a[68:71], v[24:27], v[68:71]\n"
    "ds_read_b128 a[108:111], v2 offset:11264\n"
    "v_mfma_f32_16x16x32_bf16 v[76:79], a[164:167], v[192:195], 0\n"
    "v_mfma_f32_16x16x32_bf16 v[76:79], a[168:171], v[200:203], v[76:79]\n"
    "ds_read_b128 a[112:115], v2 offset:12288\n"
    "v_cvt_pk_bf16_f32 v123, v126, v127\n"
    "v_mfma_f32_16x16x32_bf16 v[76:79], a[172:175], v[208:211], v[76:79]\n"
    "v_mfma_f32_16x16x32_bf16 v[76:79], a[176:179], v[216:219], v[76:79]\n"
    "ds_read_b128 a[116:119], v2 offset:13312\n"
    "s_add_u32 s87, s56, 21504\n"
    "v_mfma_f32_16x16x32_bf16 v[76:79], a[180:183], v[224:227], v[76:79]\n"
    "v_mfma_f32_16x16x32_bf16 v[76:79], a[184:187], v[232:235], v[76:79]\n"
    "ds_read_b128 a[120:123], v2 offset:14336\n"
    "v_mfma_f32_16x16x32_bf16 v[76:79], a[188:191], v[240:243], v[76:79]\n"
    "buffer_store_dwordx4 v[120:123], v35, s[48:51], s87 offen sc1\n"
    "v_mfma_f32_16x16x32_bf16 v[76:79], a[64:67], v[248:251], v[76:79]\n"
    "ds_read_b128 a[124:127], v2 offset:15360\n"
    "v_mfma_f32_16x16x32_bf16 v[76:79], a[68:71], v[28:31], v[76:79]\n"
    "s_waitcnt lgkmcnt(5)\n"
    "v_mfma_f32_16x16x32_bf16 v[80:83], a[72:75], v[128:131], 0\n"
    "ds_read_b128 a[128:131], v2 offset:16384\n"
    "v_mfma_f32_16x16x32_bf16 v[80:83], a[76:79], v[136:139], v[80:83]\n"
    "v_mfma_f32_16x16x32_bf16 v[80:83], a[80:83], v[144:147], v[80:83]\n"
    "ds_read_b128 a[132:135], v2 offset:17408\n"
    "v_mfma_f32_16x16x32_bf16 v[80:83], a[84:87], v[152:155], v[80:83]\n"
    "v_mfma_f32_16x16x32_bf16 v[80:83], a[88:91], v[160:163], v[80:83]\n"
    "ds_read_b128 a[136:139], v2 offset:18432\n"
    "v_cvt_pk_bf16_f32 v64, v64, v65\n"
    "v_mfma_f32_16x16x32_bf16 v[80:83], a[92:95], v[168:171], v[80:83]\n"
    "v_mfma_f32_16x16x32_bf16 v[80:83], a[96:99], v[176:179], v[80:83]\n"
    "ds_read_b128 a[140:143], v2 offset:19456\n"
    "v_cvt_pk_bf16_f32 v65, v66, v67\n"
    "v_mfma_f32_16x16x32_bf16 v[80:83], a[100:103], v[184:187], v[80:83]\n"
    "v_mfma_f32_16x16x32_bf16 v[80:83], a[104:107], v[24:27], v[80:83]\n"
    "ds_read_b128 a[144:147], v2 offset:20480\n"
    "v_mfma_f32_16x16x32_bf16 v[88:91], a[72:75], v[192:195], 0\n"
    "v_cvt_pk_bf16_f32 v66, v68, v69\n"
    "v_mfma_f32_16x16x32_bf16 v[88:91], a[76:79], v[200:203], v[88:91]\n"
    "ds_read_b128 a[148:151], v2 offset:21504\n"
    "v_mfma_f32_16x16x32_bf16 v[88:91], a[80:83], v[208:211], v[88:91]\n"
    "v_mfma_f32_16x16x32_bf16 v[88:91], a[84:87], v[216:219], v[88:91]\n"
    "ds_read_b128 a[152:155], v2 offset:22528\n"
    "v_cvt_pk_bf16_f32 v67, v70, v71\n"
    "v_mfma_f32_16x16x32_bf16 v[88:91], a[88:91], v[224:227], v[88:91]\n"
    "v_mfma_f32_16x16x32_bf16 v[88:91], a[92:95], v[232:235], v[88:91]\n"
    "ds_read_b128 a[156:159], v2 offset:23552\n"
    "s_add_u32 s87, s56, 8192\n"
    "v_mfma_f32_16x16x32_bf16 v[88:91], a[96:99], v[240:243], v[88:91]\n"
    "v_mfma_f32_16x16x32_bf16 v[88:91], a[100:103], v[248:251], v[88:91]\n"
    "ds_read_b128 a[160:163], v2 offset:24576\n"
    "v_mfma_f32_16x16x32_bf16 v[88:91], a[104:107], v[28:31], v[88:91]\n"
    "s_waitcnt lgkmcnt(5)\n"
    "v_mfma_f32_16x16x32_bf16 v[84:87], a[108:111], v[128:131], 0\n"
    "buffer_store_dwordx4 v[64:67], v35, s[48:51], s87 offen sc1\n"
    "v_mfma_f32_16x16x32_bf16 v[84:87], a[112:115], v[136:139], v[84:87]\n"
    "ds_read_b128 a[164:167], v2 offset:25600\n"
    "v_mfma_f32_16x16x32_bf16 v[84:87], a[116:119], v[144:147], v[84:87]\n"
    "ds_read_b128 a[168:171], v2 offset:26624\n"
    "v_cvt_pk_bf16_f32 v72, v72, v73\n"
    "v_mfma_f32_16x16x32_bf16 v[84:87], a[120:123], v[152:155], v[84:87]\n"
    "v_mfma_f32_16x16x32_bf16 v[84:87], a[124:127], v[160:163], v[84:87]\n"
    "ds_read_b128 a[172:175], v2 offset:27648\n"
    "v_cvt_pk_bf16_f32 v73, v74, v75\n"
    "v_mfma_f32_16x16x32_bf16 v[84:87], a[128:131], v[168:171], v[84:87]\n"
    "v_mfma_f32_16x16x32_bf16 v[84:87], a[132:135], v[176:179], v[84:87]\n"
    "ds_read_b128 a[176:179], v2 offset:28672\n"
    "v_mfma_f32_16x16x32_bf16 v[84:87], a[136:139], v[184:187], v[84:87]\n"
    "v_cvt_pk_bf16_f32 v74, v76, v77\n"
    "v_mfma_f32_16x16x32_bf16 v[84:87], a[140:143], v[24:27], v[84:87]\n"
    "ds_read_b128 a[180:183], v2 offset:29696\n"
    "v_mfma_f32_16x16x32_bf16 v[92:95], a[108:111], v[192:195], 0\n"
    "v_mfma_f32_16x16x32_bf16 v[92:95], a[112:115], v[200:203], v[92:95]\n"
    "ds_read_b128 a[184:187], v2 offset:30720\n"
    "v_cvt_pk_bf16_f32 v75, v78, v79\n"
    "v_mfma_f32_16x16x32_bf16 v[92:95], a[116:119], v[208:211], v[92:95]\n"
    "v_mfma_f32_16x16x32_bf16 v[92:95], a[120:123], v[216:219], v[92:95]\n"
    "ds_read_b128 a[188:191], v2 offset:31744\n"
    "s_add_u32 s87, s56, 22528\n"
    "v_mfma_f32_16x16x32_bf16 v[92:95], a[124:127], v[224:227], v[92:95]\n"
    "v_mfma_f32_16x16x32_bf16 v[92:95], a[128:131], v[232:235], v[92:95]\n"
    "ds_read_b128 a[64:67], v2 offset:32768\n"
    "v_mfma_f32_16x16x32_bf16 v[92:95], a[132:135], v[240:243], v[92:95]\n"
    "buffer_store_dwordx4 v[72:75], v35, s[48:51], s87 offen sc1\n"
    "v_mfma_f32_16x16x32_bf16 v[92:95], a[136:139], v[248:251], v[92:95]\n"
    "ds_read_b128 a[68:71], v2 offset:33792\n"
    "v_mfma_f32_16x16x32_bf16 v[92:95], a[140:143], v[28:31], v[92:95]\n"
    "s_waitcnt lgkmcnt(5)\n"
    "v_mfma_f32_16x16x32_bf16 v[96:99], a[144:147], v[128:131], 0\n"
    "ds_read_b128 a[72:75], v2 offset:34816\n"
    "v_mfma_f32_16x16x32_bf16 v[96:99], a[148:151], v[136:139], v[96:99]\n"
    "v_mfma_f32_16x16x32_bf16 v[96:99], a[152:155], v[144:147], v[96:99]\n"
    "ds_read_b128 a[76:79], v2 offset:35840\n"
    "v_mfma_f32_16x16x32_bf16 v[96:99], a[156:159], v[152:155], v[96:99]\n"
    "v_mfma_f32_16x16x32_bf16 v[96:99], a[160:163], v[160:163], v[96:99]\n"
    "ds_read_b128 a[80:83], v2 offset:36864\n"
    "v_cvt_pk_bf16_f32 v80, v80, v81\n"
    "v_mfma_f32_16x16x32_bf16 v[96:99], a[164:167], v[168:171], v[96:99]\n"
    "v_mfma_f32_16x16x32_bf16 v[96:99], a[168:171], v[176:179], v[96:99]\n"
    "ds_read_b128 a[84:87], v2 offset:37888\n"
    "v_cvt_pk_bf16_f32 v81, v82, v83\n"
    "v_mfma_f32_16x16x32_bf16 v[96:99], a[172:175], v[184:187], v[96:99]\n"
    "v_mfma_f32_16x16x32_bf16 v[96:99], a[176:179], v[24:27], v[96:99]\n"
    "ds_read_b128 a[88:91], v2 offset:38912\n"
    "v_mfma_f32_16x16x32_bf16 v[104:107], a[144:147], v[192:195], 0\n"
    "v_cvt_pk_bf16_f32 v82, v84, v85\n"
    "v_mfma_f32_16x16x32_bf16 v[104:107], a[148:151], v[200:203], v[104:107]\n"
    "ds_read_b128 a[92:95], v2 offset:39936\n"
    "v_mfma_f32_16x16x32_bf16 v[104:107], a[152:155], v[208:211], v[104:107]\n"
    "v_mfma_f32_16x16x32_bf16 v[104:107], a[156:159], v[216:219], v[104:107]\n"
    "ds_read_b128 a[96:99], v2 offset:40960\n"
    "v_cvt_pk_bf16_f32 v83, v86, v87\n"
    "v_mfma_f32_16x16x32_bf16 v[104:107], a[160:163], v[224:227], v[104:107]\n"
    "v_mfma_f32_16x16x32_bf16 v[104:107], a[164:167], v[232:235], v[104:107]\n"
    "ds_read_b128 a[100:103], v2 offset:41984\n"
    "s_add_u32 s87, s56, 9216\n"
    "v_mfma_f32_16x16x32_bf16 v[104:107], a[168:171], v[240:243], v[104:107]\n"
    "v_mfma_f32_16x16x32_bf16 v[104:107], a[172:175], v[248:251], v[104:107]\n"
    "ds_read_b128 a[104:107], v2 offset:43008\n"
    "v_mfma_f32_16x16x32_bf16 v[104:107], a[176:179], v[28:31], v[104:107]\n"
    "s_waitcnt lgkmcnt(5)\n"
    "v_mfma_f32_16x16x32_bf16 v[100:103], a[180:183], v[128:131], 0\n"
    "buffer_store_dwordx4 v[80:83], v35, s[48:51], s87 offen sc1\n"
    "v_mfma_f32_16x16x32_bf16 v[100:103], a[184:187], v[136:139], v[100:103]\n"
    "ds_read_b128 a[108:111], v2 offset:44032\n"
    "v_mfma_f32_16x16x32_bf16 v[100:103], a[188:191], v[144:147], v[100:103]\n"
    "ds_read_b128 a[112:115], v2 offset:45056\n"
    "v_cvt_pk_bf16_f32 v88, v88, v89\n"
    "v_mfma_f32_16x16x32_bf16 v[100:103], a[64:67], v[152:155], v[100:103]\n"
    "v_mfma_f32_16x16x32_bf16 v[100:103], a[68:71], v[160:163], v[100:103]\n"
    "ds_read_b128 a[116:119], v2 offset:46080\n"
    "v_cvt_pk_bf16_f32 v89, v90, v91\n"
    "v_mfma_f32_16x16x32_bf16 v[100:103], a[72:75], v[168:171], v[100:103]\n"
    "v_mfma_f32_16x16x32_bf16 v[100:103], a[76:79], v[176:179], v[100:103]\n"
    "ds_read_b128 a[120:123], v2 offset:47104\n"
    "v_mfma_f32_16x16x32_bf16 v[100:103], a[80:83], v[184:187], v[100:103]\n"
    "v_cvt_pk_bf16_f32 v90, v92, v93\n"
    "v_mfma_f32_16x16x32_bf16 v[100:103], a[84:87], v[24:27], v[100:103]\n"
    "ds_read_b128 a[124:127], v2 offset:48128\n"
    "v_mfma_f32_16x16x32_bf16 v[108:111], a[180:183], v[192:195], 0\n"
    "v_mfma_f32_16x16x32_bf16 v[108:111], a[184:187], v[200:203], v[108:111]\n"
    "ds_read_b128 a[128:131], v2 offset:49152\n"
    "v_cvt_pk_bf16_f32 v91, v94, v95\n"
    "v_mfma_f32_16x16x32_bf16 v[108:111], a[188:191], v[208:211], v[108:111]\n"
    "v_mfma_f32_16x16x32_bf16 v[108:111], a[64:67], v[216:219], v[108:111]\n"
    "ds_read_b128 a[132:135], v2 offset:50176\n"
    "s_add_u32 s87, s56, 23552\n"
    "v_mfma_f32_16x16x32_bf16 v[108:111], a[68:71], v[224:227], v[108:111]\n"
    "v_mfma_f32_16x16x32_bf16 v[108:111], a[72:75], v[232:235], v[108:111]\n"
    "ds_read_b128 a[136:139], v2 offset:51200\n"
    "v_mfma_f32_16x16x32_bf16 v[108:111], a[76:79], v[240:243], v[108:111]\n"
    "buffer_store_dwordx4 v[88:91], v35, s[48:51], s87 offen sc1\n"
    "v_mfma_f32_16x16x32_bf16 v[108:111], a[80:83], v[248:251], v[108:111]\n"
    "ds_read_b128 a[140:143], v2 offset:52224\n"
    "v_mfma_f32_16x16x32_bf16 v[108:111], a[84:87], v[28:31], v[108:111]\n"
    "s_waitcnt lgkmcnt(5)\n"
    "v_mfma_f32_16x16x32_bf16 v[112:115], a[88:91], v[128:131], 0\n"
    "ds_read_b128 a[144:147], v2 offset:53248\n"
    "v_mfma_f32_16x16x32_bf16 v[112:115], a[92:95], v[136:139], v[112:115]\n"
    "v_mfma_f32_16x16x32_bf16 v[112:115], a[96:99], v[144:147], v[112:115]\n"
    "ds_read_b128 a[148:151], v2 offset:54272\n"
    "v_mfma_f32_16x16x32_bf16 v[112:115], a[100:103], v[152:155], v[112:115]\n"
    "v_mfma_f32_16x16x32_bf16 v[112:115], a[104:107], v[160:163], v[112:115]\n"
    "ds_read_b128 a[152:155], v2 offset:55296\n"
    "v_cvt_pk_bf16_f32 v96, v96, v97\n"
    "v_mfma_f32_16x16x32_bf16 v[112:115], a[108:111], v[168:171], v[112:115]\n"
    "v_mfma_f32_16x16x32_bf16 v[112:115], a[112:115], v[176:179], v[112:115]\n"
    "ds_read_b128 a[156:159], v2 offset:56320\n"
    "v_cvt_pk_bf16_f32 v97, v98, v99\n"
    "v_mfma_f32_16x16x32_bf16 v[112:115], a[116:119], v[184:187], v[112:115]\n"
    "v_mfma_f32_16x16x32_bf16 v[112:115], a[120:123], v[24:27], v[112:115]\n"
    "ds_read_b128 a[160:163], v2 offset:57344\n"
    "v_mfma_f32_16x16x32_bf16 v[120:123], a[88:91], v[192:195], 0\n"
    "v_cvt_pk_bf16_f32 v98, v100, v101\n"
    "v_mfma_f32_16x16x32_bf16 v[120:123], a[92:95], v[200:203], v[120:123]\n"
    "ds_read_b128 a[164:167], v2 offset:58368\n"
    "v_mfma_f32_16x16x32_bf16 v[120:123], a[96:99], v[208:211], v[120:123]\n"
    "v_mfma_f32_16x16x32_bf16 v[120:123], a[100:103], v[216:219], v[120:123]\n"
    "ds_read_b128 a[168:171], v2 offset:59392\n"
    "v_cvt_pk_bf16_f32 v99, v102, v103\n"
    "v_mfma_f32_16x16x32_bf16 v[120:123], a[104:107], v[224:227], v[120:123]\n"
    "v_mfma_f32_16x16x32_bf16 v[120:123], a[108:111], v[232:235], v[120:123]\n"
    "ds_read_b128 a[172:175], v2 offset:60416\n"
    "s_add_u32 s87, s56, 10240\n"
    "v_mfma_f32_16x16x32_bf16 v[120:123], a[112:115], v[240:243], v[120:123]\n"
    "v_mfma_f32_16x16x32_bf16 v[120:123], a[116:119], v[248:251], v[120:123]\n"
    "ds_read_b128 a[176:179], v2 offset:61440\n"
    "v_mfma_f32_16x16x32_bf16 v[120:123], a[120:123], v[28:31], v[120:123]\n"
    "s_waitcnt lgkmcnt(5)\n"
    "v_mfma_f32_16x16x32_bf16 v[116:119], a[124:127], v[128:131], 0\n"
    "buffer_store_dwordx4 v[96:99], v35, s[48:51], s87 offen sc1\n"
    "v_mfma_f32_16x16x32_bf16 v[116:119], a[128:131], v[136:139], v[116:119]\n"
    "ds_read_b128 a[180:183], v2 offset:62464\n"
    "v_mfma_f32_16x16x32_bf16 v[116:119], a[132:135], v[144:147], v[116:119]\n"
    "ds_read_b128 a[184:187], v2 offset:63488\n"
    "v_cvt_pk_bf16_f32 v104, v104, v105\n"
    "v_mfma_f32_16x16x32_bf16 v[116:119], a[136:139], v[152:155], v[116:119]\n"
    "v_mfma_f32_16x16x32_bf16 v[116:119], a[140:143], v[160:163], v[116:119]\n"
    "ds_read_b128 a[188:191], v2 offset:64512\n"
    "v_cvt_pk_bf16_f32 v105, v106, v107\n"
    "v_mfma_f32_16x16x32_bf16 v[116:119], a[144:147], v[168:171], v[116:119]\n"
    "v_mfma_f32_16x16x32_bf16 v[116:119], a[148:151], v[176:179], v[116:119]\n"
    "ds_read_b128 a[64:67], v3 offset:0\n"
    "v_mfma_f32_16x16x32_bf16 v[116:119], a[152:155], v[184:187], v[116:119]\n"
    "v_cvt_pk_bf16_f32 v106, v108, v109\n"
    "v_mfma_f32_16x16x32_bf16 v[116:119], a[156:159], v[24:27], v[116:119]\n"
    "ds_read_b128 a[68:71], v3 offset:1024\n"
    "v_mfma_f32_16x16x32_bf16 v[124:127], a[124:127], v[192:195], 0\n"
    "v_mfma_f32_16x16x32_bf16 v[124:127], a[128:131], v[200:203], v[124:127]\n"
    "ds_read_b128 a[72:75], v3 offset:2048\n"
    "v_cvt_pk_bf16_f32 v107, v110, v111\n"
    "v_mfma_f32_16x16x32_bf16 v[124:127], a[132:135], v[208:211], v[124:127]\n"
    "v_mfma_f32_16x16x32_bf16 v[124:127], a[136:139], v[216:219], v[124:127]\n"
    "ds_read_b128 a[76:79], v3 offset:3072\n"
    "s_add_u32 s87, s56, 24576\n"
    "v_mfma_f32_16x16x32_bf16 v[124:127], a[140:143], v[224:227], v[124:127]\n"
    "v_mfma_f32_16x16x32_bf16 v[124:127], a[144:147], v[232:235], v[124:127]\n"
    "ds_read_b128 a[80:83], v3 offset:4096\n"
    "v_mfma_f32_16x16x32_bf16 v[124:127], a[148:151], v[240:243], v[124:127]\n"
    "buffer_store_dwordx4 v[104:107], v35, s[48:51], s87 offen sc1\n"
    "v_mfma_f32_16x16x32_bf16 v[124:127], a[152:155], v[248:251], v[124:127]\n"
    "ds_read_b128 a[84:87], v3 offset:5120\n"
    "v_mfma_f32_16x16x32_bf16 v[124:127], a[156:159], v[28:31], v[124:127]\n"
    "s_waitcnt lgkmcnt(5)\n"
    "v_mfma_f32_16x16x32_bf16 v[64:67], a[160:163], v[128:131], 0\n"
    "s_waitcnt vmcnt(6)\n"
    "s_barrier\n"
    "v_mfma_f32_16x16x32_bf16 v[64:67], a[164:167], v[136:139], v[64:67]\n"
    "buffer_store_dwordx2 v[60:61], v62, s[48:51], s52 offen sc1\n"
    "v_mfma_f32_16x16x32_bf16 v[64:67], a[168:171], v[144:147], v[64:67]\n"
    "ds_read_b128 a[88:91], v3 offset:6144\n"
    "s_add_u32 s52, s52, 128\n"
    "v_mfma_f32_16x16x32_bf16 v[64:67], a[172:175], v[152:155], v[64:67]\n"
    "ds_read_b128 a[92:95], v3 offset:7168\n"
    "v_mfma_f32_16x16x32_bf16 v[64:67], a[176:179], v[160:163], v[64:67]\n"
    "ds_read_b128 a[96:99], v3 offset:8192\n"
    "v_cvt_pk_bf16_f32 v112, v112, v113\n"
    "v_mfma_f32_16x16x32_bf16 v[64:67], a[180:183], v[168:171], v[64:67]\n"
    "v_mfma_f32_16x16x32_bf16 v[64:67], a[184:187], v[176:179], v[64:67]\n"
    "ds_read_b128 a[100:103], v3 offset:9216\n"
    "v_cvt_pk_bf16_f32 v113, v114, v115\n"
    "v_mfma_f32_16x16x32_bf16 v[64:67], a[188:191], v[184:187], v[64:67]\n"
    "v_mfma_f32_16x16x32_bf16 v[64:67], a[64:67], v[24:27], v[64:67]\n"
    "ds_read_b128 a[104:107], v3 offset:10240\n"
    "v_mfma_f32_16x16x32_bf16 v[72:75], a[160:163], v[192:195], 0\n"
    "v_cvt_pk_bf16_f32 v114, v116, v117\n"
    "v_mfma_f32_16x16x32_bf16 v[72:75], a[164:167], v[200:203], v[72:75]\n"
    "ds_read_b128 a[108:111], v3 offset:11264\n"
    "v_mfma_f32_16x16x32_bf16 v[72:75], a[168:171], v[208:211], v[72:75]\n"
    "v_mfma_f32_16x16x32_bf16 v[72:75], a[172:175], v[216:219], v[72:75]\n"
    "ds_read_b128 a[112:115], v3 offset:12288\n"
    "v_cvt_pk_bf16_f32 v115, v118, v119\n"
    "v_mfma_f32_16x16x32_bf16 v[72:75], a[176:179], v[224:227], v[72:75]\n"
    "v_mfma_f32_16x16x32_bf16 v[72:75], a[180:183], v[232:235], v[72:75]\n"
    "ds_read_b128 a[116:119], v3 offset:13312\n"
    "s_add_u32 s87, s56, 11264\n"
    "v_mfma_f32_16x16x32_bf16 v[72:75], a[184:187], v[240:243], v[72:75]\n"
    "v_mfma_f32_16x16x32_bf16 v[72:75], a[188:191], v[248:251], v[72:75]\n"
    "ds_read_b128 a[120:123], v3 offset:14336\n"
    "v_mfma_f32_16x16x32_bf16 v[72:75], a[64:67], v[28:31], v[72:75]\n"
    "s_waitcnt lgkmcnt(5)\n"
    "v_mfma_f32_16x16x32_bf16 v[68:71], a[68:71], v[128:131], 0\n"
    "buffer_store_dwordx4 v[112:115], v35, s[48:51], s87 offen sc1\n"
    "v_mfma_f32_16x16x32_bf16 v[68:71], a[72:75], v[136:139], v[68:71]\n"
    "ds_read_b128 a[124:127], v3 offset:15360\n"
    "v_mfma_f32_16x16x32_bf16 v[68:71], a[76:79], v[144:147], v[68:71]\n"
    "ds_read_b128 a[128:131], v3 offset:16384\n"
    "v_cvt_pk_bf16_f32 v120, v120, v121\n"
    "v_mfma_f32_16x16x32_bf16 v[68:71], a[80:83], v[152:155], v[68:71]\n"
    "v_mfma_f32_16x16x32_bf16 v[68:71], a[84:87], v[160:163], v[68:71]\n"
    "ds_read_b128 a[132:135], v3 offset:17408\n"
    "v_cvt_pk_bf16_f32 v121, v122, v123\n"
    "v_mfma_f32_16x16x32_bf16 v[68:71], a[88:91], v[168:171], v[68:71]\n"
    "v_mfma_f32_16x16x32_bf16 v[68:71], a[92:95], v[176:179], v[68:71]\n"
    "ds_read_b128 a[136:139], v3 offset:18432\n"
    "v_mfma_f32_16x16x32_bf16 v[68:71], a[96:99], v[184:187], v[68:71]\n"
    "v_cvt_pk_bf16_f32 v122, v124, v125\n"
    "v_mfma_f32_16x16x32_bf16 v[68:71], a[100:103], v[24:27], v[68:71]\n"
    "ds_read_b128 a[140:143], v3 offset:19456\n"
    "v_mfma_f32_16x16x32_bf16 v[76:79], a[68:71], v[192:195], 0\n"
    "v_mfma_f32_16x16x32_bf16 v[76:79], a[72:75], v[200:203], v[76:79]\n"
    "ds_read_b128 a[144:147], v3 offset:20480\n"
    "v_cvt_pk_bf16_f32 v123, v126, v127\n"
    "v_mfma_f32_16x16x32_bf16 v[76:79], a[76:79], v[208:211], v[76:79]\n"
    "v_mfma_f32_16x16x32_bf16 v[76:79], a[80:83], v[216:219], v[76:79]\n"
    "ds_read_b128 a[148:151], v3 offset:21504\n"
    "s_add_u32 s87, s56, 25600\n"
    "v_mfma_f32_16x16x32_bf16 v[76:79], a[84:87], v[224:227], v[76:79]\n"
    "v_mfma_f32_16x16x32_bf16 v[76:79], a[88:91], v[232:235], v[76:79]\n"
    "ds_read_b128 a[152:155], v3 offset:22528\n"
    "v_mfma_f32_16x16x32_bf16 v[76:79], a[92:95], v[240:243], v[76:79]\n"
    "buffer_store_dwordx4 v[120:123], v35, s[48:51], s87 offen sc1\n"
    "v_mfma_f32_16x16x32_bf16 v[76:79], a[96:99], v[248:251], v[76:79]\n"
    "ds_read_b128 a[156:159], v3 offset:23552\n"
    "v_mfma_f32_16x16x32_bf16 v[76:79], a[100:103], v[28:31], v[76:79]\n"
    "s_waitcnt lgkmcnt(5)\n"
    "v_mfma_f32_16x16x32_bf16 v[80:83], a[104:107], v[128:131], 0\n"
    "ds_read_b128 a[160:163], v3 offset:24576\n"
    "v_mfma_f32_16x16x32_bf16 v[80:83], a[108:111], v[136:139], v[80:83]\n"
    "v_mfma_f32_16x16x32_bf16 v[80:83], a[112:115], v[144:147], v[80:83]\n"
    "ds_read_b128 a[164:167], v3 offset:25600\n"
    "v_mfma_f32_16x16x32_bf16 v[80:83], a[116:119], v[152:155], v[80:83]\n"
    "v_mfma_f32_16x16x32_bf16 v[80:83], a[120:123], v[160:163], v[80:83]\n"
    "ds_read_b128 a[168:171], v3 offset:26624\n"
    "v_cvt_pk_bf16_f32 v64, v64, v65\n"
    "v_mfma_f32_16x16x32_bf16 v[80:83], a[124:127], v[168:171], v[80:83]\n"
    "v_mfma_f32_16x16x32_bf16 v[80:83], a[128:131], v[176:179], v[80:83]\n"
    "ds_read_b128 a[172:175], v3 offset:27648\n"
    "v_cvt_pk_bf16_f32 v65, v66, v67\n"
    "v_mfma_f32_16x16x32_bf16 v[80:83], a[132:135], v[184:187], v[80:83]\n"
    "v_mfma_f32_16x16x32_bf16 v[80:83], a[136:139], v[24:27], v[80:83]\n"
    "v_mfma_f32_16x16x32_bf16 v[88:91], a[104:107], v[192:195], 0\n"
    "v_cvt_pk_bf16_f32 v66, v68, v69\n"
    "v_mfma_f32_16x16x32_bf16 v[88:91], a[108:111], v[200:203], v[88:91]\n"
    "v_mfma_f32_16x16x32_bf16 v[88:91], a[112:115], v[208:211], v[88:91]\n"
    "v_mfma_f32_16x16x32_bf16 v[88:91], a[116:119], v[216:219], v[88:91]\n"
    "v_cvt_pk_bf16_f32 v67, v70, v71\n"
    "v_mfma_f32_16x16x32_bf16 v[88:91], a[120:123], v[224:227], v[88:91]\n"
    "v_mfma_f32_16x16x32_bf16 v[88:91], a[124:127], v[232:235], v[88:91]\n"
    "s_add_u32 s87, s56, 12288\n"
    "v_mfma_f32_16x16x32_bf16 v[88:91], a[128:131], v[240:243], v[88:91]\n"
    "v_mfma_f32_16x16x32_bf16 v[88:91], a[132:135], v[248:251], v[88:91]\n"
    "v_mfma_f32_16x16x32_bf16 v[88:91], a[136:139], v[28:31], v[88:91]\n"
    "s_waitcnt lgkmcnt(0)\n"
    "v_mfma_f32_16x16x32_bf16 v[84:87], a[140:143], v[128:131], 0\n"
    "buffer_store_dwordx4 v[64:67], v35, s[48:51], s87 offen sc1\n"
    "v_mfma_f32_16x16x32_bf16 v[84:87], a[144:147], v[136:139], v[84:87]\n"
    "v_mfma_f32_16x16x32_bf16 v[84:87], a[148:151], v[144:147], v[84:87]\n"
    "v_cvt_pk_bf16_f32 v72, v72, v73\n"
    "v_mfma_f32_16x16x32_bf16 v[84:87], a[152:155], v[152:155], v[84:87]\n"
    "v_mfma_f32_16x16x32_bf16 v[84:87], a[156:159], v[160:163], v[84:87]\n"
    "v_cvt_pk_bf16_f32 v73, v74, v75\n"
    "v_mfma_f32_16x16x32_bf16 v[84:87], a[160:163], v[168:171], v[84:87]\n"
    "v_mfma_f32_16x16x32_bf16 v[84:87], a[164:167], v[176:179], v[84:87]\n"
    "buffer_load_dwordx2 v[18:19], v16, s[48:51], s53 offen sc1\n"
    "v_mfma_f32_16x16x32_bf16 v[84:87], a[168:171], v[184:187], v[84:87]\n"
    "v_cvt_pk_bf16_f32 v74, v76, v77\n"
    "v_mfma_f32_16x16x32_bf16 v[84:87], a[172:175], v[24:27], v[84:87]\n"
    "v_mfma_f32_16x16x32_bf16 v[92:95], a[140:143], v[192:195], 0\n"
    "v_mfma_f32_16x16x32_bf16 v[92:95], a[144:147], v[200:203], v[92:95]\n"
    "v_cvt_pk_bf16_f32 v75, v78, v79\n"
    "v_mfma_f32_16x16x32_bf16 v[92:95], a[148:151], v[208:211], v[92:95]\n"
    "v_mfma_f32_16x16x32_bf16 v[92:95], a[152:155], v[216:219], v[92:95]\n"
    "s_add_u32 s87, s56, 26624\n"
    "v_mfma_f32_16x16x32_bf16 v[92:95], a[156:159], v[224:227], v[92:95]\n"
    "v_mfma_f32_16x16x32_bf16 v[92:95], a[160:163], v[232:235], v[92:95]\n"
    "v_mfma_f32_16x16x32_bf16 v[92:95], a[164:167], v[240:243], v[92:95]\n"
    "buffer_store_dwordx4 v[72:75], v35, s[48:51], s87 offen sc1\n"
    "v_mfma_f32_16x16x32_bf16 v[92:95], a[168:171], v[248:251], v[92:95]\n"
    "v_mfma_f32_16x16x32_bf16 v[92:95], a[172:175], v[28:31], v[92:95]\n"
    "s_nop 7\n"
    "v_cvt_pk_bf16_f32 v80, v80, v81\n"
    "v_cvt_pk_bf16_f32 v81, v82, v83\n"
    "v_cvt_pk_bf16_f32 v82, v84, v85\n"
    "v_cvt_pk_bf16_f32 v83, v86, v87\n"
    "s_add_u32 s87, s56, 13312\n"
    "buffer_store_dwordx4 v[80:83], v35, s[48:51], s87 offen sc1\n"
    "v_cvt_pk_bf16_f32 v88, v88, v89\n"
    "v_cvt_pk_bf16_f32 v89, v90, v91\n"
    "v_cvt_pk_bf16_f32 v90, v92, v93\n"
    "v_cvt_pk_bf16_f32 v91, v94, v95\n"
    "s_add_u32 s87, s56, 27648\n"
    "buffer_store_dwordx4 v[88:91], v35, s[48:51], s87 offen sc1\n"
    "s_waitcnt vmcnt(3)\n"
    "v_cmp_eq_u64 vcc, s[16:17], v[18:19]\n"
    "s_nop 1\n"
    "s_cmp_eq_u32 s62, 3\n"
    "s_cselect_b32 s82, 0xffff, -1\n"
    "s_cselect_b32 s80, 0, vcc_lo\n"
    "s_and_b32 s87, s80, 0xff\n"
    "s_cmp_eq_u32 s87, 0xff\n"
    "s_cbranch_scc0 .Lr0_no\n"
    "s_add_u32 s87, s54, 0\n"
    "buffer_load_dwordx4 v[128:131], v1, s[48:51], s87 offen sc1\n"
    "s_add_u32 s87, s54, 1835008\n"
    "buffer_load_dwordx4 v[132:135], v1, s[48:51], s87 offen sc1\n"
    "s_add_u32 s87, s54, 3670016\n"
    "buffer_load_dwordx4 v[136:139], v1, s[48:51], s87 offen sc1\n"
    "s_add_u32 s87, s54, 5505024\n"
    "buffer_load_dwordx4 v[140:143], v1, s[48:51], s87 offen sc1\n"
    "s_add_u32 s87, s54, 7340032\n"
    "buffer_load_dwordx4 v[144:147], v1, s[48:51], s87 offen sc1\n"
    "s_add_u32 s87, s54, 9175040\n"
    "buffer_load_dwordx4 v[148:151], v1, s[48:51], s87 offen sc1\n"
    "s_add_u32 s87, s54, 11010048\n"
    "buffer_load_dwordx4 v[152:155], v1, s[48:51], s87 offen sc1\n"
    "s_add_u32 s87, s54, 12845056\n"
    "buffer_load_dwordx4 v[156:159], v1, s[48:51], s87 offen sc1\n"
    "s_mov_b32 s81, 0xff\n"
    "s_waitcnt vmcnt(8)\n"
    "s_branch .Lr0_d\n"
    ".Lr0_no:\n"
    "s_waitcnt vmcnt(0)\n"
    ".Lr0_d:\n"
    "s_barrier\n"
    "buffer_store_dwordx2 v[60:61], v62, s[48:51], s52 offen sc1\n"
    ".Lr_loop:\n"
    "s_and_b32 s80, s80, s82\n"
    "s_andn2_b32 s80, s80, s81\n"
    "s_or_b32 s81, s81, s80\n"
    "s_and_b32 s87, s80, 65535\n"
    "s_cbranch_scc0 .Lr_h0\n"
    "s_bitcmp1_b32 s80, 0\n"
    "s_cbranch_scc0 .Lr_s0_0\n"
    "s_add_u32 s87, s54, 0\n"
    "buffer_load_dwordx4 v[128:131], v1, s[48:51], s87 offen sc1\n"
    ".Lr_s0_0:\n"
    "s_bitcmp1_b32 s80, 1\n"
    "s_cbranch_scc0 .Lr_s0_1\n"
    "s_add_u32 s87, s54, 1835008\n"
    "buffer_load_dwordx4 v[132:135], v1, s[48:51], s87 offen sc1\n"
    ".Lr_s0_1:\n"
    "s_bitcmp1_b32 s80, 2\n"
    "s_cbranch_scc0 .Lr_s0_2\n"
    "s_add_u32 s87, s54, 3670016\n"
    "buffer_load_dwordx4 v[136:139], v1, s[48:51], s87 offen sc1\n"
    ".Lr_s0_2:\n"
    "s_bitcmp1_b32 s80, 3\n"
    "s_cbranch_scc0 .Lr_s0_3\n"
    "s_add_u32 s87, s54, 5505024\n"
    "buffer_load_dwordx4 v[140:143], v1, s[48:51], s87 offen sc1\n"
    ".Lr_s0_3:\n"
    "s_bitcmp1_b32 s80, 4\n"
    "s_cbranch_scc0 .Lr_s0_4\n"
    "s_add_u32 s87, s54, 7340032\n"
    "buffer_load_dwordx4 v[144:147], v1, s[48:51], s87 offen sc1\n"
    ".Lr_s0_4:\n"
    "s_bitcmp1_b32 s80, 5\n"
    "s_cbranch_scc0 .Lr_s0_5\n"
    "s_add_u32 s87, s54, 9175040\n"
    "buffer_load_dwordx4 v[148:151], v1, s[48:51], s87 offen sc1\n"
    ".Lr_s0_5:\n"
    "s_bitcmp1_b32 s80, 6\n"
    "s_cbranch_scc0 .Lr_s0_6\n"
    "s_add_u32 s87, s54, 11010048\n"
    "buffer_load_dwordx4 v[152:155], v1, s[48:51], s87 offen sc1\n"
    ".Lr_s0_6:\n"
    "s_bitcmp1_b32 s80, 7\n"
    "s_cbranch_scc0 .Lr_s0_7\n"
    "s_add_u32 s87, s54, 12845056\n"
    "buffer_load_dwordx4 v[156:159], v1, s[48:51], s87 offen sc1\n"
    ".Lr_s0_7:\n"
    "s_bitcmp1_b32 s80, 8\n"
    "s_cbranch_scc0 .Lr_s0_8\n"
    "s_add_u32 s87, s54, 14680064\n"
    "buffer_load_dwordx4 v[160:163], v1, s[48:51], s87 offen sc1\n"
    ".Lr_s0_8:\n"
    "s_bitcmp1_b32 s80, 9\n"
    "s_cbranch_scc0 .Lr_s0_9\n"
    "s_add_u32 s87, s54, 16515072\n"
    "buffer_load_dwordx4 v[164:167], v1, s[48:51], s87 offen sc1\n"
    ".Lr_s0_9:\n"
    "s_bitcmp1_b32 s80, 10\n"
    "s_cbranch_scc0 .Lr_s0_10\n"
    "s_add_u32 s87, s54, 18350080\n"
    "buffer_load_dwordx4 v[168:171], v1, s[48:51], s87 offen sc1\n"
    ".Lr_s0_10:\n"
    "s_bitcmp1_b32 s80, 11\n"
    "s_cbranch_scc0 .Lr_s0_11\n"
    "s_add_u32 s87, s54, 20185088\n"
    "buffer_load_dwordx4 v[172:175], v1, s[48:51], s87 offen sc1\n"
    ".Lr_s0_11:\n"
    "s_bitcmp1_b32 s80, 12\n"
    "s_cbranch_scc0 .Lr_s0_12\n"
    "s_add_u32 s87, s54, 22020096\n"
    "buffer_load_dwordx4 v[176:179], v1, s[48:51], s87 offen sc1\n"
    ".Lr_s0_12:\n"
    "s_bitcmp1_b32 s80, 13\n"
    "s_cbranch_scc0 .Lr_s0_13\n"
    "s_add_u32 s87, s54, 23855104\n"
    "buffer_load_dwordx4 v[180:183], v1, s[48:51], s87 offen sc1\n"
    ".Lr_s0_13:\n"
    "s_bitcmp1_b32 s80, 14\n"
    "s_cbranch_scc0 .Lr_s0_14\n"
    "s_add_u32 s87, s54, 25690112\n"
    "buffer_load_dwordx4 v[184:187], v1, s[48:51], s87 offen sc1\n"
    ".Lr_s0_14:\n"
    "s_bitcmp1_b32 s80, 15\n"
    "s_cbranch_scc0 .Lr_s0_15\n"
    "s_add_u32 s87, s54, 27525120\n"
    "buffer_load_dwordx4 v[188:191], v1, s[48:51], s87 offen sc1\n"
    ".Lr_s0_15:\n"
    ".Lr_h0:\n"
    "s_and_b32 s87, s80, 4294901760\n"
    "s_cbranch_scc0 .Lr_h1\n"
    "s_bitcmp1_b32 s80, 16\n"
    "s_cbranch_scc0 .Lr_s1_0\n"
    "s_add_u32 s87, s54, 8192\n"
    "buffer_load_dwordx4 v[192:195], v1, s[48:51], s87 offen sc1\n"
    ".Lr_s1_0:\n"
    "s_bitcmp1_b32 s80, 17\n"
    "s_cbranch_scc0 .Lr_s1_1\n"
    "s_add_u32 s87, s54, 1843200\n"
    "buffer_load_dwordx4 v[196:199], v1, s[48:51], s87 offen sc1\n"
    ".Lr_s1_1:\n"
    "s_bitcmp1_b32 s80, 18\n"
    "s_cbranch_scc0 .Lr_s1_2\n"
    "s_add_u32 s87, s54, 3678208\n"
    "buffer_load_dwordx4 v[200:203], v1, s[48:51], s87 offen sc1\n"
    ".Lr_s1_2:\n"
    "s_bitcmp1_b32 s80, 19\n"
    "s_cbranch_scc0 .Lr_s1_3\n"
    "s_add_u32 s87, s54, 5513216\n"
    "buffer_load_dwordx4 v[204:207], v1, s[48:51], s87 offen sc1\n"
    ".Lr_s1_3:\n"
    "s_bitcmp1_b32 s80, 20\n"
    "s_cbranch_scc0 .Lr_s1_4\n"
    "s_add_u32 s87, s54, 7348224\n"
    "buffer_load_dwordx4 v[208:211], v1, s[48:51], s87 offen sc1\n"
    ".Lr_s1_4:\n"
    "s_bitcmp1_b32 s80, 21\n"
    "s_cbranch_scc0 .Lr_s1_5\n"
    "s_add_u32 s87, s54, 9183232\n"
    "buffer_load_dwordx4 v[212:215], v1, s[48:51], s87 offen sc1\n"
    ".Lr_s1_5:\n"
    "s_bitcmp1_b32 s80, 22\n"
    "s_cbranch_scc0 .Lr_s1_6\n"
    "s_add_u32 s87, s54, 11018240\n"
    "buffer_load_dwordx4 v[216:219], v1, s[48:51], s87 offen sc1\n"
    ".Lr_s1_6:\n"
    "s_bitcmp1_b32 s80, 23\n"
    "s_cbranch_scc0 .Lr_s1_7\n"
    "s_add_u32 s87, s54, 12853248\n"
    "buffer_load_dwordx4 v[220:223], v1, s[48:51], s87 offen sc1\n"
    ".Lr_s1_7:\n"
    "s_bitcmp1_b32 s80, 24\n"
    "s_cbranch_scc0 .Lr_s1_8\n"
    "s_add_u32 s87, s54, 14688256\n"
    "buffer_load_dwordx4 v[224:227], v1, s[48:51], s87 offen sc1\n"
    ".Lr_s1_8:\n"
    "s_bitcmp1_b32 s80, 25\n"
    "s_cbranch_scc0 .Lr_s1_9\n"
    "s_add_u32 s87, s54, 16523264\n"
    "buffer_load_dwordx4 v[228:231], v1, s[48:51], s87 offen sc1\n"
    ".Lr_s1_9:\n"
    "s_bitcmp1_b32 s80, 26\n"
    "s_cbranch_scc0 .Lr_s1_10\n"
    "s_add_u32 s87, s54, 18358272\n"
    "buffer_load_dwordx4 v[232:235], v1, s[48:51], s87 offen sc1\n"
    ".Lr_s1_10:\n"
    "s_bitcmp1_b32 s80, 27\n"
    "s_cbranch_scc0 .Lr_s1_11\n"
    "s_add_u32 s87, s54, 20193280\n"
    "buffer_load_dwordx4 v[236:239], v1, s[48:51], s87 offen sc1\n"
    ".Lr_s1_11:\n"
    "s_bitcmp1_b32 s80, 28\n"
    "s_cbranch_scc0 .Lr_s1_12\n"
    "s_add_u32 s87, s54, 22028288\n"
    "buffer_load_dwordx4 v[240:243], v1, s[48:51], s87 offen sc1\n"
    ".Lr_s1_12:\n"
    "s_bitcmp1_b32 s80, 29\n"
    "s_cbranch_scc0 .Lr_s1_13\n"
    "s_add_u32 s87, s54, 23863296\n"
    "buffer_load_dwordx4 v[244:247], v1, s[48:51], s87 offen sc1\n"
    ".Lr_s1_13:\n"
    "s_bitcmp1_b32 s80, 30\n"
    "s_cbranch_scc0 .Lr_s1_14\n"
    "s_add_u32 s87, s54, 25698304\n"
    "buffer_load_dwordx4 v[248:251], v1, s[48:51], s87 offen sc1\n"
    ".Lr_s1_14:\n"
    "s_bitcmp1_b32 s80, 31\n"
    "s_cbranch_scc0 .Lr_s1_15\n"
    "s_add_u32 s87, s54, 27533312\n"
    "buffer_load_dwordx4 v[252:255], v1, s[48:51], s87 offen sc1\n"
    ".Lr_s1_15:\n"
    ".Lr_h1:\n"
    "s_cmp_eq_u32 s83, -1\n"
    "s_cbranch_scc1 .Lr_fin\n"
    "s_cmp_eq_u32 s86, 0\n"
    "s_cbranch_scc1 .Lr_ns\n"
    "s_and_b32 s87, s81, 0xffff\n"
    "s_or_b32 s87, s87, s84\n"
    "s_cmp_eq_u32 s87, 0xffff\n"
    "s_cbranch_scc0 .Lr_ns\n"
    "s_waitcnt vmcnt(0)\n"
    ".Lr_s0b:\n"
    "s_mov_b32 s84, 0x10000\n"
    "v_mov_b64 v[32:33], 0\n"
    "v_mov_b64 v[34:35], 0\n"
    "v_mov_b64 v[36:37], 0\n"
    "v_mov_b64 v[38:39], 0\n"
    "v_dot2_f32_bf16 v32, v128, v12, v32\n"
    "v_dot2_f32_bf16 v33, v128, v13, v33\n"
    "v_dot2_f32_bf16 v34, v129, v12, v34\n"
    "v_dot2_f32_bf16 v35, v129, v13, v35\n"
    "v_dot2_f32_bf16 v36, v130, v12, v36\n"
    "v_dot2_f32_bf16 v37, v130, v13, v37\n"
    "v_dot2_f32_bf16 v38, v131, v12, v38\n"
    "v_dot2_f32_bf16 v39, v131, v13, v39\n"
    "v_dot2_f32_bf16 v32, v132, v12, v32\n"
    "v_dot2_f32_bf16 v33, v132, v13, v33\n"
    "v_dot2_f32_bf16 v34, v133, v12, v34\n"
    "v_dot2_f32_bf16 v35, v133, v13, v35\n"
    "v_dot2_f32_bf16 v36, v134, v12, v36\n"
    "v_dot2_f32_bf16 v37, v134, v13, v37\n"
    "v_dot2_f32_bf16 v38, v135, v12, v38\n"
    "v_dot2_f32_bf16 v39, v135, v13, v39\n"
    "v_dot2_f32_bf16 v32, v136, v12, v32\n"
    "v_dot2_f32_bf16 v33, v136, v13, v33\n"
    "v_dot2_f32_bf16 v34, v137, v12, v34\n"
    "v_dot2_f32_bf16 v35, v137, v13, v35\n"
    "v_dot2_f32_bf16 v36, v138, v12, v36\n"
    "v_dot2_f32_bf16 v37, v138, v13, v37\n"
    "v_dot2_f32_bf16 v38, v139, v12, v38\n"
    "v_dot2_f32_bf16 v39, v139, v13, v39\n"
    "v_dot2_f32_bf16 v32, v140, v12, v32\n"
    "v_dot2_f32_bf16 v33, v140, v13, v33\n"
    "v_dot2_f32_bf16 v34, v141, v12, v34\n"
    "v_dot2_f32_bf16 v35, v141, v13, v35\n"
    "v_dot2_f32_bf16 v36, v142, v12, v36\n"
    "v_dot2_f32_bf16 v37, v142, v13, v37\n"
    "v_dot2_f32_bf16 v38, v143, v12, v38\n"
    "v_dot2_f32_bf16 v39, v143, v13, v39\n"
    "v_dot2_f32_bf16 v32, v144, v12, v32\n"
    "v_dot2_f32_bf16 v33, v144, v13, v33\n"
    "v_dot2_f32_bf16 v34, v145, v12, v34\n"
    "v_dot2_f32_bf16 v35, v145, v13, v35\n"
    "v_dot2_f32_bf16 v36, v146, v12, v36\n"
    "v_dot2_f32_bf16 v37, v146, v13, v37\n"
    "v_dot2_f32_bf16 v38, v147, v12, v38\n"
    "v_dot2_f32_bf16 v39, v147, v13, v39\n"
    "v_dot2_f32_bf16 v32, v148, v12, v32\n"
    "v_dot2_f32_bf16 v33, v148, v13, v33\n"
    "v_dot2_f32_bf16 v34, v149, v12, v34\n"
    "v_dot2_f32_bf16 v35, v149, v13, v35\n"
    "v_dot2_f32_bf16 v36, v150, v12, v36\n"
    "v_dot2_f32_bf16 v37, v150, v13, v37\n"
    "v_dot2_f32_bf16 v38, v151, v12, v38\n"
    "v_dot2_f32_bf16 v39, v151, v13, v39\n"
    "v_dot2_f32_bf16 v32, v152, v12, v32\n"
    "v_dot2_f32_bf16 v33, v152, v13, v33\n"
    "v_dot2_f32_bf16 v34, v153, v12, v34\n"
    "v_dot2_f32_bf16 v35, v153, v13, v35\n"
    "v_dot2_f32_bf16 v36, v154, v12, v36\n"
    "v_dot2_f32_bf16 v37, v154, v13, v37\n"
    "v_dot2_f32_bf16 v38, v155, v12, v38\n"
    "v_dot2_f32_bf16 v39, v155, v13, v39\n"
    "v_dot2_f32_bf16 v32, v156, v12, v32\n"
    "v_dot2_f32_bf16 v33, v156, v13, v33\n"
    "v_dot2_f32_bf16 v34, v157, v12, v34\n"
    "v_dot2_f32_bf16 v35, v157, v13, v35\n"
    "v_dot2_f32_bf16 v36, v158, v12, v36\n"
    "v_dot2_f32_bf16 v37, v158, v13, v37\n"
    "v_dot2_f32_bf16 v38, v159, v12, v38\n"
    "v_dot2_f32_bf16 v39, v159, v13, v39\n"
    "v_dot2_f32_bf16 v32, v160, v12, v32\n"
    "v_dot2_f32_bf16 v33, v160, v13, v33\n"
    "v_dot2_f32_bf16 v34, v161, v12, v34\n"
    "v_dot2_f32_bf16 v35, v161, v13, v35\n"
    "v_dot2_f32_bf16 v36, v162, v12, v36\n"
    "v_dot2_f32_bf16 v37, v162, v13, v37\n"
    "v_dot2_f32_bf16 v38, v163, v12, v38\n"
    "v_dot2_f32_bf16 v39, v163, v13, v39\n"
    "v_dot2_f32_bf16 v32, v164, v12, v32\n"
    "v_dot2_f32_bf16 v33, v164, v13, v33\n"
    "v_dot2_f32_bf16 v34, v165, v12, v34\n"
    "v_dot2_f32_bf16 v35, v165, v13, v35\n"
    "v_dot2_f32_bf16 v36, v166, v12, v36\n"
    "v_dot2_f32_bf16 v37, v166, v13, v37\n"
    "v_dot2_f32_bf16 v38, v167, v12, v38\n"
    "v_dot2_f32_bf16 v39, v167, v13, v39\n"
    "v_dot2_f32_bf16 v32, v168, v12, v32\n"
    "v_dot2_f32_bf16 v33, v168, v13, v33\n"
    "v_dot2_f32_bf16 v34, v169, v12, v34\n"
    "v_dot2_f32_bf16 v35, v169, v13, v35\n"
    "v_dot2_f32_bf16 v36, v170, v12, v36\n"
    "v_dot2_f32_bf16 v37, v170, v13, v37\n"
    "v_dot2_f32_bf16 v38, v171, v12, v38\n"
    "v_dot2_f32_bf16 v39, v171, v13, v39\n"
    "v_dot2_f32_bf16 v32, v172, v12, v32\n"
    "v_dot2_f32_bf16 v33, v172, v13, v33\n"
    "v_dot2_f32_bf16 v34, v173, v12, v34\n"
    "v_dot2_f32_bf16 v35, v173, v13, v35\n"
    "v_dot2_f32_bf16 v36, v174, v12, v36\n"
    "v_dot2_f32_bf16 v37, v174, v13, v37\n"
    "v_dot2_f32_bf16 v38, v175, v12, v38\n"
    "v_dot2_f32_bf16 v39, v175, v13, v39\n"
    "v_dot2_f32_bf16 v32, v176, v12, v32\n"
    "v_dot2_f32_bf16 v33, v176, v13, v33\n"
    "v_dot2_f32_bf16 v34, v177, v12, v34\n"
    "v_dot2_f32_bf16 v35, v177, v13, v35\n"
    "v_dot2_f32_bf16 v36, v178, v12, v36\n"
    "v_dot2_f32_bf16 v37, v178, v13, v37\n"
    "v_dot2_f32_bf16 v38, v179, v12, v38\n"
    "v_dot2_f32_bf16 v39, v179, v13, v39\n"
    "v_dot2_f32_bf16 v32, v180, v12, v32\n"
    "v_dot2_f32_bf16 v33, v180, v13, v33\n"
    "v_dot2_f32_bf16 v34, v181, v12, v34\n"
    "v_dot2_f32_bf16 v35, v181, v13, v35\n"
    "v_dot2_f32_bf16 v36, v182, v12, v36\n"
    "v_dot2_f32_bf16 v37, v182, v13, v37\n"
    "v_dot2_f32_bf16 v38, v183, v12, v38\n"
    "v_dot2_f32_bf16 v39, v183, v13, v39\n"
    "v_dot2_f32_bf16 v32, v184, v12, v32\n"
    "v_dot2_f32_bf16 v33, v184, v13, v33\n"
    "v_dot2_f32_bf16 v34, v185, v12, v34\n"
    "v_dot2_f32_bf16 v35, v185, v13, v35\n"
    "v_dot2_f32_bf16 v36, v186, v12, v36\n"
    "v_dot2_f32_bf16 v37, v186, v13, v37\n"
    "v_dot2_f32_bf16 v38, v187, v12, v38\n"
    "v_dot2_f32_bf16 v39, v187, v13, v39\n"
    "v_dot2_f32_bf16 v32, v188, v12, v32\n"
    "v_dot2_f32_bf16 v33, v188, v13, v33\n"
    "v_dot2_f32_bf16 v34, v189, v12, v34\n"
    "v_dot2_f32_bf16 v35, v189, v13, v35\n"
    "v_dot2_f32_bf16 v36, v190, v12, v36\n"
    "v_dot2_f32_bf16 v37, v190, v13, v37\n"
    "v_dot2_f32_bf16 v38, v191, v12, v38\n"
    "v_dot2_f32_bf16 v39, v191, v13, v39\n"
    "s_nop 2\n"
    "v_cvt_pk_bf16_f32 v40, v32, v33\n"
    "v_cvt_pk_bf16_f32 v41, v34, v35\n"
    "v_cvt_pk_bf16_f32 v42, v36, v37\n"
    "v_cvt_pk_bf16_f32 v43, v38, v39\n"
    "buffer_store_dwordx4 v[40:43], v20, s[36:39], s57 offen offset:0 sc1\n"
    "s_cmp_eq_u32 s83, -1\n"
    "s_cbranch_scc1 .Lr_f2\n"
    ".Lr_ns:\n"
    "s_add_u32 s86, s86, 1\n"
    "v_xor_b32 v21, 4, v21\n"
    "s_cmp_lg_u32 s62, 3\n"
    "s_cbranch_scc1 .Lr_np\n"
    "s_sleep 6\n"
    "buffer_load_dwordx2 v[18:19], v16, s[48:51], s53 offen sc1\n"
    "s_waitcnt vmcnt(0)\n"
    "v_cmp_eq_u64 vcc, s[16:17], v[18:19]\n"
    "s_nop 1\n"
    "s_cmp_gt_u32 s86, 0x40000\n"
    "s_cselect_b32 s87, -1, vcc_lo\n"
    "v_mov_b32 v18, s87\n"
    "ds_write_b32 v21, v18\n"
    "s_waitcnt lgkmcnt(0)\n"
    ".Lr_np:\n"
    "s_barrier\n"
    "ds_read_b32 v18, v21\n"
    "s_waitcnt lgkmcnt(0)\n"
    "v_readfirstlane_b32 s83, v18\n"
    "s_nop 1\n"
    "s_mov_b32 s80, s83\n"
    "s_branch .Lr_loop\n"
    ".Lr_fin:\n"
    "s_waitcnt vmcnt(0)\n"
    "s_cmp_eq_u32 s84, 0\n"
    "s_cbranch_scc1 .Lr_s0b\n"
    ".Lr_f2:\n"
    "s_bitcmp1_b32 s82, 16\n"
    "s_cbranch_scc0 .Lr_f3\n"
    "v_mov_b64 v[32:33], 0\n"
    "v_mov_b64 v[34:35], 0\n"
    "v_mov_b64 v[36:37], 0\n"
    "v_mov_b64 v[38:39], 0\n"
    "v_dot2_f32_bf16 v32, v192, v12, v32\n"
    "v_dot2_f32_bf16 v33, v192, v13, v33\n"
    "v_dot2_f32_bf16 v34, v193, v12, v34\n"
    "v_dot2_f32_bf16 v35, v193, v13, v35\n"
    "v_dot2_f32_bf16 v36, v194, v12, v36\n"
    "v_dot2_f32_bf16 v37, v194, v13, v37\n"
    "v_dot2_f32_bf16 v38, v195, v12, v38\n"
    "v_dot2_f32_bf16 v39, v195, v13, v39\n"
    "v_dot2_f32_bf16 v32, v196, v12, v32\n"
    "v_dot2_f32_bf16 v33, v196, v13, v33\n"
    "v_dot2_f32_bf16 v34, v197, v12, v34\n"
    "v_dot2_f32_bf16 v35, v197, v13, v35\n"
    "v_dot2_f32_bf16 v36, v198, v12, v36\n"
    "v_dot2_f32_bf16 v37, v198, v13, v37\n"
    "v_dot2_f32_bf16 v38, v199, v12, v38\n"
    "v_dot2_f32_bf16 v39, v199, v13, v39\n"
    "v_dot2_f32_bf16 v32, v200, v12, v32\n"
    "v_dot2_f32_bf16 v33, v200, v13, v33\n"
    "v_dot2_f32_bf16 v34, v201, v12, v34\n"
    "v_dot2_f32_bf16 v35, v201, v13, v35\n"
    "v_dot2_f32_bf16 v36, v202, v12, v36\n"
    "v_dot2_f32_bf16 v37, v202, v13, v37\n"
    "v_dot2_f32_bf16 v38, v203, v12, v38\n"
    "v_dot2_f32_bf16 v39, v203, v13, v39\n"
    "v_dot2_f32_bf16 v32, v204, v12, v32\n"
    "v_dot2_f32_bf16 v33, v204, v13, v33\n"
    "v_dot2_f32_bf16 v34, v205, v12, v34\n"
    "v_dot2_f32_bf16 v35, v205, v13, v35\n"
    "v_dot2_f32_bf16 v36, v206, v12, v36\n"
    "v_dot2_f32_bf16 v37, v206, v13, v37\n"
    "v_dot2_f32_bf16 v38, v207, v12, v38\n"
    "v_dot2_f32_bf16 v39, v207, v13, v39\n"
    "v_dot2_f32_bf16 v32, v208, v12, v32\n"
    "v_dot2_f32_bf16 v33, v208, v13, v33\n"
    "v_dot2_f32_bf16 v34, v209, v12, v34\n"
    "v_dot2_f32_bf16 v35, v209, v13, v35\n"
    "v_dot2_f32_bf16 v36, v210, v12, v36\n"
    "v_dot2_f32_bf16 v37, v210, v13, v37\n"
    "v_dot2_f32_bf16 v38, v211, v12, v38\n"
    "v_dot2_f32_bf16 v39, v211, v13, v39\n"
    "v_dot2_f32_bf16 v32, v212, v12, v32\n"
    "v_dot2_f32_bf16 v33, v212, v13, v33\n"
    "v_dot2_f32_bf16 v34, v213, v12, v34\n"
    "v_dot2_f32_bf16 v35, v213, v13, v35\n"
    "v_dot2_f32_bf16 v36, v214, v12, v36\n"
    "v_dot2_f32_bf16 v37, v214, v13, v37\n"
    "v_dot2_f32_bf16 v38, v215, v12, v38\n"
    "v_dot2_f32_bf16 v39, v215, v13, v39\n"
    "v_dot2_f32_bf16 v32, v216, v12, v32\n"
    "v_dot2_f32_bf16 v33, v216, v13, v33\n"
    "v_dot2_f32_bf16 v34, v217, v12, v34\n"
    "v_dot2_f32_bf16 v35, v217, v13, v35\n"
    "v_dot2_f32_bf16 v36, v218, v12, v36\n"
    "v_dot2_f32_bf16 v37, v218, v13, v37\n"
    "v_dot2_f32_bf16 v38, v219, v12, v38\n"
    "v_dot2_f32_bf16 v39, v219, v13, v39\n"
    "v_dot2_f32_bf16 v32, v220, v12, v32\n"
    "v_dot2_f32_bf16 v33, v220, v13, v33\n"
    "v_dot2_f32_bf16 v34, v221, v12, v34\n"
    "v_dot2_f32_bf16 v35, v221, v13, v35\n"
    "v_dot2_f32_bf16 v36, v222, v12, v36\n"
    "v_dot2_f32_bf16 v37, v222, v13, v37\n"
    "v_dot2_f32_bf16 v38, v223, v12, v38\n"
    "v_dot2_f32_bf16 v39, v223, v13, v39\n"
    "v_dot2_f32_bf16 v32, v224, v12, v32\n"
    "v_dot2_f32_bf16 v33, v224, v13, v33\n"
    "v_dot2_f32_bf16 v34, v225, v12, v34\n"
    "v_dot2_f32_bf16 v35, v225, v13, v35\n"
    "v_dot2_f32_bf16 v36, v226, v12, v36\n"
    "v_dot2_f32_bf16 v37, v226, v13, v37\n"
    "v_dot2_f32_bf16 v38, v227, v12, v38\n"
    "v_dot2_f32_bf16 v39, v227, v13, v39\n"
    "v_dot2_f32_bf16 v32, v228, v12, v32\n"
    "v_dot2_f32_bf16 v33, v228, v13, v33\n"
    "v_dot2_f32_bf16 v34, v229, v12, v34\n"
    "v_dot2_f32_bf16 v35, v229, v13, v35\n"
    "v_dot2_f32_bf16 v36, v230, v12, v36\n"
    "v_dot2_f32_bf16 v37, v230, v13, v37\n"
    "v_dot2_f32_bf16 v38, v231, v12, v38\n"
    "v_dot2_f32_bf16 v39, v231, v13, v39\n"
    "v_dot2_f32_bf16 v32, v232, v12, v32\n"
    "v_dot2_f32_bf16 v33, v232, v13, v33\n"
    "v_dot2_f32_bf16 v34, v233, v12, v34\n"
    "v_dot2_f32_bf16 v35, v233, v13, v35\n"
    "v_dot2_f32_bf16 v36, v234, v12, v36\n"
    "v_dot2_f32_bf16 v37, v234, v13, v37\n"
    "v_dot2_f32_bf16 v38, v235, v12, v38\n"
    "v_dot2_f32_bf16 v39, v235, v13, v39\n"
    "v_dot2_f32_bf16 v32, v236, v12, v32\n"
    "v_dot2_f32_bf16 v33, v236, v13, v33\n"
    "v_dot2_f32_bf16 v34, v237, v12, v34\n"
    "v_dot2_f32_bf16 v35, v237, v13, v35\n"
    "v_dot2_f32_bf16 v36, v238, v12, v36\n"
    "v_dot2_f32_bf16 v37, v238, v13, v37\n"
    "v_dot2_f32_bf16 v38, v239, v12, v38\n"
    "v_dot2_f32_bf16 v39, v239, v13, v39\n"
    "v_dot2_f32_bf16 v32, v240, v12, v32\n"
    "v_dot2_f32_bf16 v33, v240, v13, v33\n"
    "v_dot2_f32_bf16 v34, v241, v12, v34\n"
    "v_dot2_f32_bf16 v35, v241, v13, v35\n"
    "v_dot2_f32_bf16 v36, v242, v12, v36\n"
    "v_dot2_f32_bf16 v37, v242, v13, v37\n"
    "v_dot2_f32_bf16 v38, v243, v12, v38\n"
    "v_dot2_f32_bf16 v39, v243, v13, v39\n"
    "v_dot2_f32_bf16 v32, v244, v12, v32\n"
    "v_dot2_f32_bf16 v33, v244, v13, v33\n"
    "v_dot2_f32_bf16 v34, v245, v12, v34\n"
    "v_dot2_f32_bf16 v35, v245, v13, v35\n"
    "v_dot2_f32_bf16 v36, v246, v12, v36\n"
    "v_dot2_f32_bf16 v37, v246, v13, v37\n"
    "v_dot2_f32_bf16 v38, v247, v12, v38\n"
    "v_dot2_f32_bf16 v39, v247, v13, v39\n"
    "v_dot2_f32_bf16 v32, v248, v12, v32\n"
    "v_dot2_f32_bf16 v33, v248, v13, v33\n"
    "v_dot2_f32_bf16 v34, v249, v12, v34\n"
    "v_dot2_f32_bf16 v35, v249, v13, v35\n"
    "v_dot2_f32_bf16 v36, v250, v12, v36\n"
    "v_dot2_f32_bf16 v37, v250, v13, v37\n"
    "v_dot2_f32_bf16 v38, v251, v12, v38\n"
    "v_dot2_f32_bf16 v39, v251, v13, v39\n"
    "v_dot2_f32_bf16 v32, v252, v12, v32\n"
    "v_dot2_f32_bf16 v33, v252, v13, v33\n"
    "v_dot2_f32_bf16 v34, v253, v12, v34\n"
    "v_dot2_f32_bf16 v35, v253, v13, v35\n"
    "v_dot2_f32_bf16 v36, v254, v12, v36\n"
    "v_dot2_f32_bf16 v37, v254, v13, v37\n"
    "v_dot2_f32_bf16 v38, v255, v12, v38\n"
    "v_dot2_f32_bf16 v39, v255, v13, v39\n"
    "s_nop 2\n"
    "v_cvt_pk_bf16_f32 v44, v32, v33\n"
    "v_cvt_pk_bf16_f32 v45, v34, v35\n"
    "v_cvt_pk_bf16_f32 v46, v36, v37\n"
    "v_cvt_pk_bf16_f32 v47, v38, v39\n"
    "buffer_store_dwordx4 v[44:47], v20, s[36:39], s57 offen offset:512 sc1\n"
    ".Lr_f3:\n"
    "buffer_store_dwordx2 v[60:61], v62, s[48:51], s74 offen sc1\n"
    "s_branch .Lk2_end\n"
    ".Lk2_ep_comb:\n"
    "s_waitcnt vmcnt(16)\n"
    "v_accvgpr_read_b32 v16, a0\n"
    "v_accvgpr_read_b32 v17, a1\n"
    "v_accvgpr_read_b32 v18, a2\n"
    "v_accvgpr_read_b32 v19, a3\n"
    "v_max_f32 v20, v6, v16\n"
    "v_sub_f32 v60, v6, v20\n"
    "v_sub_f32 v12, v16, v20\n"
    "v_exp_f32 v60, v60\n"
    "v_exp_f32 v12, v12\n"
    "s_nop 0\n"
    "v_mul_f32 v12, v12, v17\n"
    "v_fma_f32 v20, v8, v60, v12\n"
    "v_rcp_f32 v20, v20\n"
    "s_nop 0\n"
    "v_mul_f32 v20, v20, v32\n"
    "v_mul_f32 v60, v60, v20\n"
    "v_mul_f32 v12, v12, v20\n"
    "v_max_f32 v20, v7, v18\n"
    "v_sub_f32 v62, v7, v20\n"
    "v_sub_f32 v14, v18, v20\n"
    "v_exp_f32 v62, v62\n"
    "v_exp_f32 v14, v14\n"
    "s_nop 0\n"
    "v_mul_f32 v14, v14, v19\n"
    "v_fma_f32 v20, v9, v62, v14\n"
    "v_rcp_f32 v20, v20\n"
    "s_nop 0\n"
    "v_mul_f32 v20, v20, v33\n"
    "v_mul_f32 v62, v62, v20\n"
    "v_mul_f32 v14, v14, v20\n"
    "v_pk_mul_f32 v[128:129], v[128:129], v[60:61] op_sel_hi:[1,0]\n"
    "v_pk_mul_f32 v[130:131], v[130:131], v[60:61] op_sel_hi:[1,0]\n"
    "v_pk_mul_f32 v[132:133], v[132:133], v[60:61] op_sel_hi:[1,0]\n"
    "v_pk_mul_f32 v[134:135], v[134:135], v[60:61] op_sel_hi:[1,0]\n"
    "v_pk_mul_f32 v[136:137], v[136:137], v[60:61] op_sel_hi:[1,0]\n"
    "v_pk_mul_f32 v[138:139], v[138:139], v[60:61] op_sel_hi:[1,0]\n"
    "v_pk_mul_f32 v[140:141], v[140:141], v[60:61] op_sel_hi:[1,0]\n"
    "v_pk_mul_f32 v[142:143], v[142:143], v[60:61] op_sel_hi:[1,0]\n"
    "v_pk_mul_f32 v[144:145], v[144:145], v[60:61] op_sel_hi:[1,0]\n"
    "v_pk_mul_f32 v[146:147], v[146:147], v[60:61] op_sel_hi:[1,0]\n"
    "v_pk_mul_f32 v[148:149], v[148:149], v[60:61] op_sel_hi:[1,0]\n"
    "v_pk_mul_f32 v[150:151], v[150:151], v[60:61] op_sel_hi:[1,0]\n"
    "v_pk_mul_f32 v[152:153], v[152:153], v[60:61] op_sel_hi:[1,0]\n"
    "v_pk_mul_f32 v[154:155], v[154:155], v[60:61] op_sel_hi:[1,0]\n"
    "v_pk_mul_f32 v[156:157], v[156:157], v[60:61] op_sel_hi:[1,0]\n"
    "v_pk_mul_f32 v[158:159], v[158:159], v[60:61] op_sel_hi:[1,0]\n"
    "v_pk_mul_f32 v[160:161], v[160:161], v[60:61] op_sel_hi:[1,0]\n"
    "v_pk_mul_f32 v[162:163], v[162:163], v[60:61] op_sel_hi:[1,0]\n"
    "v_pk_mul_f32 v[164:165], v[164:165], v[60:61] op_sel_hi:[1,0]\n"
    "v_pk_mul_f32 v[166:167], v[166:167], v[60:61] op_sel_hi:[1,0]\n"
    "v_pk_mul_f32 v[168:169], v[168:169], v[60:61] op_sel_hi:[1,0]\n"
    "v_pk_mul_f32 v[170:171], v[170:171], v[60:61] op_sel_hi:[1,0]\n"
    "v_pk_mul_f32 v[172:173], v[172:173], v[60:61] op_sel_hi:[1,0]\n"
    "v_pk_mul_f32 v[174:175], v[174:175], v[60:61] op_sel_hi:[1,0]\n"
    "v_pk_mul_f32 v[176:177], v[176:177], v[60:61] op_sel_hi:[1,0]\n"
    "v_pk_mul_f32 v[178:179], v[178:179], v[60:61] op_sel_hi:[1,0]\n"
    "v_pk_mul_f32 v[180:181], v[180:181], v[60:61] op_sel_hi:[1,0]\n"
    "v_pk_mul_f32 v[182:183], v[182:183], v[60:61] op_sel_hi:[1,0]\n"
    "v_pk_mul_f32 v[184:185], v[184:185], v[60:61] op_sel_hi:[1,0]\n"
    "v_pk_mul_f32 v[186:187], v[186:187], v[60:61] op_sel_hi:[1,0]\n"
    "v_pk_mul_f32 v[188:189], v[188:189], v[60:61] op_sel_hi:[1,0]\n"
    "v_pk_mul_f32 v[190:191], v[190:191], v[60:61] op_sel_hi:[1,0]\n"
    "v_pk_mul_f32 v[192:193], v[192:193], v[62:63] op_sel_hi:[1,0]\n"
    "v_pk_mul_f32 v[194:195], v[194:195], v[62:63] op_sel_hi:[1,0]\n"
    "v_pk_mul_f32 v[196:197], v[196:197], v[62:63] op_sel_hi:[1,0]\n"
    "v_pk_mul_f32 v[198:199], v[198:199], v[62:63] op_sel_hi:[1,0]\n"
    "v_pk_mul_f32 v[200:201], v[200:201], v[62:63] op_sel_hi:[1,0]\n"
    "v_pk_mul_f32 v[202:203], v[202:203], v[62:63] op_sel_hi:[1,0]\n"
    "v_pk_mul_f32 v[204:205], v[204:205], v[62:63] op_sel_hi:[1,0]\n"
    "v_pk_mul_f32 v[206:207], v[206:207], v[62:63] op_sel_hi:[1,0]\n"
    "v_pk_mul_f32 v[208:209], v[208:209], v[62:63] op_sel_hi:[1,0]\n"
    "v_pk_mul_f32 v[210:211], v[210:211], v[62:63] op_sel_hi:[1,0]\n"
    "v_pk_mul_f32 v[212:213], v[212:213], v[62:63] op_sel_hi:[1,0]\n"
    "v_pk_mul_f32 v[214:215], v[214:215], v[62:63] op_sel_hi:[1,0]\n"
    "v_pk_mul_f32 v[216:217], v[216:217], v[62:63] op_sel_hi:[1,0]\n"
    "v_pk_mul_f32 v[218:219], v[218:219], v[62:63] op_sel_hi:[1,0]\n"
    "v_pk_mul_f32 v[220:221], v[220:221], v[62:63] op_sel_hi:[1,0]\n"
    "v_pk_mul_f32 v[222:223], v[222:223], v[62:63] op_sel_hi:[1,0]\n"
    "v_pk_mul_f32 v[224:225], v[224:225], v[62:63] op_sel_hi:[1,0]\n"
    "v_pk_mul_f32 v[226:227], v[226:227], v[62:63] op_sel_hi:[1,0]\n"
    "v_pk_mul_f32 v[228:229], v[228:229], v[62:63] op_sel_hi:[1,0]\n"
    "v_pk_mul_f32 v[230:231], v[230:231], v[62:63] op_sel_hi:[1,0]\n"
    "v_pk_mul_f32 v[232:233], v[232:233], v[62:63] op_sel_hi:[1,0]\n"
    "v_pk_mul_f32 v[234:235], v[234:235], v[62:63] op_sel_hi:[1,0]\n"
    "v_pk_mul_f32 v[236:237], v[236:237], v[62:63] op_sel_hi:[1,0]\n"
    "v_pk_mul_f32 v[238:239], v[238:239], v[62:63] op_sel_hi:[1,0]\n"
    "v_pk_mul_f32 v[240:241], v[240:241], v[62:63] op_sel_hi:[1,0]\n"
    "v_pk_mul_f32 v[242:243], v[242:243], v[62:63] op_sel_hi:[1,0]\n"
    "v_pk_mul_f32 v[244:245], v[244:245], v[62:63] op_sel_hi:[1,0]\n"
    "v_pk_mul_f32 v[246:247], v[246:247], v[62:63] op_sel_hi:[1,0]\n"
    "v_pk_mul_f32 v[248:249], v[248:249], v[62:63] op_sel_hi:[1,0]\n"
    "v_pk_mul_f32 v[250:251], v[250:251], v[62:63] op_sel_hi:[1,0]\n"
    "v_pk_mul_f32 v[252:253], v[252:253], v[62:63] op_sel_hi:[1,0]\n"
    "v_pk_mul_f32 v[254:255], v[254:255], v[62:63] op_sel_hi:[1,0]\n"
    "v_lshlrev_b32 v16, 16, v64\n"
    "v_and_b32 v17, 0xffff0000, v64\n"
    "v_pk_fma_f32 v[128:129], v[16:17], v[12:13], v[128:129] op_sel_hi:[1,0,1]\n"
    "v_lshlrev_b32 v18, 16, v65\n"
    "v_and_b32 v19, 0xffff0000, v65\n"
    "v_pk_fma_f32 v[130:131], v[18:19], v[12:13], v[130:131] op_sel_hi:[1,0,1]\n"
    "v_lshlrev_b32 v20, 16, v66\n"
    "v_and_b32 v21, 0xffff0000, v66\n"
    "v_pk_fma_f32 v[132:133], v[20:21], v[12:13], v[132:133] op_sel_hi:[1,0,1]\n"
    "v_lshlrev_b32 v22, 16, v67\n"
    "v_and_b32 v23, 0xffff0000, v67\n"
    "v_pk_fma_f32 v[134:135], v[22:23], v[12:13], v[134:135] op_sel_hi:[1,0,1]\n"
    "v_lshlrev_b32 v16, 16, v68\n"
    "v_and_b32 v17, 0xffff0000, v68\n"
    "v_pk_fma_f32 v[136:137], v[16:17], v[12:13], v[136:137] op_sel_hi:[1,0,1]\n"
    "v_lshlrev_b32 v18, 16, v69\n"
    "v_and_b32 v19, 0xffff0000, v69\n"
    "v_pk_fma_f32 v[138:139], v[18:19], v[12:13], v[138:139] op_sel_hi:[1,0,1]\n"
    "v_lshlrev_b32 v20, 16, v70\n"
    "v_and_b32 v21, 0xffff0000, v70\n"
    "v_pk_fma_f32 v[140:141], v[20:21], v[12:13], v[140:141] op_sel_hi:[1,0,1]\n"
    "v_lshlrev_b32 v22, 16, v71\n"
    "v_and_b32 v23, 0xffff0000, v71\n"
    "v_pk_fma_f32 v[142:143], v[22:23], v[12:13], v[142:143] op_sel_hi:[1,0,1]\n"
    "v_lshlrev_b32 v16, 16, v72\n"
    "v_and_b32 v17, 0xffff0000, v72\n"
    "v_pk_fma_f32 v[144:145], v[16:17], v[12:13], v[144:145] op_sel_hi:[1,0,1]\n"
    "v_lshlrev_b32 v18, 16, v73\n"
    "v_and_b32 v19, 0xffff0000, v73\n"
    "v_pk_fma_f32 v[146:147], v[18:19], v[12:13], v[146:147] op_sel_hi:[1,0,1]\n"
    "v_lshlrev_b32 v20, 16, v74\n"
    "v_and_b32 v21, 0xffff0000, v74\n"
    "v_pk_fma_f32 v[148:149], v[20:21], v[12:13], v[148:149] op_sel_hi:[1,0,1]\n"
    "v_lshlrev_b32 v22, 16, v75\n"
    "v_and_b32 v23, 0xffff0000, v75\n"
    "v_pk_fma_f32 v[150:151], v[22:23], v[12:13], v[150:151] op_sel_hi:[1,0,1]\n"
    "v_lshlrev_b32 v16, 16, v76\n"
    "v_and_b32 v17, 0xffff0000, v76\n"
    "v_pk_fma_f32 v[152:153], v[16:17], v[12:13], v[152:153] op_sel_hi:[1,0,1]\n"
    "v_lshlrev_b32 v18, 16, v77\n"
    "v_and_b32 v19, 0xffff0000, v77\n"
    "v_pk_fma_f32 v[154:155], v[18:19], v[12:13], v[154:155] op_sel_hi:[1,0,1]\n"
    "v_lshlrev_b32 v20, 16, v78\n"
    "v_and_b32 v21, 0xffff0000, v78\n"
    "v_pk_fma_f32 v[156:157], v[20:21], v[12:13], v[156:157] op_sel_hi:[1,0,1]\n"
    "v_lshlrev_b32 v22, 16, v79\n"
    "v_and_b32 v23, 0xffff0000, v79\n"
    "v_pk_fma_f32 v[158:159], v[22:23], v[12:13], v[158:159] op_sel_hi:[1,0,1]\n"
    "v_lshlrev_b32 v16, 16, v80\n"
    "v_and_b32 v17, 0xffff0000, v80\n"
    "v_pk_fma_f32 v[160:161], v[16:17], v[12:13], v[160:161] op_sel_hi:[1,0,1]\n"
    "v_lshlrev_b32 v18, 16, v81\n"
    "v_and_b32 v19, 0xffff0000, v81\n"
    "v_pk_fma_f32 v[162:163], v[18:19], v[12:13], v[162:163] op_sel_hi:[1,0,1]\n"
    "v_lshlrev_b32 v20, 16, v82\n"
    "v_and_b32 v21, 0xffff0000, v82\n"
    "v_pk_fma_f32 v[164:165], v[20:21], v[12:13], v[164:165] op_sel_hi:[1,0,1]\n"
    "v_lshlrev_b32 v22, 16, v83\n"
    "v_and_b32 v23, 0xffff0000, v83\n"
    "v_pk_fma_f32 v[166:167], v[22:23], v[12:13], v[166:167] op_sel_hi:[1,0,1]\n"
    "v_lshlrev_b32 v16, 16, v84\n"
    "v_and_b32 v17, 0xffff0000, v84\n"
    "v_pk_fma_f32 v[168:169], v[16:17], v[12:13], v[168:169] op_sel_hi:[1,0,1]\n"
    "v_lshlrev_b32 v18, 16, v85\n"
    "v_and_b32 v19, 0xffff0000, v85\n"
    "v_pk_fma_f32 v[170:171], v[18:19], v[12:13], v[170:171] op_sel_hi:[1,0,1]\n"
    "v_lshlrev_b32 v20, 16, v86\n"
    "v_and_b32 v21, 0xffff0000, v86\n"
    "v_pk_fma_f32 v[172:173], v[20:21], v[12:13], v[172:173] op_sel_hi:[1,0,1]\n"
    "v_lshlrev_b32 v22, 16, v87\n"
    "v_and_b32 v23, 0xffff0000, v87\n"
    "v_pk_fma_f32 v[174:175], v[22:23], v[12:13], v[174:175] op_sel_hi:[1,0,1]\n"
    "v_lshlrev_b32 v16, 16, v88\n"
    "v_and_b32 v17, 0xffff0000, v88\n"
    "v_pk_fma_f32 v[176:177], v[16:17], v[12:13], v[176:177] op_sel_hi:[1,0,1]\n"
    "v_lshlrev_b32 v18, 16, v89\n"
    "v_and_b32 v19, 0xffff0000, v89\n"
    "v_pk_fma_f32 v[178:179], v[18:19], v[12:13], v[178:179] op_sel_hi:[1,0,1]\n"
    "v_lshlrev_b32 v20, 16, v90\n"
    "v_and_b32 v21, 0xffff0000, v90\n"
    "v_pk_fma_f32 v[180:181], v[20:21], v[12:13], v[180:181] op_sel_hi:[1,0,1]\n"
    "v_lshlrev_b32 v22, 16, v91\n"
    "v_and_b32 v23, 0xffff0000, v91\n"
    "v_pk_fma_f32 v[182:183], v[22:23], v[12:13], v[182:183] op_sel_hi:[1,0,1]\n"
    "v_lshlrev_b32 v16, 16, v92\n"
    "v_and_b32 v17, 0xffff0000, v92\n"
    "v_pk_fma_f32 v[184:185], v[16:17], v[12:13], v[184:185] op_sel_hi:[1,0,1]\n"
    "v_lshlrev_b32 v18, 16, v93\n"
    "v_and_b32 v19, 0xffff0000, v93\n"
    "v_pk_fma_f32 v[186:187], v[18:19], v[12:13], v[186:187] op_sel_hi:[1,0,1]\n"
    "v_lshlrev_b32 v20, 16, v94\n"
    "v_and_b32 v21, 0xffff0000, v94\n"
    "v_pk_fma_f32 v[188:189], v[20:21], v[12:13], v[188:189] op_sel_hi:[1,0,1]\n"
    "v_lshlrev_b32 v22, 16, v95\n"
    "v_and_b32 v23, 0xffff0000, v95\n"
    "v_pk_fma_f32 v[190:191], v[22:23], v[12:13], v[190:191] op_sel_hi:[1,0,1]\n"
    "s_waitcnt vmcnt(0)\n"
    "v_lshlrev_b32 v16, 16, v96\n"
    "v_and_b32 v17, 0xffff0000, v96\n"
    "v_pk_fma_f32 v[192:193], v[16:17], v[14:15], v[192:193] op_sel_hi:[1,0,1]\n"
    "v_lshlrev_b32 v18, 16, v97\n"
    "v_and_b32 v19, 0xffff0000, v97\n"
    "v_pk_fma_f32 v[194:195], v[18:19], v[14:15], v[194:195] op_sel_hi:[1,0,1]\n"
    "v_lshlrev_b32 v20, 16, v98\n"
    "v_and_b32 v21, 0xffff0000, v98\n"
    "v_pk_fma_f32 v[196:197], v[20:21], v[14:15], v[196:197] op_sel_hi:[1,0,1]\n"
    "v_lshlrev_b32 v22, 16, v99\n"
    "v_and_b32 v23, 0xffff0000, v99\n"
    "v_pk_fma_f32 v[198:199], v[22:23], v[14:15], v[198:199] op_sel_hi:[1,0,1]\n"
    "v_lshlrev_b32 v16, 16, v100\n"
    "v_and_b32 v17, 0xffff0000, v100\n"
    "v_pk_fma_f32 v[200:201], v[16:17], v[14:15], v[200:201] op_sel_hi:[1,0,1]\n"
    "v_lshlrev_b32 v18, 16, v101\n"
    "v_and_b32 v19, 0xffff0000, v101\n"
    "v_pk_fma_f32 v[202:203], v[18:19], v[14:15], v[202:203] op_sel_hi:[1,0,1]\n"
    "v_lshlrev_b32 v20, 16, v102\n"
    "v_and_b32 v21, 0xffff0000, v102\n"
    "v_pk_fma_f32 v[204:205], v[20:21], v[14:15], v[204:205] op_sel_hi:[1,0,1]\n"
    "v_lshlrev_b32 v22, 16, v103\n"
    "v_and_b32 v23, 0xffff0000, v103\n"
    "v_pk_fma_f32 v[206:207], v[22:23], v[14:15], v[206:207] op_sel_hi:[1,0,1]\n"
    "v_lshlrev_b32 v16, 16, v104\n"
    "v_and_b32 v17, 0xffff0000, v104\n"
    "v_pk_fma_f32 v[208:209], v[16:17], v[14:15], v[208:209] op_sel_hi:[1,0,1]\n"
    "v_lshlrev_b32 v18, 16, v105\n"
    "v_and_b32 v19, 0xffff0000, v105\n"
    "v_pk_fma_f32 v[210:211], v[18:19], v[14:15], v[210:211] op_sel_hi:[1,0,1]\n"
    "v_lshlrev_b32 v20, 16, v106\n"
    "v_and_b32 v21, 0xffff0000, v106\n"
    "v_pk_fma_f32 v[212:213], v[20:21], v[14:15], v[212:213] op_sel_hi:[1,0,1]\n"
    "v_lshlrev_b32 v22, 16, v107\n"
    "v_and_b32 v23, 0xffff0000, v107\n"
    "v_pk_fma_f32 v[214:215], v[22:23], v[14:15], v[214:215] op_sel_hi:[1,0,1]\n"
    "v_lshlrev_b32 v16, 16, v108\n"
    "v_and_b32 v17, 0xffff0000, v108\n"
    "v_pk_fma_f32 v[216:217], v[16:17], v[14:15], v[216:217] op_sel_hi:[1,0,1]\n"
    "v_lshlrev_b32 v18, 16, v109\n"
    "v_and_b32 v19, 0xffff0000, v109\n"
    "v_pk_fma_f32 v[218:219], v[18:19], v[14:15], v[218:219] op_sel_hi:[1,0,1]\n"
    "v_lshlrev_b32 v20, 16, v110\n"
    "v_and_b32 v21, 0xffff0000, v110\n"
    "v_pk_fma_f32 v[220:221], v[20:21], v[14:15], v[220:221] op_sel_hi:[1,0,1]\n"
    "v_lshlrev_b32 v22, 16, v111\n"
    "v_and_b32 v23, 0xffff0000, v111\n"
    "v_pk_fma_f32 v[222:223], v[22:23], v[14:15], v[222:223] op_sel_hi:[1,0,1]\n"
    "v_lshlrev_b32 v16, 16, v112\n"
    "v_and_b32 v17, 0xffff0000, v112\n"
    "v_pk_fma_f32 v[224:225], v[16:17], v[14:15], v[224:225] op_sel_hi:[1,0,1]\n"
    "v_lshlrev_b32 v18, 16, v113\n"
    "v_and_b32 v19, 0xffff0000, v113\n"
    "v_pk_fma_f32 v[226:227], v[18:19], v[14:15], v[226:227] op_sel_hi:[1,0,1]\n"
    "v_lshlrev_b32 v20, 16, v114\n"
    "v_and_b32 v21, 0xffff0000, v114\n"
    "v_pk_fma_f32 v[228:229], v[20:21], v[14:15], v[228:229] op_sel_hi:[1,0,1]\n"
    "v_lshlrev_b32 v22, 16, v115\n"
    "v_and_b32 v23, 0xffff0000, v115\n"
    "v_pk_fma_f32 v[230:231], v[22:23], v[14:15], v[230:231] op_sel_hi:[1,0,1]\n"
    "v_lshlrev_b32 v16, 16, v116\n"
    "v_and_b32 v17, 0xffff0000, v116\n"
    "v_pk_fma_f32 v[232:233], v[16:17], v[14:15], v[232:233] op_sel_hi:[1,0,1]\n"
    "v_lshlrev_b32 v18, 16, v117\n"
    "v_and_b32 v19, 0xffff0000, v117\n"
    "v_pk_fma_f32 v[234:235], v[18:19], v[14:15], v[234:235] op_sel_hi:[1,0,1]\n"
    "v_lshlrev_b32 v20, 16, v118\n"
    "v_and_b32 v21, 0xffff0000, v118\n"
    "v_pk_fma_f32 v[236:237], v[20:21], v[14:15], v[236:237] op_sel_hi:[1,0,1]\n"
    "v_lshlrev_b32 v22, 16, v119\n"
    "v_and_b32 v23, 0xffff0000, v119\n"
    "v_pk_fma_f32 v[238:239], v[22:23], v[14:15], v[238:239] op_sel_hi:[1,0,1]\n"
    "v_lshlrev_b32 v16, 16, v120\n"
    "v_and_b32 v17, 0xffff0000, v120\n"
    "v_pk_fma_f32 v[240:241], v[16:17], v[14:15], v[240:241] op_sel_hi:[1,0,1]\n"
    "v_lshlrev_b32 v18, 16, v121\n"
    "v_and_b32 v19, 0xffff0000, v121\n"
    "v_pk_fma_f32 v[242:243], v[18:19], v[14:15], v[242:243] op_sel_hi:[1,0,1]\n"
    "v_lshlrev_b32 v20, 16, v122\n"
    "v_and_b32 v21, 0xffff0000, v122\n"
    "v_pk_fma_f32 v[244:245], v[20:21], v[14:15], v[244:245] op_sel_hi:[1,0,1]\n"
    "v_lshlrev_b32 v22, 16, v123\n"
    "v_and_b32 v23, 0xffff0000, v123\n"
    "v_pk_fma_f32 v[246:247], v[22:23], v[14:15], v[246:247] op_sel_hi:[1,0,1]\n"
    "v_lshlrev_b32 v16, 16, v124\n"
    "v_and_b32 v17, 0xffff0000, v124\n"
    "v_pk_fma_f32 v[248:249], v[16:17], v[14:15], v[248:249] op_sel_hi:[1,0,1]\n"
    "v_lshlrev_b32 v18, 16, v125\n"
    "v_and_b32 v19, 0xffff0000, v125\n"
    "v_pk_fma_f32 v[250:251], v[18:19], v[14:15], v[250:251] op_sel_hi:[1,0,1]\n"
    "v_lshlrev_b32 v20, 16, v126\n"
    "v_and_b32 v21, 0xffff0000, v126\n"
    "v_pk_fma_f32 v[252:253], v[20:21], v[14:15], v[252:253] op_sel_hi:[1,0,1]\n"
    "v_lshlrev_b32 v22, 16, v127\n"
    "v_and_b32 v23, 0xffff0000, v127\n"
    "v_pk_fma_f32 v[254:255], v[22:23], v[14:15], v[254:255] op_sel_hi:[1,0,1]\n"
    "s_branch .Lk2_ep_store\n"
    ".Lk2_kvc:\n"
    "v_mov_b64 v[64:65], 0\n"
    "v_mov_b64 v[66:67], 0\n"
    "v_mov_b64 v[68:69], 0\n"
    "v_mov_b64 v[70:71], 0\n"
    "v_mov_b64 v[72:73], 0\n"
    "v_mov_b64 v[74:75], 0\n"
    "v_mov_b64 v[76:77], 0\n"
    "v_mov_b64 v[78:79], 0\n"
    "v_mov_b64 v[80:81], 0\n"
    "v_mov_b64 v[82:83], 0\n"
    "v_mov_b64 v[84:85], 0\n"
    "v_mov_b64 v[86:87], 0\n"
    "v_mov_b64 v[88:89], 0\n"
    "v_mov_b64 v[90:91], 0\n"
    "v_mov_b64 v[92:93], 0\n"
    "v_mov_b64 v[94:95], 0\n"
    "s_bitset1_b32 s55, 4\n"
    "buffer_load_dwordx4 v[64:67], v5, s[36:39], s88 offen sc1\n"
    "s_add_u32 s88, s88, 4096\n"
    "s_cmp_ge_u32 s88, 65536\n"
    "s_cbranch_scc1 .Lk2_kvc_ret\n"
    "buffer_load_dwordx4 v[68:71], v5, s[36:39], s88 offen sc1\n"
    "s_add_u32 s88, s88, 4096\n"
    "s_cmp_ge_u32 s88, 65536\n"
    "s_cbranch_scc1 .Lk2_kvc_ret\n"
    "buffer_load_dwordx4 v[72:75], v5, s[36:39], s88 offen sc1\n"
    "s_add_u32 s88, s88, 4096\n"
    "s_cmp_ge_u32 s88, 65536\n"
    "s_cbranch_scc1 .Lk2_kvc_ret\n"
    "buffer_load_dwordx4 v[76:79], v5, s[36:39], s88 offen sc1\n"
    "s_add_u32 s88, s88, 4096\n"
    "s_cmp_ge_u32 s88, 65536\n"
    "s_cbranch_scc1 .Lk2_kvc_ret\n"
    "buffer_load_dwordx4 v[80:83], v5, s[36:39], s88 offen sc1\n"
    "s_add_u32 s88, s88, 4096\n"
    "s_cmp_ge_u32 s88, 65536\n"
    "s_cbranch_scc1 .Lk2_kvc_ret\n"
    "buffer_load_dwordx4 v[84:87], v5, s[36:39], s88 offen sc1\n"
    "s_add_u32 s88, s88, 4096\n"
    "s_cmp_ge_u32 s88, 65536\n"
    "s_cbranch_scc1 .Lk2_kvc_ret\n"
    "buffer_load_dwordx4 v[88:91], v5, s[36:39], s88 offen sc1\n"
    "s_add_u32 s88, s88, 4096\n"
    "s_cmp_ge_u32 s88, 65536\n"
    "s_cbranch_scc1 .Lk2_kvc_ret\n"
    "buffer_load_dwordx4 v[92:95], v5, s[36:39], s88 offen sc1\n"
    "s_add_u32 s88, s88, 4096\n"
    "s_cmp_ge_u32 s88, 65536\n"
    "s_cbranch_scc1 .Lk2_kvc_ret\n"
    "s_waitcnt vmcnt(0)\n"
    "v_pk_add_f32 v[56:57], v[56:57], v[64:65]\n"
    "v_pk_add_f32 v[58:59], v[58:59], v[66:67]\n"
    "v_pk_add_f32 v[56:57], v[56:57], v[68:69]\n"
    "v_pk_add_f32 v[58:59], v[58:59], v[70:71]\n"
    "v_pk_add_f32 v[56:57], v[56:57], v[72:73]\n"
    "v_pk_add_f32 v[58:59], v[58:59], v[74:75]\n"
    "v_pk_add_f32 v[56:57], v[56:57], v[76:77]\n"
    "v_pk_add_f32 v[58:59], v[58:59], v[78:79]\n"
    "v_pk_add_f32 v[56:57], v[56:57], v[80:81]\n"
    "v_pk_add_f32 v[58:59], v[58:59], v[82:83]\n"
    "v_pk_add_f32 v[56:57], v[56:57], v[84:85]\n"
    "v_pk_add_f32 v[58:59], v[58:59], v[86:87]\n"
    "v_pk_add_f32 v[56:57], v[56:57], v[88:89]\n"
    "v_pk_add_f32 v[58:59], v[58:59], v[90:91]\n"
    "v_pk_add_f32 v[56:57], v[56:57], v[92:93]\n"
    "v_pk_add_f32 v[58:59], v[58:59], v[94:95]\n"
    "s_branch .Lk2_kvc\n"
    ".Lk2_kva:\n"
    "v_pk_add_f32 v[56:57], v[56:57], v[64:65]\n"
    "v_pk_add_f32 v[58:59], v[58:59], v[66:67]\n"
    "v_pk_add_f32 v[56:57], v[56:57], v[68:69]\n"
    "v_pk_add_f32 v[58:59], v[58:59], v[70:71]\n"
    "v_pk_add_f32 v[56:57], v[56:57], v[72:73]\n"
    "v_pk_add_f32 v[58:59], v[58:59], v[74:75]\n"
    "v_pk_add_f32 v[56:57], v[56:57], v[76:77]\n"
    "v_pk_add_f32 v[58:59], v[58:59], v[78:79]\n"
    "v_pk_add_f32 v[56:57], v[56:57], v[80:81]\n"
    "v_pk_add_f32 v[58:59], v[58:59], v[82:83]\n"
    "v_pk_add_f32 v[56:57], v[56:57], v[84:85]\n"
    "v_pk_add_f32 v[58:59], v[58:59], v[86:87]\n"
    "v_pk_add_f32 v[56:57], v[56:57], v[88:89]\n"
    "v_pk_add_f32 v[58:59], v[58:59], v[90:91]\n"
    "v_pk_add_f32 v[56:57], v[56:57], v[92:93]\n"
    "v_pk_add_f32 v[58:59], v[58:59], v[94:95]\n"
    "s_branch .Lk2_kva_ret\n"
    ".Lk2_poll:\n"
    "s_mov_b32 s81, 0\n"
    ".Lk2_spin:\n"
    "v_cmp_eq_u64 vcc, s[16:17], v[24:25]\n"
    "s_nop 1\n"
    "s_cbranch_vccnz .Lk2_got\n"
    "s_add_u32 s81, s81, 1\n"
    "s_cmp_gt_u32 s81, 0x40000\n"
    "s_cbranch_scc1 .Lk2_got\n"
    "s_sleep 8\n"
    "buffer_load_dwordx2 v[24:25], off, s[48:51], s73 sc1\n"
    "s_waitcnt vmcnt(0)\n"
    "s_branch .Lk2_spin\n"
    ".Lk2_got:\n"
    "s_branch .Lk2_poll_ret\n"
    ".Lk2_cold:\n"
    ".Lk2_rare0:\n"
    "s_call_b64 s[46:47], .Lk2_r0\n"
    "s_branch .Lk2_rare0_ret\n"
    ".Lk2_rare0e:\n"
    "s_call_b64 s[46:47], .Lk2_r0\n"
    "s_branch .Lk2_rare0e_ret\n"
    ".Lk2_rare1:\n"
    "s_call_b64 s[46:47], .Lk2_r1\n"
    "s_branch .Lk2_rare1_ret\n"
    ".Lk2_rare1p:\n"
    "s_call_b64 s[46:47], .Lk2_r1\n"
    "s_branch .Lk2_rare1p_ret\n"
    ".Lk2_r0:\n"
    "s_nop 15\n"
    "v_max3_f32 v60, v64, v65, v66\n"
    "v_max3_f32 v61, v68, v69, v70\n"
    "v_max3_f32 v60, v60, v67, v72\n"
    "v_max3_f32 v61, v61, v71, v76\n"
    "v_max3_f32 v60, v60, v73, v74\n"
    "v_max3_f32 v61, v61, v77, v78\n"
    "v_max3_f32 v60, v60, v75, v80\n"
    "v_max3_f32 v61, v61, v79, v84\n"
    "v_max3_f32 v60, v60, v81, v82\n"
    "v_max3_f32 v61, v61, v85, v86\n"
    "v_max3_f32 v60, v60, v83, v88\n"
    "v_max3_f32 v61, v61, v87, v92\n"
    "v_max3_f32 v60, v60, v89, v90\n"
    "v_max3_f32 v61, v61, v93, v94\n"
    "v_max_f32 v60, v60, v91\n"
    "v_max_f32 v61, v61, v95\n"
    "v_mov_b32 v16, v60\n"
    "v_mov_b32 v17, v61\n"
    "s_nop 0\n"
    "v_permlane32_swap_b32 v16, v60\n"
    "v_permlane32_swap_b32 v17, v61\n"
    "v_max_f32 v60, v60, v16\n"
    "v_max_f32 v61, v61, v17\n"
    "v_mov_b32 v16, v60\n"
    "v_mov_b32 v17, v61\n"
    "s_nop 0\n"
    "v_permlane16_swap_b32 v16, v60\n"
    "v_permlane16_swap_b32 v17, v61\n"
    "v_max_f32 v60, v60, v16\n"
    "v_max_f32 v61, v61, v17\n"
    "v_max_f32 v60, 0, v60\n"
    "v_add_f32 v6, v6, v60\n"
    "v_exp_f32 v62, -v60\n"
    "v_sub_f32 v24, 0, v6\n"
    "v_sub_f32 v25, 0, v6\n"
    "v_sub_f32 v26, 0, v6\n"
    "v_sub_f32 v27, 0, v6\n"
    "v_mul_f32 v8, v8, v62\n"
    "v_sub_f32 v64, v64, v60\n"
    "v_sub_f32 v65, v65, v60\n"
    "v_sub_f32 v66, v66, v60\n"
    "v_sub_f32 v67, v67, v60\n"
    "v_sub_f32 v72, v72, v60\n"
    "v_sub_f32 v73, v73, v60\n"
    "v_sub_f32 v74, v74, v60\n"
    "v_sub_f32 v75, v75, v60\n"
    "v_sub_f32 v80, v80, v60\n"
    "v_sub_f32 v81, v81, v60\n"
    "v_sub_f32 v82, v82, v60\n"
    "v_sub_f32 v83, v83, v60\n"
    "v_sub_f32 v88, v88, v60\n"
    "v_sub_f32 v89, v89, v60\n"
    "v_sub_f32 v90, v90, v60\n"
    "v_sub_f32 v91, v91, v60\n"
    "v_sub_f32 v96, v96, v60\n"
    "v_sub_f32 v97, v97, v60\n"
    "v_sub_f32 v98, v98, v60\n"
    "v_sub_f32 v99, v99, v60\n"
    "v_sub_f32 v104, v104, v60\n"
    "v_sub_f32 v105, v105, v60\n"
    "v_sub_f32 v106, v106, v60\n"
    "v_sub_f32 v107, v107, v60\n"
    "v_sub_f32 v112, v112, v60\n"
    "v_sub_f32 v113, v113, v60\n"
    "v_sub_f32 v114, v114, v60\n"
    "v_sub_f32 v115, v115, v60\n"
    "v_sub_f32 v120, v120, v60\n"
    "v_sub_f32 v121, v121, v60\n"
    "v_sub_f32 v122, v122, v60\n"
    "v_sub_f32 v123, v123, v60\n"
    "v_mul_f32 v128, v128, v62\n"
    "v_mul_f32 v129, v129, v62\n"
    "v_mul_f32 v130, v130, v62\n"
    "v_mul_f32 v131, v131, v62\n"
    "v_mul_f32 v132, v132, v62\n"
    "v_mul_f32 v133, v133, v62\n"
    "v_mul_f32 v134, v134, v62\n"
    "v_mul_f32 v135, v135, v62\n"
    "v_mul_f32 v136, v136, v62\n"
    "v_mul_f32 v137, v137, v62\n"
    "v_mul_f32 v138, v138, v62\n"
    "v_mul_f32 v139, v139, v62\n"
    "v_mul_f32 v140, v140, v62\n"
    "v_mul_f32 v141, v141, v62\n"
    "v_mul_f32 v142, v142, v62\n"
    "v_mul_f32 v143, v143, v62\n"
    "v_mul_f32 v144, v144, v62\n"
    "v_mul_f32 v145, v145, v62\n"
    "v_mul_f32 v146, v146, v62\n"
    "v_mul_f32 v147, v147, v62\n"
    "v_mul_f32 v148, v148, v62\n"
    "v_mul_f32 v149, v149, v62\n"
    "v_mul_f32 v150, v150, v62\n"
    "v_mul_f32 v151, v151, v62\n"
    "v_mul_f32 v152, v152, v62\n"
    "v_mul_f32 v153, v153, v62\n"
    "v_mul_f32 v154, v154, v62\n"
    "v_mul_f32 v155, v155, v62\n"
    "v_mul_f32 v156, v156, v62\n"
    "v_mul_f32 v157, v157, v62\n"
    "v_mul_f32 v158, v158, v62\n"
    "v_mul_f32 v159, v159, v62\n"
    "v_mul_f32 v160, v160, v62\n"
    "v_mul_f32 v161, v161, v62\n"
    "v_mul_f32 v162, v162, v62\n"
    "v_mul_f32 v163, v163, v62\n"
    "v_mul_f32 v164, v164, v62\n"
    "v_mul_f32 v165, v165, v62\n"
    "v_mul_f32 v166, v166, v62\n"
    "v_mul_f32 v167, v167, v62\n"
    "v_mul_f32 v168, v168, v62\n"
    "v_mul_f32 v169, v169, v62\n"
    "v_mul_f32 v170, v170, v62\n"
    "v_mul_f32 v171, v171, v62\n"
    "v_mul_f32 v172, v172, v62\n"
    "v_mul_f32 v173, v173, v62\n"
    "v_mul_f32 v174, v174, v62\n"
    "v_mul_f32 v175, v175, v62\n"
    "v_mul_f32 v176, v176, v62\n"
    "v_mul_f32 v177, v177, v62\n"
    "v_mul_f32 v178, v178, v62\n"
    "v_mul_f32 v179, v179, v62\n"
    "v_mul_f32 v180, v180, v62\n"
    "v_mul_f32 v181, v181, v62\n"
    "v_mul_f32 v182, v182, v62\n"
    "v_mul_f32 v183, v183, v62\n"
    "v_mul_f32 v184, v184, v62\n"
    "v_mul_f32 v185, v185, v62\n"
    "v_mul_f32 v186, v186, v62\n"
    "v_mul_f32 v187, v187, v62\n"
    "v_mul_f32 v188, v188, v62\n"
    "v_mul_f32 v189, v189, v62\n"
    "v_mul_f32 v190, v190, v62\n"
    "v_mul_f32 v191, v191, v62\n"
    "v_max_f32 v61, 0, v61\n"
    "v_add_f32 v7, v7, v61\n"
    "v_exp_f32 v63, -v61\n"
    "v_sub_f32 v28, 0, v7\n"
    "v_sub_f32 v29, 0, v7\n"
    "v_sub_f32 v30, 0, v7\n"
    "v_sub_f32 v31, 0, v7\n"
    "v_mul_f32 v9, v9, v63\n"
    "v_sub_f32 v68, v68, v61\n"
    "v_sub_f32 v69, v69, v61\n"
    "v_sub_f32 v70, v70, v61\n"
    "v_sub_f32 v71, v71, v61\n"
    "v_sub_f32 v76, v76, v61\n"
    "v_sub_f32 v77, v77, v61\n"
    "v_sub_f32 v78, v78, v61\n"
    "v_sub_f32 v79, v79, v61\n"
    "v_sub_f32 v84, v84, v61\n"
    "v_sub_f32 v85, v85, v61\n"
    "v_sub_f32 v86, v86, v61\n"
    "v_sub_f32 v87, v87, v61\n"
    "v_sub_f32 v92, v92, v61\n"
    "v_sub_f32 v93, v93, v61\n"
    "v_sub_f32 v94, v94, v61\n"
    "v_sub_f32 v95, v95, v61\n"
    "v_sub_f32 v100, v100, v61\n"
    "v_sub_f32 v101, v101, v61\n"
    "v_sub_f32 v102, v102, v61\n"
    "v_sub_f32 v103, v103, v61\n"
    "v_sub_f32 v108, v108, v61\n"
    "v_sub_f32 v109, v109, v61\n"
    "v_sub_f32 v110, v110, v61\n"
    "v_sub_f32 v111, v111, v61\n"
    "v_sub_f32 v116, v116, v61\n"
    "v_sub_f32 v117, v117, v61\n"
    "v_sub_f32 v118, v118, v61\n"
    "v_sub_f32 v119, v119, v61\n"
    "v_sub_f32 v124, v124, v61\n"
    "v_sub_f32 v125, v125, v61\n"
    "v_sub_f32 v126, v126, v61\n"
    "v_sub_f32 v127, v127, v61\n"
    "v_mul_f32 v192, v192, v63\n"
    "v_mul_f32 v193, v193, v63\n"
    "v_mul_f32 v194, v194, v63\n"
    "v_mul_f32 v195, v195, v63\n"
    "v_mul_f32 v196, v196, v63\n"
    "v_mul_f32 v197, v197, v63\n"
    "v_mul_f32 v198, v198, v63\n"
    "v_mul_f32 v199, v199, v63\n"
    "v_mul_f32 v200, v200, v63\n"
    "v_mul_f32 v201, v201, v63\n"
    "v_mul_f32 v202, v202, v63\n"
    "v_mul_f32 v203, v203, v63\n"
    "v_mul_f32 v204, v204, v63\n"
    "v_mul_f32 v205, v205, v63\n"
    "v_mul_f32 v206, v206, v63\n"
    "v_mul_f32 v207, v207, v63\n"
    "v_mul_f32 v208, v208, v63\n"
    "v_mul_f32 v209, v209, v63\n"
    "v_mul_f32 v210, v210, v63\n"
    "v_mul_f32 v211, v211, v63\n"
    "v_mul_f32 v212, v212, v63\n"
    "v_mul_f32 v213, v213, v63\n"
    "v_mul_f32 v214, v214, v63\n"
    "v_mul_f32 v215, v215, v63\n"
    "v_mul_f32 v216, v216, v63\n"
    "v_mul_f32 v217, v217, v63\n"
    "v_mul_f32 v218, v218, v63\n"
    "v_mul_f32 v219, v219, v63\n"
    "v_mul_f32 v220, v220, v63\n"
    "v_mul_f32 v221, v221, v63\n"
    "v_mul_f32 v222, v222, v63\n"
    "v_mul_f32 v223, v223, v63\n"
    "v_mul_f32 v224, v224, v63\n"
    "v_mul_f32 v225, v225, v63\n"
    "v_mul_f32 v226, v226, v63\n"
    "v_mul_f32 v227, v227, v63\n"
    "v_mul_f32 v228, v228, v63\n"
    "v_mul_f32 v229, v229, v63\n"
    "v_mul_f32 v230, v230, v63\n"
    "v_mul_f32 v231, v231, v63\n"
    "v_mul_f32 v232, v232, v63\n"
    "v_mul_f32 v233, v233, v63\n"
    "v_mul_f32 v234, v234, v63\n"
    "v_mul_f32 v235, v235, v63\n"
    "v_mul_f32 v236, v236, v63\n"
    "v_mul_f32 v237, v237, v63\n"
    "v_mul_f32 v238, v238, v63\n"
    "v_mul_f32 v239, v239, v63\n"
    "v_mul_f32 v240, v240, v63\n"
    "v_mul_f32 v241, v241, v63\n"
    "v_mul_f32 v242, v242, v63\n"
    "v_mul_f32 v243, v243, v63\n"
    "v_mul_f32 v244, v244, v63\n"
    "v_mul_f32 v245, v245, v63\n"
    "v_mul_f32 v246, v246, v63\n"
    "v_mul_f32 v247, v247, v63\n"
    "v_mul_f32 v248, v248, v63\n"
    "v_mul_f32 v249, v249, v63\n"
    "v_mul_f32 v250, v250, v63\n"
    "v_mul_f32 v251, v251, v63\n"
    "v_mul_f32 v252, v252, v63\n"
    "v_mul_f32 v253, v253, v63\n"
    "v_mul_f32 v254, v254, v63\n"
    "v_mul_f32 v255, v255, v63\n"
    "v_exp_f32 v16, v64\n"
    "v_exp_f32 v17, v65\n"
    "v_exp_f32 v18, v66\n"
    "v_exp_f32 v19, v67\n"
    "v_add_f32 v10, v16, v17\n"
    "v_cvt_pk_bf16_f32 v32, v16, v17\n"
    "v_exp_f32 v20, v72\n"
    "v_exp_f32 v21, v73\n"
    "v_add_f32 v10, v10, v18\n"
    "v_add_f32 v10, v10, v19\n"
    "v_cvt_pk_bf16_f32 v33, v18, v19\n"
    "v_exp_f32 v22, v74\n"
    "v_exp_f32 v23, v75\n"
    "v_add_f32 v10, v10, v20\n"
    "v_add_f32 v10, v10, v21\n"
    "v_cvt_pk_bf16_f32 v34, v20, v21\n"
    "v_exp_f32 v16, v68\n"
    "v_exp_f32 v17, v69\n"
    "v_add_f32 v10, v10, v22\n"
    "v_add_f32 v10, v10, v23\n"
    "v_cvt_pk_bf16_f32 v35, v22, v23\n"
    "v_exp_f32 v18, v70\n"
    "v_exp_f32 v19, v71\n"
    "v_add_f32 v11, v16, v17\n"
    "v_cvt_pk_bf16_f32 v36, v16, v17\n"
    "v_exp_f32 v20, v76\n"
    "v_exp_f32 v21, v77\n"
    "v_add_f32 v11, v11, v18\n"
    "v_add_f32 v11, v11, v19\n"
    "v_cvt_pk_bf16_f32 v37, v18, v19\n"
    "v_exp_f32 v22, v78\n"
    "v_exp_f32 v23, v79\n"
    "v_add_f32 v11, v11, v20\n"
    "v_add_f32 v11, v11, v21\n"
    "v_cvt_pk_bf16_f32 v38, v20, v21\n"
    "v_exp_f32 v16, v80\n"
    "v_exp_f32 v17, v81\n"
    "v_add_f32 v11, v11, v22\n"
    "v_add_f32 v11, v11, v23\n"
    "v_cvt_pk_bf16_f32 v39, v22, v23\n"
    "v_exp_f32 v18, v82\n"
    "v_exp_f32 v19, v83\n"
    "v_add_f32 v10, v10, v16\n"
    "v_add_f32 v10, v10, v17\n"
    "v_cvt_pk_bf16_f32 v48, v16, v17\n"
    "v_exp_f32 v20, v88\n"
    "v_exp_f32 v21, v89\n"
    "v_add_f32 v10, v10, v18\n"
    "v_add_f32 v10, v10, v19\n"
    "v_cvt_pk_bf16_f32 v49, v18, v19\n"
    "v_exp_f32 v22, v90\n"
    "v_exp_f32 v23, v91\n"
    "v_add_f32 v10, v10, v20\n"
    "v_add_f32 v10, v10, v21\n"
    "v_cvt_pk_bf16_f32 v50, v20, v21\n"
    "v_exp_f32 v16, v84\n"
    "v_exp_f32 v17, v85\n"
    "v_add_f32 v10, v10, v22\n"
    "v_add_f32 v10, v10, v23\n"
    "v_cvt_pk_bf16_f32 v51, v22, v23\n"
    "v_exp_f32 v18, v86\n"
    "v_exp_f32 v19, v87\n"
    "v_add_f32 v11, v11, v16\n"
    "v_add_f32 v11, v11, v17\n"
    "v_cvt_pk_bf16_f32 v52, v16, v17\n"
    "v_exp_f32 v20, v92\n"
    "v_exp_f32 v21, v93\n"
    "v_add_f32 v11, v11, v18\n"
    "v_add_f32 v11, v11, v19\n"
    "v_cvt_pk_bf16_f32 v53, v18, v19\n"
    "v_exp_f32 v22, v94\n"
    "v_exp_f32 v23, v95\n"
    "v_add_f32 v11, v11, v20\n"
    "v_add_f32 v11, v11, v21\n"
    "v_cvt_pk_bf16_f32 v54, v20, v21\n"
    "s_nop 0\n"
    "v_add_f32 v11, v11, v22\n"
    "v_add_f32 v11, v11, v23\n"
    "v_cvt_pk_bf16_f32 v55, v22, v23\n"
    "s_nop 7\n"
    "s_setpc_b64 s[46:47]\n"
    ".Lk2_r1:\n"
    "s_nop 15\n"
    "v_max3_f32 v60, v96, v97, v98\n"
    "v_max3_f32 v61, v100, v101, v102\n"
    "v_max3_f32 v60, v60, v99, v104\n"
    "v_max3_f32 v61, v61, v103, v108\n"
    "v_max3_f32 v60, v60, v105, v106\n"
    "v_max3_f32 v61, v61, v109, v110\n"
    "v_max3_f32 v60, v60, v107, v112\n"
    "v_max3_f32 v61, v61, v111, v116\n"
    "v_max3_f32 v60, v60, v113, v114\n"
    "v_max3_f32 v61, v61, v117, v118\n"
    "v_max3_f32 v60, v60, v115, v120\n"
    "v_max3_f32 v61, v61, v119, v124\n"
    "v_max3_f32 v60, v60, v121, v122\n"
    "v_max3_f32 v61, v61, v125, v126\n"
    "v_max_f32 v60, v60, v123\n"
    "v_max_f32 v61, v61, v127\n"
    "v_mov_b32 v16, v60\n"
    "v_mov_b32 v17, v61\n"
    "s_nop 0\n"
    "v_permlane32_swap_b32 v16, v60\n"
    "v_permlane32_swap_b32 v17, v61\n"
    "v_max_f32 v60, v60, v16\n"
    "v_max_f32 v61, v61, v17\n"
    "v_mov_b32 v16, v60\n"
    "v_mov_b32 v17, v61\n"
    "s_nop 0\n"
    "v_permlane16_swap_b32 v16, v60\n"
    "v_permlane16_swap_b32 v17, v61\n"
    "v_max_f32 v60, v60, v16\n"
    "v_max_f32 v61, v61, v17\n"
    "v_max_f32 v60, 0, v60\n"
    "v_add_f32 v6, v6, v60\n"
    "v_exp_f32 v62, -v60\n"
    "v_sub_f32 v24, 0, v6\n"
    "v_sub_f32 v25, 0, v6\n"
    "v_sub_f32 v26, 0, v6\n"
    "v_sub_f32 v27, 0, v6\n"
    "v_mul_f32 v8, v8, v62\n"
    "v_sub_f32 v96, v96, v60\n"
    "v_sub_f32 v97, v97, v60\n"
    "v_sub_f32 v98, v98, v60\n"
    "v_sub_f32 v99, v99, v60\n"
    "v_sub_f32 v104, v104, v60\n"
    "v_sub_f32 v105, v105, v60\n"
    "v_sub_f32 v106, v106, v60\n"
    "v_sub_f32 v107, v107, v60\n"
    "v_sub_f32 v112, v112, v60\n"
    "v_sub_f32 v113, v113, v60\n"
    "v_sub_f32 v114, v114, v60\n"
    "v_sub_f32 v115, v115, v60\n"
    "v_sub_f32 v120, v120, v60\n"
    "v_sub_f32 v121, v121, v60\n"
    "v_sub_f32 v122, v122, v60\n"
    "v_sub_f32 v123, v123, v60\n"
    "v_sub_f32 v64, v64, v60\n"
    "v_sub_f32 v65, v65, v60\n"
    "v_sub_f32 v66, v66, v60\n"
    "v_sub_f32 v67, v67, v60\n"
    "v_sub_f32 v72, v72, v60\n"
    "v_sub_f32 v73, v73, v60\n"
    "v_sub_f32 v74, v74, v60\n"
    "v_sub_f32 v75, v75, v60\n"
    "v_sub_f32 v80, v80, v60\n"
    "v_sub_f32 v81, v81, v60\n"
    "v_sub_f32 v82, v82, v60\n"
    "v_sub_f32 v83, v83, v60\n"
    "v_sub_f32 v88, v88, v60\n"
    "v_sub_f32 v89, v89, v60\n"
    "v_sub_f32 v90, v90, v60\n"
    "v_sub_f32 v91, v91, v60\n"
    "v_mul_f32 v128, v128, v62\n"
    "v_mul_f32 v129, v129, v62\n"
    "v_mul_f32 v130, v130, v62\n"
    "v_mul_f32 v131, v131, v62\n"
    "v_mul_f32 v132, v132, v62\n"
    "v_mul_f32 v133, v133, v62\n"
    "v_mul_f32 v134, v134, v62\n"
    "v_mul_f32 v135, v135, v62\n"
    "v_mul_f32 v136, v136, v62\n"
    "v_mul_f32 v137, v137, v62\n"
    "v_mul_f32 v138, v138, v62\n"
    "v_mul_f32 v139, v139, v62\n"
    "v_mul_f32 v140, v140, v62\n"
    "v_mul_f32 v141, v141, v62\n"
    "v_mul_f32 v142, v142, v62\n"
    "v_mul_f32 v143, v143, v62\n"
    "v_mul_f32 v144, v144, v62\n"
    "v_mul_f32 v145, v145, v62\n"
    "v_mul_f32 v146, v146, v62\n"
    "v_mul_f32 v147, v147, v62\n"
    "v_mul_f32 v148, v148, v62\n"
    "v_mul_f32 v149, v149, v62\n"
    "v_mul_f32 v150, v150, v62\n"
    "v_mul_f32 v151, v151, v62\n"
    "v_mul_f32 v152, v152, v62\n"
    "v_mul_f32 v153, v153, v62\n"
    "v_mul_f32 v154, v154, v62\n"
    "v_mul_f32 v155, v155, v62\n"
    "v_mul_f32 v156, v156, v62\n"
    "v_mul_f32 v157, v157, v62\n"
    "v_mul_f32 v158, v158, v62\n"
    "v_mul_f32 v159, v159, v62\n"
    "v_mul_f32 v160, v160, v62\n"
    "v_mul_f32 v161, v161, v62\n"
    "v_mul_f32 v162, v162, v62\n"
    "v_mul_f32 v163, v163, v62\n"
    "v_mul_f32 v164, v164, v62\n"
    "v_mul_f32 v165, v165, v62\n"
    "v_mul_f32 v166, v166, v62\n"
    "v_mul_f32 v167, v167, v62\n"
    "v_mul_f32 v168, v168, v62\n"
    "v_mul_f32 v169, v169, v62\n"
    "v_mul_f32 v170, v170, v62\n"
    "v_mul_f32 v171, v171, v62\n"
    "v_mul_f32 v172, v172, v62\n"
    "v_mul_f32 v173, v173, v62\n"
    "v_mul_f32 v174, v174, v62\n"
    "v_mul_f32 v175, v175, v62\n"
    "v_mul_f32 v176, v176, v62\n"
    "v_mul_f32 v177, v177, v62\n"
    "v_mul_f32 v178, v178, v62\n"
    "v_mul_f32 v179, v179, v62\n"
    "v_mul_f32 v180, v180, v62\n"
    "v_mul_f32 v181, v181, v62\n"
    "v_mul_f32 v182, v182, v62\n"
    "v_mul_f32 v183, v183, v62\n"
    "v_mul_f32 v184, v184, v62\n"
    "v_mul_f32 v185, v185, v62\n"
    "v_mul_f32 v186, v186, v62\n"
    "v_mul_f32 v187, v187, v62\n"
    "v_mul_f32 v188, v188, v62\n"
    "v_mul_f32 v189, v189, v62\n"
    "v_mul_f32 v190, v190, v62\n"
    "v_mul_f32 v191, v191, v62\n"
    "v_max_f32 v61, 0, v61\n"
    "v_add_f32 v7, v7, v61\n"
    "v_exp_f32 v63, -v61\n"
    "v_sub_f32 v28, 0, v7\n"
    "v_sub_f32 v29, 0, v7\n"
    "v_sub_f32 v30, 0, v7\n"
    "v_sub_f32 v31, 0, v7\n"
    "v_mul_f32 v9, v9, v63\n"
    "v_sub_f32 v100, v100, v61\n"
    "v_sub_f32 v101, v101, v61\n"
    "v_sub_f32 v102, v102, v61\n"
    "v_sub_f32 v103, v103, v61\n"
    "v_sub_f32 v108, v108, v61\n"
    "v_sub_f32 v109, v109, v61\n"
    "v_sub_f32 v110, v110, v61\n"
    "v_sub_f32 v111, v111, v61\n"
    "v_sub_f32 v116, v116, v61\n"
    "v_sub_f32 v117, v117, v61\n"
    "v_sub_f32 v118, v118, v61\n"
    "v_sub_f32 v119, v119, v61\n"
    "v_sub_f32 v124, v124, v61\n"
    "v_sub_f32 v125, v125, v61\n"
    "v_sub_f32 v126, v126, v61\n"
    "v_sub_f32 v127, v127, v61\n"
    "v_sub_f32 v68, v68, v61\n"
    "v_sub_f32 v69, v69, v61\n"
    "v_sub_f32 v70, v70, v61\n"
    "v_sub_f32 v71, v71, v61\n"
    "v_sub_f32 v76, v76, v61\n"
    "v_sub_f32 v77, v77, v61\n"
    "v_sub_f32 v78, v78, v61\n"
    "v_sub_f32 v79, v79, v61\n"
    "v_sub_f32 v84, v84, v61\n"
    "v_sub_f32 v85, v85, v61\n"
    "v_sub_f32 v86, v86, v61\n"
    "v_sub_f32 v87, v87, v61\n"
    "v_sub_f32 v92, v92, v61\n"
    "v_sub_f32 v93, v93, v61\n"
    "v_sub_f32 v94, v94, v61\n"
    "v_sub_f32 v95, v95, v61\n"
    "v_mul_f32 v192, v192, v63\n"
    "v_mul_f32 v193, v193, v63\n"
    "v_mul_f32 v194, v194, v63\n"
    "v_mul_f32 v195, v195, v63\n"
    "v_mul_f32 v196, v196, v63\n"
    "v_mul_f32 v197, v197, v63\n"
    "v_mul_f32 v198, v198, v63\n"
    "v_mul_f32 v199, v199, v63\n"
    "v_mul_f32 v200, v200, v63\n"
    "v_mul_f32 v201, v201, v63\n"
    "v_mul_f32 v202, v202, v63\n"
    "v_mul_f32 v203, v203, v63\n"
    "v_mul_f32 v204, v204, v63\n"
    "v_mul_f32 v205, v205, v63\n"
    "v_mul_f32 v206, v206, v63\n"
    "v_mul_f32 v207, v207, v63\n"
    "v_mul_f32 v208, v208, v63\n"
    "v_mul_f32 v209, v209, v63\n"
    "v_mul_f32 v210, v210, v63\n"
    "v_mul_f32 v211, v211, v63\n"
    "v_mul_f32 v212, v212, v63\n"
    "v_mul_f32 v213, v213, v63\n"
    "v_mul_f32 v214, v214, v63\n"
    "v_mul_f32 v215, v215, v63\n"
    "v_mul_f32 v216, v216, v63\n"
    "v_mul_f32 v217, v217, v63\n"
    "v_mul_f32 v218, v218, v63\n"
    "v_mul_f32 v219, v219, v63\n"
    "v_mul_f32 v220, v220, v63\n"
    "v_mul_f32 v221, v221, v63\n"
    "v_mul_f32 v222, v222, v63\n"
    "v_mul_f32 v223, v223, v63\n"
    "v_mul_f32 v224, v224, v63\n"
    "v_mul_f32 v225, v225, v63\n"
    "v_mul_f32 v226, v226, v63\n"
    "v_mul_f32 v227, v227, v63\n"
    "v_mul_f32 v228, v228, v63\n"
    "v_mul_f32 v229, v229, v63\n"
    "v_mul_f32 v230, v230, v63\n"
    "v_mul_f32 v231, v231, v63\n"
    "v_mul_f32 v232, v232, v63\n"
    "v_mul_f32 v233, v233, v63\n"
    "v_mul_f32 v234, v234, v63\n"
    "v_mul_f32 v235, v235, v63\n"
    "v_mul_f32 v236, v236, v63\n"
    "v_mul_f32 v237, v237, v63\n"
    "v_mul_f32 v238, v238, v63\n"
    "v_mul_f32 v239, v239, v63\n"
    "v_mul_f32 v240, v240, v63\n"
    "v_mul_f32 v241, v241, v63\n"
    "v_mul_f32 v242, v242, v63\n"
    "v_mul_f32 v243, v243, v63\n"
    "v_mul_f32 v244, v244, v63\n"
    "v_mul_f32 v245, v245, v63\n"
    "v_mul_f32 v246, v246, v63\n"
    "v_mul_f32 v247, v247, v63\n"
    "v_mul_f32 v248, v248, v63\n"
    "v_mul_f32 v249, v249, v63\n"
    "v_mul_f32 v250, v250, v63\n"
    "v_mul_f32 v251, v251, v63\n"
    "v_mul_f32 v252, v252, v63\n"
    "v_mul_f32 v253, v253, v63\n"
    "v_mul_f32 v254, v254, v63\n"
    "v_mul_f32 v255, v255, v63\n"
    "v_exp_f32 v16, v96\n"
    "v_exp_f32 v17, v97\n"
    "v_exp_f32 v18, v98\n"
    "v_exp_f32 v19, v99\n"
    "v_add_f32 v10, v16, v17\n"
    "v_cvt_pk_bf16_f32 v40, v16, v17\n"
    "v_exp_f32 v20, v104\n"
    "v_exp_f32 v21, v105\n"
    "v_add_f32 v10, v10, v18\n"
    "v_add_f32 v10, v10, v19\n"
    "v_cvt_pk_bf16_f32 v41, v18, v19\n"
    "v_exp_f32 v22, v106\n"
    "v_exp_f32 v23, v107\n"
    "v_add_f32 v10, v10, v20\n"
    "v_add_f32 v10, v10, v21\n"
    "v_cvt_pk_bf16_f32 v42, v20, v21\n"
    "v_exp_f32 v16, v100\n"
    "v_exp_f32 v17, v101\n"
    "v_add_f32 v10, v10, v22\n"
    "v_add_f32 v10, v10, v23\n"
    "v_cvt_pk_bf16_f32 v43, v22, v23\n"
    "v_exp_f32 v18, v102\n"
    "v_exp_f32 v19, v103\n"
    "v_add_f32 v11, v16, v17\n"
    "v_cvt_pk_bf16_f32 v44, v16, v17\n"
    "v_exp_f32 v20, v108\n"
    "v_exp_f32 v21, v109\n"
    "v_add_f32 v11, v11, v18\n"
    "v_add_f32 v11, v11, v19\n"
    "v_cvt_pk_bf16_f32 v45, v18, v19\n"
    "v_exp_f32 v22, v110\n"
    "v_exp_f32 v23, v111\n"
    "v_add_f32 v11, v11, v20\n"
    "v_add_f32 v11, v11, v21\n"
    "v_cvt_pk_bf16_f32 v46, v20, v21\n"
    "v_exp_f32 v16, v112\n"
    "v_exp_f32 v17, v113\n"
    "v_add_f32 v11, v11, v22\n"
    "v_add_f32 v11, v11, v23\n"
    "v_cvt_pk_bf16_f32 v47, v22, v23\n"
    "v_exp_f32 v18, v114\n"
    "v_exp_f32 v19, v115\n"
    "v_add_f32 v10, v10, v16\n"
    "v_add_f32 v10, v10, v17\n"
    "v_cvt_pk_bf16_f32 v48, v16, v17\n"
    "v_exp_f32 v20, v120\n"
    "v_exp_f32 v21, v121\n"
    "v_add_f32 v10, v10, v18\n"
    "v_add_f32 v10, v10, v19\n"
    "v_cvt_pk_bf16_f32 v49, v18, v19\n"
    "v_exp_f32 v22, v122\n"
    "v_exp_f32 v23, v123\n"
    "v_add_f32 v10, v10, v20\n"
    "v_add_f32 v10, v10, v21\n"
    "v_cvt_pk_bf16_f32 v50, v20, v21\n"
    "v_exp_f32 v16, v116\n"
    "v_exp_f32 v17, v117\n"
    "v_add_f32 v10, v10, v22\n"
    "v_add_f32 v10, v10, v23\n"
    "v_cvt_pk_bf16_f32 v51, v22, v23\n"
    "v_exp_f32 v18, v118\n"
    "v_exp_f32 v19, v119\n"
    "v_add_f32 v11, v11, v16\n"
    "v_add_f32 v11, v11, v17\n"
    "v_cvt_pk_bf16_f32 v52, v16, v17\n"
    "v_exp_f32 v20, v124\n"
    "v_exp_f32 v21, v125\n"
    "v_add_f32 v11, v11, v18\n"
    "v_add_f32 v11, v11, v19\n"
    "v_cvt_pk_bf16_f32 v53, v18, v19\n"
    "v_exp_f32 v22, v126\n"
    "v_exp_f32 v23, v127\n"
    "v_add_f32 v11, v11, v20\n"
    "v_add_f32 v11, v11, v21\n"
    "v_cvt_pk_bf16_f32 v54, v20, v21\n"
    "s_nop 0\n"
    "v_add_f32 v11, v11, v22\n"
    "v_add_f32 v11, v11, v23\n"
    "v_cvt_pk_bf16_f32 v55, v22, v23\n"
    "s_nop 7\n"
    "s_setpc_b64 s[46:47]\n"
    ".Lk2_raree:\n"
    "s_nop 15\n"
    "v_max3_f32 v60, v100, v101, v102\n"
    "v_max3_f32 v60, v60, v103, v108\n"
    "v_max3_f32 v60, v60, v109, v110\n"
    "v_max3_f32 v60, v60, v111, v116\n"
    "v_max3_f32 v60, v60, v117, v118\n"
    "v_max3_f32 v60, v60, v119, v124\n"
    "v_max3_f32 v60, v60, v125, v126\n"
    "v_max_f32 v60, v60, v127\n"
    "v_mov_b32 v16, v60\n"
    "v_mov_b32 v17, v61\n"
    "s_nop 0\n"
    "v_permlane32_swap_b32 v16, v60\n"
    "v_permlane32_swap_b32 v17, v61\n"
    "v_max_f32 v60, v60, v16\n"
    "v_max_f32 v61, v61, v17\n"
    "v_mov_b32 v16, v60\n"
    "v_mov_b32 v17, v61\n"
    "s_nop 0\n"
    "v_permlane16_swap_b32 v16, v60\n"
    "v_permlane16_swap_b32 v17, v61\n"
    "v_max_f32 v60, v60, v16\n"
    "v_max_f32 v61, v61, v17\n"
    "v_max_f32 v60, 0, v60\n"
    "v_add_f32 v7, v7, v60\n"
    "v_exp_f32 v62, -v60\n"
    "v_sub_f32 v100, v100, v60\n"
    "v_sub_f32 v101, v101, v60\n"
    "v_sub_f32 v102, v102, v60\n"
    "v_sub_f32 v103, v103, v60\n"
    "v_sub_f32 v108, v108, v60\n"
    "v_sub_f32 v109, v109, v60\n"
    "v_sub_f32 v110, v110, v60\n"
    "v_sub_f32 v111, v111, v60\n"
    "v_sub_f32 v116, v116, v60\n"
    "v_sub_f32 v117, v117, v60\n"
    "v_sub_f32 v118, v118, v60\n"
    "v_sub_f32 v119, v119, v60\n"
    "v_sub_f32 v124, v124, v60\n"
    "v_sub_f32 v125, v125, v60\n"
    "v_sub_f32 v126, v126, v60\n"
    "v_sub_f32 v127, v127, v60\n"
    "v_mul_f32 v9, v9, v62\n"
    "v_mul_f32 v192, v192, v62\n"
    "v_mul_f32 v193, v193, v62\n"
    "v_mul_f32 v194, v194, v62\n"
    "v_mul_f32 v195, v195, v62\n"
    "v_mul_f32 v196, v196, v62\n"
    "v_mul_f32 v197, v197, v62\n"
    "v_mul_f32 v198, v198, v62\n"
    "v_mul_f32 v199, v199, v62\n"
    "v_mul_f32 v200, v200, v62\n"
    "v_mul_f32 v201, v201, v62\n"
    "v_mul_f32 v202, v202, v62\n"
    "v_mul_f32 v203, v203, v62\n"
    "v_mul_f32 v204, v204, v62\n"
    "v_mul_f32 v205, v205, v62\n"
    "v_mul_f32 v206, v206, v62\n"
    "v_mul_f32 v207, v207, v62\n"
    "v_mul_f32 v208, v208, v62\n"
    "v_mul_f32 v209, v209, v62\n"
    "v_mul_f32 v210, v210, v62\n"
    "v_mul_f32 v211, v211, v62\n"
    "v_mul_f32 v212, v212, v62\n"
    "v_mul_f32 v213, v213, v62\n"
    "v_mul_f32 v214, v214, v62\n"
    "v_mul_f32 v215, v215, v62\n"
    "v_mul_f32 v216, v216, v62\n"
    "v_mul_f32 v217, v217, v62\n"
    "v_mul_f32 v218, v218, v62\n"
    "v_mul_f32 v219, v219, v62\n"
    "v_mul_f32 v220, v220, v62\n"
    "v_mul_f32 v221, v221, v62\n"
    "v_mul_f32 v222, v222, v62\n"
    "v_mul_f32 v223, v223, v62\n"
    "v_mul_f32 v224, v224, v62\n"
    "v_mul_f32 v225, v225, v62\n"
    "v_mul_f32 v226, v226, v62\n"
    "v_mul_f32 v227, v227, v62\n"
    "v_mul_f32 v228, v228, v62\n"
    "v_mul_f32 v229, v229, v62\n"
    "v_mul_f32 v230, v230, v62\n"
    "v_mul_f32 v231, v231, v62\n"
    "v_mul_f32 v232, v232, v62\n"
    "v_mul_f32 v233, v233, v62\n"
    "v_mul_f32 v234, v234, v62\n"
    "v_mul_f32 v235, v235, v62\n"
    "v_mul_f32 v236, v236, v62\n"
    "v_mul_f32 v237, v237, v62\n"
    "v_mul_f32 v238, v238, v62\n"
    "v_mul_f32 v239, v239, v62\n"
    "v_mul_f32 v240, v240, v62\n"
    "v_mul_f32 v241, v241, v62\n"
    "v_mul_f32 v242, v242, v62\n"
    "v_mul_f32 v243, v243, v62\n"
    "v_mul_f32 v244, v244, v62\n"
    "v_mul_f32 v245, v245, v62\n"
    "v_mul_f32 v246, v246, v62\n"
    "v_mul_f32 v247, v247, v62\n"
    "v_mul_f32 v248, v248, v62\n"
    "v_mul_f32 v249, v249, v62\n"
    "v_mul_f32 v250, v250, v62\n"
    "v_mul_f32 v251, v251, v62\n"
    "v_mul_f32 v252, v252, v62\n"
    "v_mul_f32 v253, v253, v62\n"
    "v_mul_f32 v254, v254, v62\n"
    "v_mul_f32 v255, v255, v62\n"
    "v_exp_f32 v16, v100\n"
    "v_exp_f32 v17, v101\n"
    "s_nop 0\n"
    "v_add_f32 v11, v16, v17\n"
    "v_cvt_pk_bf16_f32 v44, v16, v17\n"
    "v_exp_f32 v18, v102\n"
    "v_exp_f32 v19, v103\n"
    "s_nop 0\n"
    "v_add_f32 v11, v11, v18\n"
    "v_add_f32 v11, v11, v19\n"
    "v_cvt_pk_bf16_f32 v45, v18, v19\n"
    "v_exp_f32 v20, v108\n"
    "v_exp_f32 v21, v109\n"
    "s_nop 0\n"
    "v_add_f32 v11, v11, v20\n"
    "v_add_f32 v11, v11, v21\n"
    "v_cvt_pk_bf16_f32 v46, v20, v21\n"
    "v_exp_f32 v22, v110\n"
    "v_exp_f32 v23, v111\n"
    "s_nop 0\n"
    "v_add_f32 v11, v11, v22\n"
    "v_add_f32 v11, v11, v23\n"
    "v_cvt_pk_bf16_f32 v47, v22, v23\n"
    "v_exp_f32 v16, v116\n"
    "v_exp_f32 v17, v117\n"
    "s_nop 0\n"
    "v_add_f32 v11, v11, v16\n"
    "v_add_f32 v11, v11, v17\n"
    "v_cvt_pk_bf16_f32 v40, v16, v17\n"
    "v_exp_f32 v18, v118\n"
    "v_exp_f32 v19, v119\n"
    "s_nop 0\n"
    "v_add_f32 v11, v11, v18\n"
    "v_add_f32 v11, v11, v19\n"
    "v_cvt_pk_bf16_f32 v41, v18, v19\n"
    "v_exp_f32 v20, v124\n"
    "v_exp_f32 v21, v125\n"
    "s_nop 0\n"
    "v_add_f32 v11, v11, v20\n"
    "v_add_f32 v11, v11, v21\n"
    "v_cvt_pk_bf16_f32 v42, v20, v21\n"
    "v_exp_f32 v22, v126\n"
    "v_exp_f32 v23, v127\n"
    "s_nop 0\n"
    "v_add_f32 v11, v11, v22\n"
    "v_add_f32 v11, v11, v23\n"
    "v_cvt_pk_bf16_f32 v43, v22, v23\n"
    "s_nop 7\n"
    "s_branch .Lk2_raree_ret\n"
    ".Lk2_end:\n"
    : "+{v1}"(ln16), "+{v[2:3]}"(vA01), "+{v[6:7]}"(vW01), "+{v[8:9]}"(vRA), "+{v[10:11]}"(pf1), "+{v[12:15]}"(vRW), "+{s[36:39]}"(sA0), "+{s[44:47]}"(sWd), "+{s[68:75]}"(sM), "+{s[80:95]}"(sOFF), "+{s[64:67]}"(sH),
      "+{s[52:55]}"(sX), "+{s[56:59]}"(sWb), "+{s[60:63]}"(sWq), "+{a[220:221]}"(aW), "+{a[224:231]}"(aE), "+{a[240:255]}"(aP), "+{a[232:239]}"(aR), "+{s[16:17]}"(pE)
    : "{s[48:51]}"(sO) : "v0","v4","v5","v16","v17","v18","v19","v20","v21","v22","v23","v24","v25","v26","v27","v28","v29","v30","v31","v32","v33","v34","v35","v36","v37","v38","v39","v40","v41","v42","v43","v44","v45","v46","v47","v48","v49","v50","v51","v52","v53","v54","v55","v56","v57","v58","v59","v60","v61","v62","v63","v64","v65","v66","v67","v68","v69","v70","v71","v72","v73","v74","v75","v76","v77","v78","v79","v80","v81","v82","v83","v84","v85","v86","v87","v88","v89","v90","v91","v92","v93","v94","v95","v96","v97","v98","v99","v100","v101","v102","v103","v104","v105","v106","v107","v108","v109","v110","v111","v112","v113","v114","v115","v116","v117","v118","v119","v120","v121","v122","v123","v124","v125","v126","v127","v128","v129","v130","v131","v132","v133","v134","v135","v136","v137","v138","v139","v140","v141","v142","v143","v144","v145","v146","v147","v148","v149","v150","v151","v152","v153","v154","v155","v156","v157","v158","v159","v160","v161","v162","v163","v164","v165","v166","v167","v168","v169","v170","v171","v172","v173","v174","v175","v176","v177","v178","v179","v180","v181","v182","v183","v184","v185","v186","v187","v188","v189","v190","v191","v192","v193","v194","v195","v196","v197","v198","v199","v200","v201","v202","v203","v204","v205","v206","v207","v208","v209","v210","v211","v212","v213","v214","v215","v216","v217","v218","v219","v220","v221","v222","v223","v224","v225","v226","v227","v228","v229","v230","v231","v232","v233","v234","v235","v236","v237","v238","v239","v240","v241","v242","v243","v244","v245","v246","v247","v248","v249","v250","v251","v252","v253","v254","v255","a0","a1","a2","a3","a4","a5","a6","a7","a8","a9","a10","a11","a12","a13","a14","a15","a16","a17","a18","a19","a20","a21","a22","a23","a24","a25","a26","a27","a28","a29","a30","a31","a32","a33","a34","a35","a36","a37","a38","a39","a40","a41","a42","a43","a44","a45","a46","a47","a48","a49","a50","a51","a52","a53","a54","a55","a56","a57","a58","a59","a60","a61","a62","a63","a64","a65","a66","a67","a68","a69","a70","a71","a72","a73","a74","a75","a76","a77","a78","a79","a80","a81","a82","a83","a84","a85","a86","a87","a88","a89","a90","a91","a92","a93","a94","a95","a96","a97","a98","a99","a100","a101","a102","a103","a104","a105","a106","a107","a108","a109","a110","a111","a112","a113","a114","a115","a116","a117","a118","a119","a120","a121","a122","a123","a124","a125","a126","a127","a128","a129","a130","a131","a132","a133","a134","a135","a136","a137","a138","a139","a140","a141","a142","a143","a144","a145","a146","a147","a148","a149","a150","a151","a152","a153","a154","a155","a156","a157","a158","a159","a160","a161","a162","a163","a164","a165","a166","a167","a168","a169","a170","a171","a172","a173","a174","a175","a176","a177","a178","a179","a180","a181","a182","a183","a184","a185","a186","a187","a188","a189","a190","a191","a192","a193","a194","a195","a196","a197","a198","a199","a200","a201","a202","a203","a204","a205","a206","a207","a208","a209","a210","a211","a212","a213","a214","a215","a216","a217","a218","a219","a222","a223","s18","s19","s34","s35","s40","s41","s42","s43","s76","s77","s78","s79","s96","s97","s98","s99","vcc","scc","memory");
}

extern "C" void kernel_launch(void* const* d_in, const int* in_sizes, int n_in, void* d_out, int out_size, void* d_ws, size_t ws_size, hipStream_t stream) {
  kf_main<<<256, 256, 0, stream>>>((const char*)d_in[0], (const char*)d_in[1], (const char*)d_in[2], (const char*)d_in[3], (const char*)d_in[4], (const char*)d_in[7], (const char*)d_in[9],
                                   (const char*)d_in[5], (const char*)d_in[8], (char*)d_ws, (char*)d_out);
}
```
